```python
import torch

def _softmax(x, axis=-1):
    return torch.softmax(x, dim=axis)
def _gelu(x):
    return torch.nn.functional.gelu(x, approximate="tanh")
def _silu(x):
    return torch.nn.functional.silu(x)
def _sigmoid(x):
    return torch.sigmoid(x)
def _layernorm(x, axis=-1, eps=1e-5):
    m = x.mean(dim=axis, keepdim=True)
    v = x.var(dim=axis, keepdim=True, unbiased=False)
    return (x - m) / torch.sqrt(v + eps)
def _scatter_add(tgt, idx, src, axis=0):
    return tgt.clone().index_add_(axis, idx, src)
def _segment_sum(src, seg, n):
    out = torch.zeros((n,) + src.shape[1:], dtype=src.dtype)
    return out.index_add_(0, seg, src)
def _pad(x, pw):
    flat = tuple(v for pair in reversed(pw) for v in pair)
    return torch.nn.functional.pad(x, flat)

kernel_name = 'standalone__gqa_softmax__near_ridge'

def setup_inputs():

    _w = (lambda a: (lambda t: t.to(torch.bfloat16) if t.is_floating_point() else t)(torch.as_tensor(a)))
    B, Hq, Hkv, Sq, Sk, D = 16, 32, 16, 496, 512, 128
    return {
        "q": _w((lambda *s: torch.randn(*s, dtype=torch.bfloat16) * 0.8)(B, Hq, Sq, D)),
        "k": _w((lambda *s: torch.randn(*s, dtype=torch.bfloat16) * 0.8)(B, Hkv, Sk, D)),
        "v": _w((lambda *s: torch.randn(*s, dtype=torch.bfloat16) * 0.05)(B, Hkv, Sk, D)),
    }

def reference(q, k, v):
    B, Hq, Sq, D = q.shape
    Hkv = k.shape[1]
    rep = Hq // Hkv
    k = (lambda x, n, axis: torch.repeat_interleave(x, n, dim=axis))(k, rep, axis=1)
    v = (lambda x, n, axis: torch.repeat_interleave(x, n, dim=axis))(v, rep, axis=1)
    scale = D ** -0.5
    scores = torch.einsum("bhqd,bhkd->bhqk", q, k) * scale
    p = _softmax(scores, axis=-1)
    return torch.einsum("bhqk,bhkd->bhqd", p, v)
```

```cpp
#include <hip/hip_runtime.h>
#include <hip/hip_bf16.h>
#include <cstdio>

using bf16 = __hip_bfloat16;
constexpr int   D = 128, NW = 8, QBLK = 32, KVBLK = 64;
constexpr float SCALE = 0.088388347648318440f;
#define ATTN_THR 8.f
constexpr float THR = ATTN_THR;
#define ATTN_LAYOUT 0
constexpr int LAYOUT = ATTN_LAYOUT;
#define ATTN_SDEPTH 2
constexpr int SDEPTH = ATTN_SDEPTH;
static_assert(SDEPTH == 1 || SDEPTH == 2, "SDEPTH is 1 or 2");
#define ATTN_B 4
#define ATTN_HQ 64
#define ATTN_HKV 8
#define ATTN_S 2048
#define ATTN_SKV ATTN_S
constexpr int B = ATTN_B, HQ = ATTN_HQ, HKV = ATTN_HKV, S = ATTN_S, SKV = ATTN_SKV;
constexpr int LDQ = LAYOUT ? HQ * D : D, LDK = LAYOUT ? HKV * D : D, LDO = LDQ;

constexpr size_t SHM_V = KVBLK * D * 2, SHM_K = KVBLK * D * 2, SHM_ATTN = 2 * SHM_V + 2 * SHM_K + NW * 64 * 4;

using bf16x8 = __attribute__((ext_vector_type(8))) short;
using s16x4  = __attribute__((ext_vector_type(4))) short;
using f32x16 = __attribute__((ext_vector_type(16))) float;
using f32x8  = __attribute__((ext_vector_type(8))) float;
using u32x4  = __attribute__((ext_vector_type(4))) unsigned;
#define KSWZ(row, colB) ((row) * 256 + ((colB) ^ (((row) & 7) << 4)))
#define SBAR() __builtin_amdgcn_sched_barrier(0)
__device__ __forceinline__ int crow(int r, int hi) { return (r & 3) + 8 * (r >> 2) + 4 * hi; }
__device__ __forceinline__ unsigned cvtpk(float lo, float hi) {
  unsigned r; asm volatile("v_cvt_pk_bf16_f32 %0, %1, %2" : "=v"(r) : "v"(lo), "v"(hi)); return r;
}
template <typename TIn> struct Stage;
template <> struct Stage<bf16>  { using T = bf16x8;
  __device__ static __forceinline__ T ld8(const bf16* p) { return *reinterpret_cast<const bf16x8*>(p); }
  __device__ static __forceinline__ bf16x8 tobf(T x) { return x; } };
template <> struct Stage<float> { using T = f32x8;
  __device__ static __forceinline__ T ld8(const float* p) { return *reinterpret_cast<const f32x8*>(p); }
  __device__ static __forceinline__ bf16x8 tobf(T x) {
    u32x4 w = {cvtpk(x[0], x[1]), cvtpk(x[2], x[3]), cvtpk(x[4], x[5]), cvtpk(x[6], x[7])}; return *reinterpret_cast<bf16x8*>(&w); } };

__device__ __forceinline__ void partialSM(f32x16& p0, f32x16& p1, float& m_reg, float& mn, float& alpha) {
  constexpr float C = SCALE * 1.4426950408889634f;
  float pmax = p0[0]; for (int r = 1; r < 16; ++r) pmax = fmaxf(pmax, p0[r]); for (int r = 0; r < 16; ++r) pmax = fmaxf(pmax, p1[r]);
  { auto rr = __builtin_amdgcn_permlane32_swap(__float_as_uint(pmax), __float_as_uint(pmax), false, false);
    pmax = fmaxf(__uint_as_float(rr[0]), __uint_as_float(rr[1])); }
  if (__builtin_expect(__all(pmax - m_reg <= THR / SCALE), 1)) { mn = m_reg; alpha = 1.f; }
  else { mn = fmaxf(m_reg, pmax); alpha = __builtin_amdgcn_exp2f((m_reg - mn) * C); m_reg = mn; }
  float mnC = -mn * C;
  for (int r = 0; r < 16; ++r) p0[r] = fmaf(p0[r], C, mnC); for (int r = 0; r < 16; ++r) p1[r] = fmaf(p1[r], C, mnC);
  for (int r = 0; r < 16; ++r) p0[r] = __builtin_amdgcn_exp2f(p0[r]);
}
__device__ __forceinline__ void finishSM(f32x16& p0, f32x16& p1, float alpha, float& l_reg, bf16x8& pa0, bf16x8& pa1, bf16x8& pa2, bf16x8& pa3) {
  for (int r = 0; r < 16; ++r) p1[r] = __builtin_amdgcn_exp2f(p1[r]);
  float ps = 0; for (int r = 0; r < 16; ++r) ps += p0[r]; for (int r = 0; r < 16; ++r) ps += p1[r];
  { auto rr = __builtin_amdgcn_permlane32_swap(__float_as_uint(ps), __float_as_uint(ps), false, false);
    ps = __uint_as_float(rr[0]) + __uint_as_float(rr[1]); }
  l_reg = l_reg * alpha + ps;
#define PK4(P, BASE, OUT) do { unsigned a0 = cvtpk(P[BASE + 0], P[BASE + 1]), a1 = cvtpk(P[BASE + 2], P[BASE + 3]);   \
    unsigned b0 = cvtpk(P[BASE + 4], P[BASE + 5]), b1 = cvtpk(P[BASE + 6], P[BASE + 7]);                              \
    auto r0 = __builtin_amdgcn_permlane32_swap(a0, b0, false, false); auto r1 = __builtin_amdgcn_permlane32_swap(a1, b1, false, false); \
    u32x4 w = {r0[0], r1[0], r0[1], r1[1]}; OUT = *reinterpret_cast<bf16x8*>(&w); } while (0)
  PK4(p0, 0, pa0); PK4(p0, 8, pa1); PK4(p1, 0, pa2); PK4(p1, 8, pa3);
#undef PK4
}
__device__ __forceinline__ void qkt(f32x16& p0, f32x16& p1, const bf16* Ks, const bf16x8* qr, int r32, int hi) {
  p0 = f32x16{}; p1 = f32x16{};
  for (int d0 = 0; d0 < 8; ++d0) { int cb = (d0 * 16 + hi * 8) * 2;
    bf16x8 b0 = *reinterpret_cast<const bf16x8*>((const char*)Ks + KSWZ(r32, cb));
    bf16x8 b1 = *reinterpret_cast<const bf16x8*>((const char*)Ks + KSWZ(32 + r32, cb));
    p0 = __builtin_amdgcn_mfma_f32_32x32x16_bf16(b0, qr[d0], p0, 0, 0, 0);
    p1 = __builtin_amdgcn_mfma_f32_32x32x16_bf16(b1, qr[d0], p1, 0, 0, 0); }
}
__device__ __forceinline__ int v_st(int k, int c) { const int kk = (k & ~0xC) | ((k & 4) << 1) | ((k & 8) >> 1); return ((kk >> 3) * 4 + (c >> 5)) * 512 + ((kk & 7) * 32 + (c & 31)) * 2; }
__device__ __forceinline__ int v_rd_base(int lane) { return ((lane & 3) << 3) | (((lane >> 2) & 3) << 6) | (((lane >> 4) & 1) << 5) | (((lane >> 5) & 1) << 8); }
constexpr int v_rd_off(int d0, int ks, int half) { return d0 * 512 + ks * 4096 + half * 2048; }
template <int OFF> __device__ __forceinline__ s16x4 tr_read(int vb) {
  s16x4 r; asm volatile("ds_read_b64_tr_b16 %0, %1 offset:%2" : "=&v"(r) : "v"(vb), "i"(OFF) : "memory"); return r;
}
template <int D0> __device__ __forceinline__ void pv_one(f32x16& od, int vb, bf16x8 pa0, bf16x8 pa1, bf16x8 pa2, bf16x8 pa3) {
  const s16x4 l0 = tr_read<v_rd_off(D0, 0, 0)>(vb), h0 = tr_read<v_rd_off(D0, 0, 1)>(vb), l1 = tr_read<v_rd_off(D0, 1, 0)>(vb), h1 = tr_read<v_rd_off(D0, 1, 1)>(vb);
  const s16x4 l2 = tr_read<v_rd_off(D0, 2, 0)>(vb), h2 = tr_read<v_rd_off(D0, 2, 1)>(vb), l3 = tr_read<v_rd_off(D0, 3, 0)>(vb), h3 = tr_read<v_rd_off(D0, 3, 1)>(vb);
  asm volatile("s_waitcnt lgkmcnt(0)" ::: "memory"); SBAR();
#define PK(L, H) (bf16x8){L[0], L[1], L[2], L[3], H[0], H[1], H[2], H[3]}
  od = __builtin_amdgcn_mfma_f32_32x32x16_bf16(pa0, PK(l0, h0), od, 0, 0, 0);
  od = __builtin_amdgcn_mfma_f32_32x32x16_bf16(pa1, PK(l1, h1), od, 0, 0, 0);
  od = __builtin_amdgcn_mfma_f32_32x32x16_bf16(pa2, PK(l2, h2), od, 0, 0, 0);
  od = __builtin_amdgcn_mfma_f32_32x32x16_bf16(pa3, PK(l3, h3), od, 0, 0, 0);
#undef PK
}
__device__ __forceinline__ void pv_d0(f32x16* o, int vb, bf16x8 pa0, bf16x8 pa1, bf16x8 pa2, bf16x8 pa3) {
  pv_one<0>(o[0], vb, pa0, pa1, pa2, pa3); pv_one<1>(o[1], vb, pa0, pa1, pa2, pa3); pv_one<2>(o[2], vb, pa0, pa1, pa2, pa3); pv_one<3>(o[3], vb, pa0, pa1, pa2, pa3);
}

template <typename TQ>
__device__ __forceinline__ void attn_dense_body(const TQ* __restrict__ Qb, const bf16* __restrict__ Kh, const bf16* __restrict__ Vh,
                                                bf16* __restrict__ Ob, int seq, char* lds) {
  using St = Stage<bf16>; using SQ = Stage<TQ>;
  const int tid = threadIdx.x, wid = tid >> 6, lane = tid & 63, r32 = lane & 31, hi = lane >> 5;
  bf16* V_lds = (bf16*)lds; bf16* K_lds = (bf16*)(lds + 2 * SHM_V);
  float* ws = (float*)(lds + 2 * SHM_V + 2 * SHM_K) + wid * 64; float* li_l = ws; float* al_l = ws + 32;
  float m_reg = -1e30f, l_reg = 0; f32x16 o[4] = {}; bf16x8 qr[8];
  int qrow = wid * QBLK + r32; if (qrow > 247) qrow = 247; const TQ* Qw = Qb + (long)qrow * LDQ + hi * 8;
#pragma unroll
  for (int d0 = 0; d0 < 8; ++d0) qr[d0] = SQ::tobf(SQ::ld8(Qw + d0 * 16));
  const int sr = tid >> 4, sc = (tid & 15) * 8, vst0 = v_st(sr, sc), vst1 = v_st(32 + sr, sc);
  const int vb0 = (int)(uintptr_t)V_lds + v_rd_base(lane);
  struct { typename St::T vs0, vs1, ks0, ks1; } sr_[SDEPTH];
#define SLOAD(i, k0) do { sr_[i].vs0 = St::ld8(&Vh[(long)((k0) + sr) * LDK + sc]); sr_[i].vs1 = St::ld8(&Vh[(long)((k0) + 32 + sr) * LDK + sc]); \
    sr_[i].ks0 = St::ld8(&Kh[(long)((k0) + sr) * LDK + sc]); sr_[i].ks1 = St::ld8(&Kh[(long)((k0) + 32 + sr) * LDK + sc]); } while (0)
#define SWRITE(b, i) do { *(bf16x8*)((char*)V_lds + (b) * SHM_V + vst0) = St::tobf(sr_[i].vs0);          \
    *(bf16x8*)((char*)V_lds + (b) * SHM_V + vst1) = St::tobf(sr_[i].vs1); int kc = sc * 2;               \
    *(bf16x8*)((char*)K_lds + (b) * SHM_K + KSWZ(sr, kc)) = St::tobf(sr_[i].ks0);                       \
    *(bf16x8*)((char*)K_lds + (b) * SHM_K + KSWZ(32 + sr, kc)) = St::tobf(sr_[i].ks1); } while (0)
#define SWAIT() do { if constexpr (SDEPTH == 2) asm volatile("s_waitcnt vmcnt(4)" ::: "memory"); else asm volatile("s_waitcnt vmcnt(0)" ::: "memory"); } while (0)
#define RESC(a) do { if (__any((a) < 1.f)) { if (hi == 0) al_l[r32] = (a); asm volatile("s_waitcnt lgkmcnt(0)" ::: "memory"); \
    for (int d = 0; d < 4; ++d) for (int r = 0; r < 16; ++r) o[d][r] *= al_l[crow(r, hi)]; } } while (0)
  f32x16 pA0, pA1, pB0, pB1; float mnA, mnB, alA, alB; bf16x8 pa0, pa1, pa2, pa3; const int NT = seq / KVBLK;
  constexpr int SE = 0, SO = SDEPTH - 1;
  SLOAD(SE, 0); asm volatile("s_waitcnt vmcnt(0)" ::: "memory"); SWRITE(0, SE); __syncthreads();
  qkt(pA0, pA1, K_lds, qr, r32, hi); partialSM(pA0, pA1, m_reg, mnA, alA);
  SLOAD(SO, KVBLK); if constexpr (SDEPTH == 2) { if (2 < NT) SLOAD(SE, 2 * KVBLK); }
  SWAIT(); SWRITE(1, SO); __syncthreads();
  for (int j = 1; j + 1 < NT; j += 2) {
    SBAR(); qkt(pB0, pB1, (bf16*)((char*)K_lds + SHM_K), qr, r32, hi);
    finishSM(pA0, pA1, alA, l_reg, pa0, pa1, pa2, pa3); SBAR();
    SLOAD(SO, (j + SDEPTH) * KVBLK); SBAR();
    pv_d0(o, vb0, pa0, pa1, pa2, pa3); partialSM(pB0, pB1, m_reg, mnB, alB);
    __syncthreads(); SWAIT(); SWRITE(0, SE);
    RESC(alB); __syncthreads();
    SBAR(); qkt(pA0, pA1, K_lds, qr, r32, hi);
    finishSM(pB0, pB1, alB, l_reg, pa0, pa1, pa2, pa3); SBAR();
    if (SDEPTH == 1 || j + 3 < NT) SLOAD(SE, (j + 1 + SDEPTH) * KVBLK); SBAR();
    pv_d0(o, vb0 + (int)SHM_V, pa0, pa1, pa2, pa3); partialSM(pA0, pA1, m_reg, mnA, alA);
    __syncthreads(); SWAIT(); SWRITE(1, SO);
    RESC(alA); __syncthreads();
  }
  SBAR(); qkt(pB0, pB1, (bf16*)((char*)K_lds + SHM_K), qr, r32, hi);
  finishSM(pA0, pA1, alA, l_reg, pa0, pa1, pa2, pa3); SBAR();
  pv_d0(o, vb0, pa0, pa1, pa2, pa3); partialSM(pB0, pB1, m_reg, mnB, alB);
  __syncthreads(); RESC(alB);
  finishSM(pB0, pB1, alB, l_reg, pa0, pa1, pa2, pa3); SBAR();
  pv_d0(o, vb0 + (int)SHM_V, pa0, pa1, pa2, pa3);
  if (hi == 0) li_l[r32] = l_reg; asm volatile("s_waitcnt lgkmcnt(0)" ::: "memory");
  float rli[16];
#pragma unroll
  for (int r = 0; r < 16; ++r) rli[r] = __builtin_amdgcn_rcpf(li_l[crow(r, hi)]);
  bf16* Ow = Ob + (long)(wid * QBLK) * LDO;
#pragma unroll
  for (int r = 0; r < 16; ++r) { int orow = crow(r, hi);
    if (wid * QBLK + orow < 248) for (int d0 = 0; d0 < 4; ++d0) Ow[(long)orow * LDO + d0 * 32 + r32] = __float2bfloat16(o[d0][r] * rli[r]); }
#undef SLOAD
#undef SWRITE
#undef SWAIT
#undef RESC
}

__global__ __launch_bounds__(64 * NW, 1)
void attn_fwd_dense(const bf16* __restrict__ Q, const bf16* __restrict__ K, const bf16* __restrict__ V, bf16* __restrict__ O) {
  extern __shared__ __attribute__((aligned(16))) char shm[];
  const int id = blockIdx.x, xcd = id & 7, j = id >> 3, g = (j >> 2) * 8 + xcd, y = j & 3;
  const long q0 = ((long)g * 992 + y * 248) * D, k0 = (long)g * 512 * D;
  attn_dense_body<bf16>(Q + q0, K + k0, V + k0, O + q0, 512, shm);
}
extern "C" void kernel_launch(void* const* d_in, const int* in_sizes, int n_in, void* d_out, int out_size, void* d_ws, size_t ws_size, hipStream_t stream) {
  hipFuncSetAttribute((const void*)attn_fwd_dense, hipFuncAttributeMaxDynamicSharedMemorySize, (int)SHM_ATTN);
  hipLaunchKernelGGL(attn_fwd_dense, dim3(1024), dim3(64 * NW), SHM_ATTN, stream,
                     (const bf16*)d_in[0], (const bf16*)d_in[1], (const bf16*)d_in[2], (bf16*)d_out);
}
```

```cpp
#include <hip/hip_runtime.h>
#include <hip/hip_bf16.h>

typedef unsigned short u16;

typedef float f32x4 __attribute__((ext_vector_type(4)));
typedef __bf16 bf16x8 __attribute__((ext_vector_type(8)));
typedef __bf16 bf16x2 __attribute__((ext_vector_type(2)));
typedef float f32x2 __attribute__((ext_vector_type(2)));
typedef unsigned u32x4 __attribute__((ext_vector_type(4)));
__device__ __forceinline__ unsigned pack_bf16(float lo, float hi) {
  const f32x2 v = {lo, hi};
  return __builtin_bit_cast(unsigned, __builtin_convertvector(v, bf16x2));
}

__device__ __forceinline__ void exact_tile(const u16* __restrict__ Q, const u16* __restrict__ K, const u16* __restrict__ V,
                                           __hip_bfloat16* __restrict__ O, int cols, int lane) {
  const float c = 0.12751743082459868f;
  const int i = lane & 15, g = lane >> 4;
  bf16x8 q[4];
#pragma unroll
  for (int d = 0; d < 4; ++d) q[d] = *(const bf16x8*)(Q + i * 128 + 32 * d + 8 * g);
  f32x4 s[32];
  float m = -INFINITY;
#pragma unroll
  for (int kt = 0; kt < 32; ++kt) {
    f32x4 a = {0.f, 0.f, 0.f, 0.f};
#pragma unroll
    for (int d = 0; d < 4; ++d)
      a = __builtin_amdgcn_mfma_f32_16x16x32_bf16(*(const bf16x8*)(K + (16 * kt + i) * 128 + 32 * d + 8 * g), q[d], a, 0, 0, 0);
    s[kt] = a;
    m = fmaxf(fmaxf(m, fmaxf(a[0], a[1])), fmaxf(a[2], a[3]));
    if ((kt & 3) == 3) __builtin_amdgcn_sched_barrier(0);
  }
  m = fmaxf(m, __shfl_xor(m, 16));
  m = fmaxf(m, __shfl_xor(m, 32));
  float l = 0.f;
  f32x4 o[8];
#pragma unroll
  for (int e = 0; e < 8; ++e) o[e] = f32x4{0.f, 0.f, 0.f, 0.f};
#pragma unroll
  for (int ks = 0; ks < 16; ++ks) {
    float p[8];
    u32x4 v[8], pk;
#pragma unroll
    for (int j = 0; j < 8; ++j) {
      p[j] = exp2f((s[2 * ks + (j >> 2)][j & 3] - m) * c);
      l += p[j];
      v[j] = *(const u32x4*)(V + (32 * ks + 16 * (j >> 2) + 4 * g + (j & 3)) * 128 + 8 * i);
    }
#pragma unroll
    for (int j = 0; j < 4; ++j) pk[j] = pack_bf16(p[2 * j], p[2 * j + 1]);
#pragma unroll
    for (int e = 0; e < 8; ++e) {
      u32x4 b;
#pragma unroll
      for (int j = 0; j < 4; ++j) b[j] = __builtin_amdgcn_perm(v[2 * j + 1][e >> 1], v[2 * j][e >> 1], (e & 1) ? 0x07060302u : 0x05040100u);
      o[e] = __builtin_amdgcn_mfma_f32_16x16x32_bf16(__builtin_bit_cast(bf16x8, pk), __builtin_bit_cast(bf16x8, b), o[e], 0, 0, 0);
    }
    __builtin_amdgcn_sched_barrier(0);
  }
  l += __shfl_xor(l, 16);
  l += __shfl_xor(l, 32);
  const float il = 1.f / l;
#pragma unroll
  for (int r = 0; r < 4; ++r) {
    const float w = __shfl(il, 4 * g + r);
    u32x4 t;
#pragma unroll
    for (int j = 0; j < 4; ++j) t[j] = pack_bf16(o[2 * j][r] * w, o[2 * j + 1][r] * w);
    if ((cols >> (i >> 3)) & 1) *(u32x4*)(O + (4 * g + r) * 128 + 8 * i) = t;
  }
}

__global__ __launch_bounds__(256, 1) __attribute__((aligned(4096))) void gqa_attn(const u16* __restrict__ Q, const u16* __restrict__ K, const u16* __restrict__ V,
                                                   __hip_bfloat16* __restrict__ O) {
  __shared__ __attribute__((aligned(16))) char lds[163840];
  const unsigned wave = __builtin_amdgcn_readfirstlane(threadIdx.x >> 6);
  const unsigned wg = blockIdx.x;
  const unsigned ldsb = (unsigned)(size_t)lds;
  unsigned mask;
  asm volatile(
"s_load_dwordx2 s[36:37], %1, 8\n"
"s_load_dwordx2 s[44:45], %1, 0\n"
"s_load_dwordx2 s[40:41], %1, 16\n"
"s_load_dwordx2 s[48:49], %1, 24\n"
"s_mov_b32 s60, %2\n"
"s_mov_b32 s61, m0\n"
"v_mbcnt_lo_u32_b32 v0, -1, 0\n"
"v_mbcnt_hi_u32_b32 v0, -1, v0\n"
"s_getpc_b64 s[62:63]\n Lpc%=:\n"
"s_lshr_b32 s57, %3, 3\n"
"s_lshl_b32 s57, s57, 7\n"
"v_lshl_add_u32 v17, v0, 12, s57\n"
"v_min_u32 v17, Lend%=-Lpc%=, v17\n"
"global_load_dword v235, v17, s[62:63]\n"
"s_mov_b32 s38, 33554432\n"
"s_mov_b32 s39, 0x20000\n"
"s_mov_b32 s46, 65011712\n"
"s_mov_b32 s47, 0x20000\n"
"s_mov_b32 s42, 33554432\n"
"s_mov_b32 s43, 0x20000\n"
"s_mov_b32 s50, 0\n"
"s_mov_b32 s51, 0x20000\n"
"s_lshr_b32 s59, %3, 5\n"
"s_lshl_b32 s59, s59, 3\n"
"s_and_b32 s62, %3, 7\n"
"s_add_u32 s59, s59, s62\n"
"s_lshl_b32 s64, s59, 17\n"
"s_add_u32 s64, s64, 25165824\n"
"s_bfe_u32 s69, %3, 0x20003\n"
"s_mul_i32 s59, s59, 253952\n"
"s_lshl_b32 s62, s69, 16\n"
"s_add_u32 s59, s59, s62\n"
"s_mul_i32 s53, s60, 4352\n"
"s_add_u32 s53, s53, %4\n"
"s_mul_i32 s54, s60, 4128\n"
"s_add_u32 s54, s54, %4\n"
"s_add_u32 s54, s54, 69632\n"
"s_mul_i32 s55, s60, 4384\n"
"s_add_u32 s55, s55, %4\n"
"s_add_u32 s55, s55, 135680\n"
"s_cmp_lt_u32 0, s69\n"
"s_cselect_b32 s62, 8192, 0\n"
"s_sub_u32 s62, s59, s62\n"
"s_add_u32 s62, s62, 48758784\n"
"s_cmp_eq_u32 s69, 0\n"
"s_cselect_b32 s76, 3, 4\n"
"s_mul_i32 s76, s76, s60\n"
"s_lshl_b32 s76, s76, 12\n"
"s_add_u32 s76, s76, s62\n"
"s_mov_b32 s86, s76\n"
"v_lshrrev_b32 v1, 4, v0\n"
"v_and_b32 v2, 15, v0\n"
"v_xor_b32 v3, v2, v1\n"
"v_lshlrev_b32 v3, 4, v3\n"
"v_lshl_add_u32 v3, v1, 8, v3\n"
"v_lshl_add_u32 v247, s60, 12, v3\n"
"v_bfe_u32 v4, v0, 2, 2\n"
"v_and_b32 v5, 3, v0\n"
"v_and_b32 v6, 2, v4\n"
"v_xor_b32 v6, v6, v5\n"
"v_lshlrev_b32 v6, 4, v6\n"
"v_lshl_add_u32 v6, v1, 6, v6\n"
"v_lshl_add_u32 v249, v4, 8, v6\n"
"s_waitcnt lgkmcnt(0)\n"
"s_getreg_b32 s57, hwreg(HW_REG_XCC_ID, 0, 3)\n s_lshr_b32 s57, 85, s57\n s_bitcmp0_b32 s57, 0\n s_cbranch_scc1 Lgo%=\n s_sleep 8\n Lgo%=:\n"
"s_and_b32 s45, s45, 0xffff\n"
"s_add_u32 m0, s55, 0\n"
"s_nop 0\n"
"buffer_load_dwordx4 v249, s[44:47], s76 offen offset:0 nt lds\n"
"s_add_u32 m0, s55, 8\n"
"s_nop 0\n"
"buffer_load_dwordx4 v249, s[44:47], s76 offen offset:1024 nt lds\n"
"s_add_u32 m0, s55, 16\n"
"s_nop 0\n"
"buffer_load_dwordx4 v249, s[44:47], s76 offen offset:2048 nt lds\n"
"s_add_u32 m0, s55, 24\n"
"s_nop 0\n"
"buffer_load_dwordx4 v249, s[44:47], s76 offen offset:3072 nt lds\n"
"s_add_u32 s76, s76, 4096\n"
"s_and_b32 s37, s37, 0xffff\n"
"s_add_u32 m0, s53, 0\n"
"s_nop 0\n"
"buffer_load_dwordx4 v247, s[36:39], s64 offen offset:0 lds\n"
"s_add_u32 m0, s53, 64\n"
"s_nop 0\n"
"buffer_load_dwordx4 v247, s[36:39], s64 offen offset:1024 lds\n"
"s_add_u32 m0, s53, 128\n"
"s_nop 0\n"
"buffer_load_dwordx4 v247, s[36:39], s64 offen offset:2048 lds\n"
"s_add_u32 m0, s53, 192\n"
"s_nop 0\n"
"buffer_load_dwordx4 v247, s[36:39], s64 offen offset:3072 lds\n"
"s_add_u32 m0, s54, 33024\n"
"s_nop 0\n"
"buffer_load_dwordx4 v249, s[44:47], s76 offen offset:0 nt lds\n"
"s_add_u32 m0, s54, 33032\n"
"s_nop 0\n"
"buffer_load_dwordx4 v249, s[44:47], s76 offen offset:1024 nt lds\n"
"s_add_u32 m0, s54, 33040\n"
"s_nop 0\n"
"buffer_load_dwordx4 v249, s[44:47], s76 offen offset:2048 nt lds\n"
"s_add_u32 m0, s54, 33048\n"
"s_nop 0\n"
"buffer_load_dwordx4 v249, s[44:47], s76 offen offset:3072 nt lds\n"
"s_add_u32 s76, s76, 4096\n"
"s_and_b32 s41, s41, 0xffff\n"
"s_and_b32 s71, s60, 1\n"
"s_lshr_b32 s35, s60, 1\n"
"s_add_u32 s35, s35, 12\n"
"s_lshl_b32 s35, s35, 12\n"
"s_add_u32 s63, s35, s62\n"
"s_add_u32 s77, s86, 12288\n"
"s_cmp_eq_u32 s69, 0\n"
"s_cselect_b32 s77, s63, s77\n"
"s_cselect_b32 s63, s71, 0\n"
"s_lshl_b32 s63, s63, 7\n"
"s_add_u32 s87, s77, s63\n"
"s_add_u32 s65, s64, 16384\n"
"s_add_u32 s66, s64, 32768\n"
"v_lshrrev_b32 v7, 5, v0\n"
"v_and_b32 v8, 1, v1\n"
"v_lshlrev_b32 v9, 4, v5\n"
"v_lshl_add_u32 v10, v8, 6, v9\n"
"v_lshl_add_u32 v10, v4, 8, v10\n"
"v_lshl_add_u32 v10, v7, 11, v10\n"
"v_lshl_add_u32 v248, s60, 12, v10\n"
"v_lshrrev_b32 v11, 2, v2\n"
"v_mul_u32_u24 v12, 1088, v11\n"
"v_lshl_add_u32 v12, v5, 8, v12\n"
"v_xor_b32 v13, v1, v5\n"
"v_lshl_add_u32 v12, v13, 4, v12\n"
"v_add_u32 v12, %4, v12\n"
"s_mul_i32 s62, s71, 8704\n"
"s_sub_u32 s63, 8704, s62\n"
"v_add_u32 v244, s62, v12\n"
"v_add_u32 v222, s63, v12\n"
"v_mul_u32_u24 v12, 1032, v8\n"
"v_lshl_add_u32 v12, v7, 9, v12\n"
"v_lshl_add_u32 v12, v4, 6, v12\n"
"v_add_u32 v12, v12, v9\n"
"v_add_u32 v12, 69632, v12\n"
"v_add_u32 v12, %4, v12\n"
"s_mul_i32 s62, s71, 8256\n"
"s_sub_u32 s63, 8256, s62\n"
"v_add_u32 v245, s62, v12\n"
"v_add_u32 v255, s63, v12\n"
"v_mul_u32_u24 v12, 1032, v11\n"
"v_lshl_add_u32 v12, v5, 6, v12\n"
"v_bfe_u32 v13, v0, 1, 1\n"
"v_xor_b32 v13, v13, v8\n"
"v_lshl_add_u32 v12, v13, 5, v12\n"
"v_add_u32 v246, s55, v12\n"
"v_add_u32 v13, s54, v12\n"
"v_add_u32 v232, 33024, v13\n"
"v_add_u32 v13, s53, v12\n"
"v_add_u32 v233, 52224, v13\n"
"v_add_u32 v13, s54, v12\n"
"v_add_u32 v234, 49536, v13\n"
"s_mul_i32 s62, s60, 2304\n"
"s_add_u32 s62, s62, %4\n"
"s_add_u32 s62, s62, 153216\n"
"v_mul_u32_u24 v12, 144, v2\n"
"v_lshl_add_u32 v12, v1, 4, v12\n"
"v_add_u32 v216, s62, v12\n"
"v_bfe_u32 v13, v0, 2, 3\n"
"v_bcnt_u32_b32 v13, v13, 0\n"
"v_and_b32 v13, 1, v13\n"
"v_lshl_add_u32 v13, v7, 1, v13\n"
"v_lshl_add_u32 v13, v8, 3, v13\n"
"v_bfe_u32 v14, v0, 3, 1\n"
"v_lshl_add_u32 v14, v14, 6, v9\n"
"v_mul_u32_u24 v12, 144, v13\n"
"v_add_u32 v12, v12, v14\n"
"v_add_u32 v217, s62, v12\n"
"v_lshl_add_u32 v250, v13, 8, v14\n"
"v_mov_b32 v14, 0x3f803f80\n"
"v_cmp_gt_u32 vcc, 2, v5\n"
"s_nop 1\n"
"v_cndmask_b32 v14, 0, v14, vcc\n"
"v_accvgpr_write_b32 a144, v14\n"
"v_accvgpr_write_b32 a145, v14\n"
"v_accvgpr_write_b32 a146, v14\n"
"v_accvgpr_write_b32 a147, v14\n"
"v_and_b32 v13, 1, v0\n"
"v_mov_b32 v14, 0xeeeeeeee\n"
"v_mov_b32 v15, 0x44444444\n"
"v_cmp_eq_u32 vcc, 1, v13\n"
"s_nop 1\n"
"v_cndmask_b32 v251, v15, v14, vcc\n"
"v_mov_b32 v14, 15875\n"
"v_mov_b32 v15, 47576\n"
"v_cmp_eq_u32 vcc, 1, v7\n"
"s_nop 1\n"
"v_cndmask_b32 v14, v14, v15, vcc\n"
"v_lshlrev_b32 v13, 4, v13\n"
"v_lshlrev_b32 v14, v13, v14\n"
"v_lshrrev_b32 v15, 1, v2\n"
"v_lshlrev_b32 v16, 2, v8\n"
"v_sub_u32 v15, v15, v16\n"
"v_cmp_eq_u32 vcc, 0, v15\n"
"s_nop 1\n"
"v_cndmask_b32 v16, 0, v14, vcc\n"
"v_accvgpr_write_b32 a148, v16\n"
"v_cmp_eq_u32 vcc, 1, v15\n"
"s_nop 1\n"
"v_cndmask_b32 v16, 0, v14, vcc\n"
"v_accvgpr_write_b32 a149, v16\n"
"v_cmp_eq_u32 vcc, 2, v15\n"
"s_nop 1\n"
"v_cndmask_b32 v16, 0, v14, vcc\n"
"v_accvgpr_write_b32 a150, v16\n"
"v_cmp_eq_u32 vcc, 3, v15\n"
"s_nop 1\n"
"v_cndmask_b32 v16, 0, v14, vcc\n"
"v_accvgpr_write_b32 a151, v16\n"
"v_accvgpr_write_b32 a128, 0\n"
"v_accvgpr_write_b32 a129, 0\n"
"v_accvgpr_write_b32 a130, 0\n"
"v_accvgpr_write_b32 a131, 0\n"
"v_accvgpr_write_b32 a132, 0\n"
"v_accvgpr_write_b32 a133, 0\n"
"v_accvgpr_write_b32 a134, 0\n"
"v_accvgpr_write_b32 a135, 0\n"
"v_accvgpr_write_b32 a136, 0\n"
"v_accvgpr_write_b32 a137, 0\n"
"v_accvgpr_write_b32 a138, 0\n"
"v_accvgpr_write_b32 a139, 0\n"
"v_accvgpr_write_b32 a140, 0\n"
"v_accvgpr_write_b32 a141, 0\n"
"v_accvgpr_write_b32 a142, 0\n"
"v_accvgpr_write_b32 a143, 0\n"
"s_mov_b32 s72, 0x07060302\n"
"s_mov_b64 exec, 0\n s_cmp_eq_u32 s60, 0\n s_cbranch_scc1 Lhop0_0_%=\n s_cmp_eq_u32 s60, 1\n s_cbranch_scc1 Lhop0_1_%=\n s_cmp_eq_u32 s60, 2\n s_cbranch_scc1 Lhop0_2_%=\n s_branch Lhop0_3_%=\n Lback0_%=:\n s_mov_b64 exec, -1\n"
"s_add_u32 m0, s54, 0\n"
"s_nop 0\n"
"buffer_load_dwordx4 v248, s[40:43], s64 offen offset:0 lds\n"
"s_add_u32 m0, s54, 8\n"
"s_nop 0\n"
"buffer_load_dwordx4 v248, s[40:43], s64 offen offset:1024 lds\n"
"s_add_u32 m0, s54, 1936\n"
"s_nop 0\n"
"buffer_load_dwordx4 v248, s[40:43], s64 offen offset:128 lds\n"
"s_add_u32 m0, s54, 1944\n"
"s_nop 0\n"
"buffer_load_dwordx4 v248, s[40:43], s64 offen offset:1152 lds\n"
"s_waitcnt vmcnt(12)\n"
"ds_read2_b64 a[152:155], v246 offset0:0 offset1:2\n"
"ds_read2_b64 a[156:159], v246 offset0:1 offset1:3\n"
"ds_read2_b64 a[160:163], v246 offset0:32 offset1:34\n"
"ds_read2_b64 a[164:167], v246 offset0:33 offset1:35\n"
"ds_read2_b64 a[168:171], v246 offset0:64 offset1:66\n"
"ds_read2_b64 a[172:175], v246 offset0:65 offset1:67\n"
"ds_read2_b64 a[176:179], v246 offset0:96 offset1:98\n"
"ds_read2_b64 a[180:183], v246 offset0:97 offset1:99\n"
"s_add_u32 m0, s53, 52224\n"
"s_nop 0\n"
"buffer_load_dwordx4 v249, s[44:47], s76 offen offset:0 nt lds\n"
"s_add_u32 m0, s53, 52232\n"
"s_nop 0\n"
"buffer_load_dwordx4 v249, s[44:47], s76 offen offset:1024 nt lds\n"
"s_waitcnt lgkmcnt(7)\n"
"v_mfma_f32_16x16x32_bf16 v[0:3], a[148:151], a[152:155], 0\n"
"s_waitcnt lgkmcnt(6)\n"
"v_mfma_f32_16x16x32_bf16 v[4:7], a[148:151], a[156:159], 0\n"
"s_waitcnt lgkmcnt(5)\n"
"v_mfma_f32_16x16x32_bf16 v[8:11], a[148:151], a[160:163], 0\n"
"s_waitcnt lgkmcnt(4)\n"
"v_mfma_f32_16x16x32_bf16 v[12:15], a[148:151], a[164:167], 0\n"
"s_nop 1\n"
"v_cvt_pk_bf16_f32 v128, v0, v1\n"
"v_cvt_pk_bf16_f32 v129, v2, v3\n"
"s_waitcnt lgkmcnt(3)\n"
"v_mfma_f32_16x16x32_bf16 v[16:19], a[148:151], a[168:171], 0\n"
"v_cvt_pk_bf16_f32 v130, v4, v5\n"
"v_cvt_pk_bf16_f32 v131, v6, v7\n"
"s_waitcnt lgkmcnt(2)\n"
"v_mfma_f32_16x16x32_bf16 v[20:23], a[148:151], a[172:175], 0\n"
"v_cvt_pk_bf16_f32 v132, v8, v9\n"
"v_cvt_pk_bf16_f32 v133, v10, v11\n"
"s_waitcnt lgkmcnt(1)\n"
"v_mfma_f32_16x16x32_bf16 v[24:27], a[148:151], a[176:179], 0\n"
"v_cvt_pk_bf16_f32 v134, v12, v13\n"
"v_cvt_pk_bf16_f32 v135, v14, v15\n"
"s_waitcnt lgkmcnt(0)\n"
"v_mfma_f32_16x16x32_bf16 v[28:31], a[148:151], a[180:183], 0\n"
"v_cvt_pk_bf16_f32 v136, v16, v17\n"
"v_cvt_pk_bf16_f32 v137, v18, v19\n"
"v_cvt_pk_bf16_f32 v138, v20, v21\n"
"v_cvt_pk_bf16_f32 v139, v22, v23\n"
"v_cvt_pk_bf16_f32 v140, v24, v25\n"
"v_cvt_pk_bf16_f32 v141, v26, v27\n"
"s_nop 1\n"
"v_cvt_pk_bf16_f32 v142, v28, v29\n"
"v_cvt_pk_bf16_f32 v143, v30, v31\n"
"s_add_u32 m0, s53, 52240\n"
"s_nop 0\n"
"buffer_load_dwordx4 v249, s[44:47], s76 offen offset:2048 nt lds\n"
"s_add_u32 m0, s53, 52248\n"
"s_nop 0\n"
"buffer_load_dwordx4 v249, s[44:47], s76 offen offset:3072 nt lds\n"
"s_mov_b32 s76, s77\n"
"s_waitcnt vmcnt(12)\n"
"s_barrier\n"
"ds_read_b128 a[0:3], v244 offset:0\n"
"ds_read_b128 a[4:7], v244 offset:64\n"
"ds_read_b128 a[8:11], v244 offset:128\n"
"ds_read_b128 a[12:15], v244 offset:192\n"
"ds_read_b128 a[28:31], v244 offset:4544\n"
"ds_read_b128 a[24:27], v244 offset:4480\n"
"ds_read_b128 a[20:23], v244 offset:4416\n"
"ds_read_b128 a[16:19], v244 offset:4352\n"
"ds_read_b128 a[32:35], v222 offset:0\n"
"ds_read_b128 a[36:39], v222 offset:64\n"
"ds_read_b128 a[40:43], v222 offset:128\n"
"ds_read_b128 a[44:47], v222 offset:192\n"
"ds_read_b128 a[60:63], v222 offset:4544\n"
"ds_read_b128 a[56:59], v222 offset:4480\n"
"ds_read_b128 a[52:55], v222 offset:4416\n"
"ds_read_b128 a[48:51], v222 offset:4352\n"
"s_waitcnt lgkmcnt(14)\n"
"v_mfma_f32_16x16x32_bf16 v[192:195], a[0:3], v[128:131], -4.0\n"
"s_cmp_lt_u32 1, s69\n"
"v_mfma_f32_16x16x32_bf16 v[192:195], a[4:7], v[132:135], v[192:195]\n"
"s_cselect_b32 s62, 8192, 0\n"
"s_waitcnt lgkmcnt(13)\n"
"v_mfma_f32_16x16x32_bf16 v[192:195], a[8:11], v[136:139], v[192:195]\n"
"s_sub_u32 s62, s59, s62\n"
"s_waitcnt lgkmcnt(12)\n"
"v_mfma_f32_16x16x32_bf16 v[192:195], a[12:15], v[140:143], v[192:195]\n"
"s_waitcnt lgkmcnt(9)\n"
"v_mfma_f32_16x16x32_bf16 v[196:199], a[28:31], v[140:143], -4.0\n"
"s_add_u32 s62, s62, 32505856\n"
"v_mfma_f32_16x16x32_bf16 v[196:199], a[24:27], v[136:139], v[196:199]\n"
"s_cmp_eq_u32 s69, 1\n"
"v_mfma_f32_16x16x32_bf16 v[196:199], a[20:23], v[132:135], v[196:199]\n"
"s_cselect_b32 s78, 3, 4\n"
"s_waitcnt lgkmcnt(8)\n"
"v_mfma_f32_16x16x32_bf16 v[196:199], a[16:19], v[128:131], v[196:199]\n"
"s_waitcnt lgkmcnt(5)\n"
"v_mfma_f32_16x16x32_bf16 v[224:227], a[32:35], v[128:131], -4.0\n"
"s_mul_i32 s78, s78, s60\n"
"v_exp_f32 v192, v192\n"
"v_mfma_f32_16x16x32_bf16 v[224:227], a[36:39], v[132:135], v[224:227]\n"
"s_lshl_b32 s78, s78, 12\n"
"v_mfma_f32_16x16x32_bf16 v[224:227], a[40:43], v[136:139], v[224:227]\n"
"s_add_u32 s78, s78, s62\n"
"v_exp_f32 v193, v193\n"
"s_waitcnt lgkmcnt(4)\n"
"v_mfma_f32_16x16x32_bf16 v[224:227], a[44:47], v[140:143], v[224:227]\n"
"s_waitcnt lgkmcnt(0)\n"
"v_perm_b32 v200, v193, v192, s72\n"
"v_mfma_f32_16x16x32_bf16 v[228:231], a[60:63], v[140:143], -4.0\n"
"s_mov_b32 s88, s78\n"
"v_exp_f32 v194, v194\n"
"v_mfma_f32_16x16x32_bf16 v[228:231], a[56:59], v[136:139], v[228:231]\n"
"s_add_u32 s63, s35, s62\n"
"v_mfma_f32_16x16x32_bf16 v[228:231], a[52:55], v[132:135], v[228:231]\n"
"s_add_u32 s79, s88, 12288\n"
"v_exp_f32 v195, v195\n"
"v_mfma_f32_16x16x32_bf16 v[228:231], a[48:51], v[128:131], v[228:231]\n"
"v_perm_b32 v201, v195, v194, s72\n"
"v_exp_f32 v196, v196\n"
"v_exp_f32 v197, v197\n"
"s_nop 0\n"
"v_perm_b32 v202, v197, v196, s72\n"
"v_exp_f32 v198, v198\n"
"v_exp_f32 v199, v199\n"
"s_nop 0\n"
"v_perm_b32 v203, v199, v198, s72\n"
"s_mov_b64 exec, 0\n s_cmp_eq_u32 s60, 0\n s_cbranch_scc1 Lhop1_0_%=\n s_cmp_eq_u32 s60, 1\n s_cbranch_scc1 Lhop1_1_%=\n s_cmp_eq_u32 s60, 2\n s_cbranch_scc1 Lhop1_2_%=\n s_branch Lhop1_3_%=\n Lback1_%=:\n s_mov_b64 exec, -1\n"
"s_add_u32 m0, s53, 17408\n"
"s_nop 0\n"
"buffer_load_dwordx4 v247, s[36:39], s65 offen offset:0 lds\n"
"s_add_u32 m0, s53, 17472\n"
"s_nop 0\n"
"buffer_load_dwordx4 v247, s[36:39], s65 offen offset:1024 lds\n"
"s_add_u32 m0, s53, 17536\n"
"s_nop 0\n"
"buffer_load_dwordx4 v247, s[36:39], s65 offen offset:2048 lds\n"
"s_add_u32 m0, s53, 17600\n"
"s_nop 0\n"
"buffer_load_dwordx4 v247, s[36:39], s65 offen offset:3072 lds\n"
"s_waitcnt vmcnt(12)\n"
"ds_read2_b64 a[184:187], v232 offset0:0 offset1:2\n"
"ds_read2_b64 a[188:191], v232 offset0:1 offset1:3\n"
"ds_read2_b64 a[192:195], v232 offset0:32 offset1:34\n"
"ds_read2_b64 a[196:199], v232 offset0:33 offset1:35\n"
"ds_read2_b64 a[200:203], v232 offset0:64 offset1:66\n"
"ds_read2_b64 a[204:207], v232 offset0:65 offset1:67\n"
"ds_read2_b64 a[208:211], v232 offset0:96 offset1:98\n"
"ds_read2_b64 a[212:215], v232 offset0:97 offset1:99\n"
"v_exp_f32 v224, v224\n"
"v_exp_f32 v225, v225\n"
"s_nop 0\n"
"v_perm_b32 v204, v225, v224, s72\n"
"v_exp_f32 v226, v226\n"
"v_exp_f32 v227, v227\n"
"s_nop 0\n"
"v_perm_b32 v205, v227, v226, s72\n"
"s_waitcnt lgkmcnt(7)\n"
"v_mfma_f32_16x16x32_bf16 v[32:35], a[148:151], a[184:187], 0\n"
"s_cmp_eq_u32 s69, 1\n"
"s_waitcnt lgkmcnt(6)\n"
"v_mfma_f32_16x16x32_bf16 v[36:39], a[148:151], a[188:191], 0\n"
"s_cselect_b32 s79, s63, s79\n"
"s_waitcnt lgkmcnt(5)\n"
"v_mfma_f32_16x16x32_bf16 v[40:43], a[148:151], a[192:195], 0\n"
"s_cselect_b32 s63, s71, 0\n"
"s_waitcnt lgkmcnt(4)\n"
"v_mfma_f32_16x16x32_bf16 v[44:47], a[148:151], a[196:199], 0\n"
"s_waitcnt lgkmcnt(0)\n"
"v_cvt_pk_bf16_f32 v144, v32, v33\n"
"v_cvt_pk_bf16_f32 v145, v34, v35\n"
"v_mfma_f32_16x16x32_bf16 v[48:51], a[148:151], a[200:203], 0\n"
"s_lshl_b32 s63, s63, 7\n"
"v_cvt_pk_bf16_f32 v146, v36, v37\n"
"v_cvt_pk_bf16_f32 v147, v38, v39\n"
"v_mfma_f32_16x16x32_bf16 v[52:55], a[148:151], a[204:207], 0\n"
"s_add_u32 s89, s79, s63\n"
"v_cvt_pk_bf16_f32 v148, v40, v41\n"
"v_cvt_pk_bf16_f32 v149, v42, v43\n"
"v_mfma_f32_16x16x32_bf16 v[56:59], a[148:151], a[208:211], 0\n"
"s_cmp_lt_u32 2, s69\n"
"v_cvt_pk_bf16_f32 v150, v44, v45\n"
"v_cvt_pk_bf16_f32 v151, v46, v47\n"
"v_mfma_f32_16x16x32_bf16 v[60:63], a[148:151], a[212:215], 0\n"
"s_cselect_b32 s62, 8192, 0\n"
"v_cvt_pk_bf16_f32 v152, v48, v49\n"
"v_cvt_pk_bf16_f32 v153, v50, v51\n"
"v_cvt_pk_bf16_f32 v154, v52, v53\n"
"v_cvt_pk_bf16_f32 v155, v54, v55\n"
"v_cvt_pk_bf16_f32 v156, v56, v57\n"
"v_cvt_pk_bf16_f32 v157, v58, v59\n"
"s_nop 0\n"
"v_cvt_pk_bf16_f32 v158, v60, v61\n"
"v_cvt_pk_bf16_f32 v159, v62, v63\n"
"s_add_u32 m0, s54, 49536\n"
"s_nop 0\n"
"buffer_load_dwordx4 v249, s[44:47], s76 offen offset:0 nt lds\n"
"s_add_u32 m0, s54, 49544\n"
"s_nop 0\n"
"buffer_load_dwordx4 v249, s[44:47], s76 offen offset:1024 nt lds\n"
"s_add_u32 m0, s54, 49552\n"
"s_nop 0\n"
"buffer_load_dwordx4 v249, s[44:47], s76 offen offset:2048 nt lds\n"
"s_add_u32 m0, s54, 49560\n"
"s_nop 0\n"
"buffer_load_dwordx4 v249, s[44:47], s76 offen offset:3072 nt lds\n"
"s_waitcnt vmcnt(12)\n"
"s_barrier\n"
"v_mfma_f32_16x16x32_bf16 v[192:195], a[0:3], v[144:147], -4.0\n"
"s_sub_u32 s62, s59, s62\n"
"v_exp_f32 v228, v228\n"
"v_mfma_f32_16x16x32_bf16 v[192:195], a[4:7], v[148:151], v[192:195]\n"
"s_add_u32 s62, s62, 16252928\n"
"ds_read_b64_tr_b16 a[64:65], v245 offset:0\n"
"ds_read_b64_tr_b16 a[66:67], v245 offset:4128\n"
"v_mfma_f32_16x16x32_bf16 v[192:195], a[8:11], v[152:155], v[192:195]\n"
"s_cmp_eq_u32 s69, 2\n"
"v_exp_f32 v229, v229\n"
"v_mfma_f32_16x16x32_bf16 v[192:195], a[12:15], v[156:159], v[192:195]\n"
"s_cselect_b32 s80, 3, 4\n"
"ds_read_b64_tr_b16 a[68:69], v255 offset:0\n"
"v_perm_b32 v206, v229, v228, s72\n"
"v_mfma_f32_16x16x32_bf16 v[196:199], a[28:31], v[156:159], -4.0\n"
"s_mul_i32 s80, s80, s60\n"
"v_exp_f32 v230, v230\n"
"v_mfma_f32_16x16x32_bf16 v[196:199], a[24:27], v[152:155], v[196:199]\n"
"s_lshl_b32 s80, s80, 12\n"
"ds_read_b64_tr_b16 a[70:71], v255 offset:4128\n"
"ds_read_b64_tr_b16 a[76:77], v255 offset:8\n"
"v_mfma_f32_16x16x32_bf16 v[196:199], a[20:23], v[148:151], v[196:199]\n"
"s_add_u32 s80, s80, s62\n"
"v_exp_f32 v231, v231\n"
"v_mfma_f32_16x16x32_bf16 v[196:199], a[16:19], v[144:147], v[196:199]\n"
"ds_read_b64_tr_b16 a[78:79], v255 offset:4136\n"
"v_perm_b32 v207, v231, v230, s72\n"
"v_mfma_f32_16x16x32_bf16 v[224:227], a[32:35], v[144:147], -4.0\n"
"s_mov_b32 s90, s80\n"
"v_exp_f32 v192, v192\n"
"v_mfma_f32_16x16x32_bf16 v[224:227], a[36:39], v[148:151], v[224:227]\n"
"s_add_u32 s63, s35, s62\n"
"ds_read_b64_tr_b16 a[72:73], v245 offset:8\n"
"ds_read_b64_tr_b16 a[74:75], v245 offset:4136\n"
"v_mfma_f32_16x16x32_bf16 v[224:227], a[40:43], v[152:155], v[224:227]\n"
"s_add_u32 s81, s90, 12288\n"
"v_exp_f32 v193, v193\n"
"v_mfma_f32_16x16x32_bf16 v[224:227], a[44:47], v[156:159], v[224:227]\n"
"ds_read_b64_tr_b16 a[80:81], v245 offset:256\n"
"v_perm_b32 v208, v193, v192, s72\n"
"v_mfma_f32_16x16x32_bf16 v[228:231], a[60:63], v[156:159], -4.0\n"
"s_cmp_eq_u32 s69, 2\n"
"v_exp_f32 v194, v194\n"
"v_mfma_f32_16x16x32_bf16 v[228:231], a[56:59], v[152:155], v[228:231]\n"
"s_cselect_b32 s81, s63, s81\n"
"ds_read_b64_tr_b16 a[82:83], v245 offset:4384\n"
"ds_read_b64_tr_b16 a[84:85], v255 offset:256\n"
"v_mfma_f32_16x16x32_bf16 v[228:231], a[52:55], v[148:151], v[228:231]\n"
"s_cselect_b32 s63, s71, 0\n"
"v_exp_f32 v195, v195\n"
"v_mfma_f32_16x16x32_bf16 v[228:231], a[48:51], v[144:147], v[228:231]\n"
"s_waitcnt lgkmcnt(7)\n"
"ds_read_b64_tr_b16 a[86:87], v255 offset:4384\n"
"v_perm_b32 v209, v195, v194, s72\n"
"s_waitcnt lgkmcnt(11)\n"
"v_accvgpr_read_b32 v253, a64\n"
"s_waitcnt lgkmcnt(9)\n"
"v_accvgpr_read_b32 v254, a68\n"
"v_max_f32 v253, abs(v253), abs(v254)\n"
"s_nop 1\n"
"v_max_f32_dpp v253, v253, v253 row_shr:1 row_mask:0xf bank_mask:0xf\n"
"s_nop 1\n"
"v_max_f32_dpp v253, v253, v253 row_shr:2 row_mask:0xf bank_mask:0xf\n"
"s_nop 1\n"
"v_max_f32_dpp v253, v253, v253 row_shr:4 row_mask:0xf bank_mask:0xf\n"
"s_nop 1\n"
"v_max_f32_dpp v253, v253, v253 row_shr:8 row_mask:0xf bank_mask:0xf\n"
"s_nop 1\n"
"v_readlane_b32 s73, v253, 15\n"
"s_nop 1\n"
"s_and_b32 s73, s73, 0x7f800000\n s_cselect_b32 s74, 0x1000000, 0\n s_add_u32 s73, s73, s74\n s_cmp_lt_u32 s73, 0x64000000\n s_cselect_b32 s73, s73, 0\n"
"v_mov_b32 v218, s73\n"
"v_mov_b32 v219, s73\n"
"v_mov_b32 v220, s73\n"
"v_mov_b32 v221, s73\n"
"v_smfmac_f32_16x16x64_bf16 a[128:131], a[144:147], v[200:207], v251\n"
"s_lshl_b32 s63, s63, 7\n"
"ds_read_b64_tr_b16 a[92:93], v255 offset:264\n"
"ds_read_b64_tr_b16 a[94:95], v255 offset:4392\n"
"v_mfma_f32_16x16x32_bf16 v[0:3], a[64:67], v[200:203], v[218:221]\n"
"s_add_u32 s91, s81, s63\n"
"v_exp_f32 v196, v196\n"
"v_mfma_f32_16x16x32_bf16 v[0:3], a[68:71], v[204:207], v[0:3]\n"
"s_cmp_lt_u32 3, s69\n"
"ds_read_b64_tr_b16 a[88:89], v245 offset:264\n"
"ds_read_b64_tr_b16 a[90:91], v245 offset:4392\n"
"ds_read_b64_tr_b16 a[96:97], v245 offset:2064\n"
"ds_read_b64_tr_b16 a[98:99], v245 offset:6192\n"
"s_waitcnt lgkmcnt(12)\n"
"v_mfma_f32_16x16x32_bf16 v[4:7], a[76:79], v[204:207], v[218:221]\n"
"s_cselect_b32 s62, 8192, 0\n"
"v_exp_f32 v197, v197\n"
"s_waitcnt lgkmcnt(10)\n"
"v_mfma_f32_16x16x32_bf16 v[4:7], a[72:75], v[200:203], v[4:7]\n"
"s_sub_u32 s62, s59, s62\n"
"ds_read_b64_tr_b16 a[100:101], v255 offset:2064\n"
"ds_read_b64_tr_b16 a[102:103], v255 offset:6192\n"
"ds_read_b64_tr_b16 a[108:109], v255 offset:2072\n"
"ds_read_b64_tr_b16 a[110:111], v255 offset:6200\n"
"v_perm_b32 v210, v197, v196, s72\n"
"s_waitcnt lgkmcnt(12)\n"
"v_mfma_f32_16x16x32_bf16 v[8:11], a[80:83], v[200:203], v[218:221]\n"
"s_cmp_eq_u32 s69, 3\n"
"v_exp_f32 v198, v198\n"
"s_waitcnt lgkmcnt(10)\n"
"v_mfma_f32_16x16x32_bf16 v[8:11], a[84:87], v[204:207], v[8:11]\n"
"s_cselect_b32 s82, 3, 4\n"
"ds_read_b64_tr_b16 a[104:105], v245 offset:2072\n"
"ds_read_b64_tr_b16 a[106:107], v245 offset:6200\n"
"ds_read_b64_tr_b16 a[112:113], v245 offset:2320\n"
"ds_read_b64_tr_b16 a[114:115], v245 offset:6448\n"
"s_waitcnt lgkmcnt(12)\n"
"v_mfma_f32_16x16x32_bf16 v[12:15], a[92:95], v[204:207], v[218:221]\n"
"s_waitcnt lgkmcnt(6)\n"
"v_exp_f32 v199, v199\n"
"v_mfma_f32_16x16x32_bf16 v[12:15], a[88:91], v[200:203], v[12:15]\n"
"s_mul_i32 s82, s82, s60\n"
"ds_read_b64_tr_b16 a[116:117], v255 offset:2320\n"
"ds_read_b64_tr_b16 a[118:119], v255 offset:6448\n"
"ds_read_b64_tr_b16 a[124:125], v255 offset:2328\n"
"ds_read_b64_tr_b16 a[126:127], v255 offset:6456\n"
"v_perm_b32 v211, v199, v198, s72\n"
"s_mov_b64 exec, 0\n s_cmp_eq_u32 s60, 0\n s_cbranch_scc1 Lhop2_0_%=\n s_cmp_eq_u32 s60, 1\n s_cbranch_scc1 Lhop2_1_%=\n s_cmp_eq_u32 s60, 2\n s_cbranch_scc1 Lhop2_2_%=\n s_branch Lhop2_3_%=\n Lback2_%=:\n s_mov_b64 exec, -1\n"
"s_add_u32 m0, s54, 16512\n"
"s_nop 0\n"
"buffer_load_dwordx4 v248, s[40:43], s65 offen offset:0 lds\n"
"s_add_u32 m0, s54, 16520\n"
"s_nop 0\n"
"buffer_load_dwordx4 v248, s[40:43], s65 offen offset:1024 lds\n"
"s_add_u32 m0, s54, 18448\n"
"s_nop 0\n"
"buffer_load_dwordx4 v248, s[40:43], s65 offen offset:128 lds\n"
"s_add_u32 m0, s54, 18456\n"
"s_nop 0\n"
"buffer_load_dwordx4 v248, s[40:43], s65 offen offset:1152 lds\n"
"s_waitcnt vmcnt(12)\n"
"ds_read2_b64 a[216:219], v233 offset0:0 offset1:2\n"
"ds_read2_b64 a[220:223], v233 offset0:1 offset1:3\n"
"ds_read2_b64 a[224:227], v233 offset0:32 offset1:34\n"
"ds_read2_b64 a[228:231], v233 offset0:33 offset1:35\n"
"ds_read2_b64 a[232:235], v233 offset0:64 offset1:66\n"
"ds_read2_b64 a[236:239], v233 offset0:65 offset1:67\n"
"ds_read2_b64 a[240:243], v233 offset0:96 offset1:98\n"
"ds_read2_b64 a[244:247], v233 offset0:97 offset1:99\n"
"v_mfma_f32_16x16x32_bf16 v[16:19], a[96:99], v[200:203], v[218:221]\n"
"s_lshl_b32 s82, s82, 12\n"
"v_exp_f32 v224, v224\n"
"v_mfma_f32_16x16x32_bf16 v[16:19], a[100:103], v[204:207], v[16:19]\n"
"s_add_u32 s82, s82, s62\n"
"ds_read_b64_tr_b16 a[120:121], v245 offset:2328\n"
"ds_read_b64_tr_b16 a[122:123], v245 offset:6456\n"
"s_waitcnt lgkmcnt(14)\n"
"v_mfma_f32_16x16x32_bf16 v[20:23], a[108:111], v[204:207], v[218:221]\n"
"s_waitcnt lgkmcnt(12)\n"
"v_exp_f32 v225, v225\n"
"v_mfma_f32_16x16x32_bf16 v[20:23], a[104:107], v[200:203], v[20:23]\n"
"s_mov_b32 s92, s82\n"
"v_perm_b32 v212, v225, v224, s72\n"
"v_mfma_f32_16x16x32_bf16 v[24:27], a[112:115], v[200:203], v[218:221]\n"
"s_add_u32 s63, s35, s62\n"
"v_exp_f32 v226, v226\n"
"v_mfma_f32_16x16x32_bf16 v[24:27], a[116:119], v[204:207], v[24:27]\n"
"s_add_u32 s83, s92, 12288\n"
"s_waitcnt lgkmcnt(10)\n"
"v_mfma_f32_16x16x32_bf16 v[28:31], a[124:127], v[204:207], v[218:221]\n"
"s_waitcnt lgkmcnt(0)\n"
"v_exp_f32 v227, v227\n"
"v_mfma_f32_16x16x32_bf16 v[28:31], a[120:123], v[200:203], v[28:31]\n"
"s_cmp_eq_u32 s69, 3\n"
"v_perm_b32 v213, v227, v226, s72\n"
"v_mfma_f32_16x16x32_bf16 v[64:67], a[148:151], a[216:219], 0\n"
"s_cselect_b32 s83, s63, s83\n"
"v_mfma_f32_16x16x32_bf16 v[68:71], a[148:151], a[220:223], 0\n"
"s_cselect_b32 s63, s71, 0\n"
"v_mfma_f32_16x16x32_bf16 v[72:75], a[148:151], a[224:227], 0\n"
"Lhop0_0_%=:\n s_cbranch_execz Lhop0_4_%=\n"
"v_mfma_f32_16x16x32_bf16 v[76:79], a[148:151], a[228:231], 0\n"
"s_lshl_b32 s63, s63, 7\n"
"s_nop 1\n"
"v_cvt_pk_bf16_f32 v160, v64, v65\n"
"v_cvt_pk_bf16_f32 v161, v66, v67\n"
"v_mfma_f32_16x16x32_bf16 v[80:83], a[148:151], a[232:235], 0\n"
"s_add_u32 s93, s83, s63\n"
"v_cvt_pk_bf16_f32 v162, v68, v69\n"
"v_cvt_pk_bf16_f32 v163, v70, v71\n"
"v_mfma_f32_16x16x32_bf16 v[84:87], a[148:151], a[236:239], 0\n"
"s_mul_i32 s75, s71, 8768\n"
"v_cvt_pk_bf16_f32 v164, v72, v73\n"
"v_cvt_pk_bf16_f32 v165, v74, v75\n"
"v_mfma_f32_16x16x32_bf16 v[88:91], a[148:151], a[240:243], 0\n"
"v_cvt_pk_bf16_f32 v166, v76, v77\n"
"v_cvt_pk_bf16_f32 v167, v78, v79\n"
"v_mfma_f32_16x16x32_bf16 v[92:95], a[148:151], a[244:247], 0\n"
"s_sub_u32 s75, 4384, s75\n"
"v_cvt_pk_bf16_f32 v168, v80, v81\n"
"v_cvt_pk_bf16_f32 v169, v82, v83\n"
"v_cvt_pk_bf16_f32 v170, v84, v85\n"
"v_cvt_pk_bf16_f32 v171, v86, v87\n"
"v_cvt_pk_bf16_f32 v172, v88, v89\n"
"v_cvt_pk_bf16_f32 v173, v90, v91\n"
"s_nop 0\n"
"v_cvt_pk_bf16_f32 v174, v92, v93\n"
"v_cvt_pk_bf16_f32 v175, v94, v95\n"
"v_mfma_f32_16x16x32_bf16 v[192:195], a[0:3], v[160:163], -4.0\n"
"s_add_u32 s70, s69, 1\n"
"v_exp_f32 v228, v228\n"
"v_mfma_f32_16x16x32_bf16 v[192:195], a[4:7], v[164:167], v[192:195]\n"
"s_mov_b32 s56, 0\n"
"v_mfma_f32_16x16x32_bf16 v[192:195], a[8:11], v[168:171], v[192:195]\n"
"v_exp_f32 v229, v229\n"
"v_mfma_f32_16x16x32_bf16 v[192:195], a[12:15], v[172:175], v[192:195]\n"
"s_mov_b32 s57, 0\n"
"v_perm_b32 v214, v229, v228, s72\n"
"v_mfma_f32_16x16x32_bf16 v[196:199], a[28:31], v[172:175], -4.0\n"
"s_mov_b32 s58, 0x57800000\n"
"v_exp_f32 v230, v230\n"
"v_mfma_f32_16x16x32_bf16 v[196:199], a[24:27], v[168:171], v[196:199]\n"
"s_mov_b32 s84, 0\n"
"v_mfma_f32_16x16x32_bf16 v[196:199], a[20:23], v[164:167], v[196:199]\n"
"v_exp_f32 v231, v231\n"
"v_mfma_f32_16x16x32_bf16 v[196:199], a[16:19], v[160:163], v[196:199]\n"
"s_mov_b32 s85, 0\n"
"v_perm_b32 v215, v231, v230, s72\n"
"v_mfma_f32_16x16x32_bf16 v[224:227], a[32:35], v[160:163], -4.0\n"
"s_and_b32 s49, s49, 0xffff\n"
"v_exp_f32 v192, v192\n"
"v_mfma_f32_16x16x32_bf16 v[224:227], a[36:39], v[164:167], v[224:227]\n"
"s_add_u32 s52, s64, 32768\n"
"v_mfma_f32_16x16x32_bf16 v[224:227], a[40:43], v[168:171], v[224:227]\n"
"v_exp_f32 v193, v193\n"
"v_mfma_f32_16x16x32_bf16 v[224:227], a[44:47], v[172:175], v[224:227]\n"
"s_mov_b64 s[76:77], s[78:79]\n"
"v_perm_b32 v200, v193, v192, s72\n"
"v_mfma_f32_16x16x32_bf16 v[228:231], a[60:63], v[172:175], -4.0\n"
"s_mov_b64 s[78:79], s[80:81]\n"
"v_exp_f32 v194, v194\n"
"v_mfma_f32_16x16x32_bf16 v[228:231], a[56:59], v[168:171], v[228:231]\n"
"s_mov_b64 s[80:81], s[82:83]\n"
"v_mfma_f32_16x16x32_bf16 v[228:231], a[52:55], v[164:167], v[228:231]\n"
"v_exp_f32 v195, v195\n"
"v_mfma_f32_16x16x32_bf16 v[228:231], a[48:51], v[160:163], v[228:231]\n"
"v_perm_b32 v201, v195, v194, s72\n"
"v_smfmac_f32_16x16x64_bf16 a[132:135], a[144:147], v[208:215], v251\n"
"v_mfma_f32_16x16x32_bf16 v[32:35], a[64:67], v[208:211], v[218:221]\n"
"v_exp_f32 v196, v196\n"
"v_mfma_f32_16x16x32_bf16 v[32:35], a[68:71], v[212:215], v[32:35]\n"
"v_mfma_f32_16x16x32_bf16 v[36:39], a[76:79], v[212:215], v[218:221]\n"
"v_exp_f32 v197, v197\n"
"v_mfma_f32_16x16x32_bf16 v[36:39], a[72:75], v[208:211], v[36:39]\n"
"v_perm_b32 v202, v197, v196, s72\n"
"v_mfma_f32_16x16x32_bf16 v[40:43], a[80:83], v[208:211], v[218:221]\n"
"v_exp_f32 v198, v198\n"
"v_mfma_f32_16x16x32_bf16 v[40:43], a[84:87], v[212:215], v[40:43]\n"
"v_mfma_f32_16x16x32_bf16 v[44:47], a[92:95], v[212:215], v[218:221]\n"
"v_exp_f32 v199, v199\n"
"v_mfma_f32_16x16x32_bf16 v[44:47], a[88:91], v[208:211], v[44:47]\n"
"v_perm_b32 v203, v199, v198, s72\n"
"s_add_u32 m0, s53, 34816\n"
"s_nop 0\n"
"buffer_load_dwordx4 v247, s[36:39], s66 offen offset:0 lds\n"
"s_add_u32 m0, s53, 34880\n"
"s_nop 0\n"
"buffer_load_dwordx4 v247, s[36:39], s66 offen offset:1024 lds\n"
"s_add_u32 m0, s53, 34944\n"
"s_nop 0\n"
"buffer_load_dwordx4 v247, s[36:39], s66 offen offset:2048 lds\n"
"s_add_u32 m0, s53, 35008\n"
"s_nop 0\n"
"buffer_load_dwordx4 v247, s[36:39], s66 offen offset:3072 lds\n"
"s_waitcnt vmcnt(8)\n"
"ds_read2_b64 a[152:155], v234 offset0:0 offset1:2\n"
"ds_read2_b64 a[156:159], v234 offset0:1 offset1:3\n"
"ds_read2_b64 a[160:163], v234 offset0:32 offset1:34\n"
"ds_read2_b64 a[164:167], v234 offset0:33 offset1:35\n"
"ds_read2_b64 a[168:171], v234 offset0:64 offset1:66\n"
"ds_read2_b64 a[172:175], v234 offset0:65 offset1:67\n"
"ds_read2_b64 a[176:179], v234 offset0:96 offset1:98\n"
"ds_read2_b64 a[180:183], v234 offset0:97 offset1:99\n"
"v_mfma_f32_16x16x32_bf16 v[48:51], a[96:99], v[208:211], v[218:221]\n"
"v_exp_f32 v224, v224\n"
"v_mfma_f32_16x16x32_bf16 v[48:51], a[100:103], v[212:215], v[48:51]\n"
"v_mfma_f32_16x16x32_bf16 v[52:55], a[108:111], v[212:215], v[218:221]\n"
"v_exp_f32 v225, v225\n"
"v_mfma_f32_16x16x32_bf16 v[52:55], a[104:107], v[208:211], v[52:55]\n"
"s_waitcnt vmcnt(12) lgkmcnt(8)\n"
"v_perm_b32 v204, v225, v224, s72\n"
"v_mfma_f32_16x16x32_bf16 v[56:59], a[112:115], v[208:211], v[218:221]\n"
"v_exp_f32 v226, v226\n"
"v_mfma_f32_16x16x32_bf16 v[56:59], a[116:119], v[212:215], v[56:59]\n"
"v_mfma_f32_16x16x32_bf16 v[60:63], a[124:127], v[212:215], v[218:221]\n"
"s_waitcnt lgkmcnt(7)\n"
"v_exp_f32 v227, v227\n"
"v_mfma_f32_16x16x32_bf16 v[60:63], a[120:123], v[208:211], v[60:63]\n"
"s_barrier\n"
"v_perm_b32 v205, v227, v226, s72\n"
"v_mfma_f32_16x16x32_bf16 v[96:99], a[148:151], a[152:155], 0\n"
"s_waitcnt lgkmcnt(6)\n"
"v_mfma_f32_16x16x32_bf16 v[100:103], a[148:151], a[156:159], 0\n"
"s_waitcnt lgkmcnt(5)\n"
"v_mfma_f32_16x16x32_bf16 v[104:107], a[148:151], a[160:163], 0\n"
"s_waitcnt lgkmcnt(0)\n"
"v_mfma_f32_16x16x32_bf16 v[108:111], a[148:151], a[164:167], 0\n"
"s_nop 1\n"
"v_cvt_pk_bf16_f32 v176, v96, v97\n"
"v_cvt_pk_bf16_f32 v177, v98, v99\n"
"v_mfma_f32_16x16x32_bf16 v[112:115], a[148:151], a[168:171], 0\n"
"v_cvt_pk_bf16_f32 v178, v100, v101\n"
"v_cvt_pk_bf16_f32 v179, v102, v103\n"
"v_mfma_f32_16x16x32_bf16 v[116:119], a[148:151], a[172:175], 0\n"
"v_cvt_pk_bf16_f32 v180, v104, v105\n"
"v_cvt_pk_bf16_f32 v181, v106, v107\n"
"v_mfma_f32_16x16x32_bf16 v[120:123], a[148:151], a[176:179], 0\n"
"v_cvt_pk_bf16_f32 v182, v108, v109\n"
"v_cvt_pk_bf16_f32 v183, v110, v111\n"
"v_mfma_f32_16x16x32_bf16 v[124:127], a[148:151], a[180:183], 0\n"
"v_cvt_pk_bf16_f32 v184, v112, v113\n"
"v_cvt_pk_bf16_f32 v185, v114, v115\n"
"v_cvt_pk_bf16_f32 v186, v116, v117\n"
"v_cvt_pk_bf16_f32 v187, v118, v119\n"
"s_nop 0\n"
"v_cvt_pk_bf16_f32 v188, v120, v121\n"
"v_cvt_pk_bf16_f32 v189, v122, v123\n"
"s_nop 0\n"
"v_cvt_pk_bf16_f32 v190, v124, v125\n"
"v_cvt_pk_bf16_f32 v191, v126, v127\n"
"s_cmp_eq_u32 s69, 0\n"
"s_waitcnt lgkmcnt(0)\n"
"s_cbranch_scc1 LHS%=\n"
"v_mfma_f32_16x16x32_bf16 v[192:195], a[0:3], v[176:179], -4.0\n"
"v_exp_f32 v228, v228\n"
"v_mfma_f32_16x16x32_bf16 v[192:195], a[4:7], v[180:183], v[192:195]\n"
"ds_read_b128 a[0:3], v244 offset:17408\n"
"ds_read_b128 a[4:7], v244 offset:17472\n"
"v_mfma_f32_16x16x32_bf16 v[192:195], a[8:11], v[184:187], v[192:195]\n"
"v_exp_f32 v229, v229\n"
"v_mfma_f32_16x16x32_bf16 v[192:195], a[12:15], v[188:191], v[192:195]\n"
"ds_read_b128 a[8:11], v244 offset:17536\n"
"v_perm_b32 v206, v229, v228, s72\n"
"v_mfma_f32_16x16x32_bf16 v[196:199], a[28:31], v[188:191], -4.0\n"
"s_add_u32 m0, s54, 33024\n"
"v_exp_f32 v230, v230\n"
"v_mfma_f32_16x16x32_bf16 v[196:199], a[24:27], v[184:187], v[196:199]\n"
"buffer_load_dwordx4 v248, s[40:43], s52 offen offset:0 lds\n"
"v_mfma_f32_16x16x32_bf16 v[196:199], a[20:23], v[180:183], v[196:199]\n"
"v_exp_f32 v231, v231\n"
"v_mfma_f32_16x16x32_bf16 v[196:199], a[16:19], v[176:179], v[196:199]\n"
"ds_read_b128 a[12:15], v244 offset:17600\n"
"v_perm_b32 v207, v231, v230, s72\n"
"v_mfma_f32_16x16x32_bf16 v[224:227], a[32:35], v[176:179], -4.0\n"
"v_exp_f32 v192, v192\n"
"v_mfma_f32_16x16x32_bf16 v[224:227], a[36:39], v[180:183], v[224:227]\n"
"ds_read_b128 a[28:31], v244 offset:21952\n"
"ds_read_b128 a[24:27], v244 offset:21888\n"
"v_mfma_f32_16x16x32_bf16 v[224:227], a[40:43], v[184:187], v[224:227]\n"
"v_exp_f32 v193, v193\n"
"v_mfma_f32_16x16x32_bf16 v[224:227], a[44:47], v[188:191], v[224:227]\n"
"ds_read_b128 a[20:23], v244 offset:21824\n"
"v_perm_b32 v208, v193, v192, s72\n"
"v_mfma_f32_16x16x32_bf16 v[228:231], a[60:63], v[188:191], -4.0\n"
"s_add_u32 m0, s54, 33032\n"
"v_exp_f32 v194, v194\n"
"v_mfma_f32_16x16x32_bf16 v[228:231], a[56:59], v[184:187], v[228:231]\n"
"buffer_load_dwordx4 v248, s[40:43], s52 offen offset:1024 lds\n"
"v_mfma_f32_16x16x32_bf16 v[228:231], a[52:55], v[180:183], v[228:231]\n"
"v_exp_f32 v195, v195\n"
"v_mfma_f32_16x16x32_bf16 v[228:231], a[48:51], v[176:179], v[228:231]\n"
"ds_read_b128 a[16:19], v244 offset:21760\n"
"v_perm_b32 v209, v195, v194, s72\n"
"v_smfmac_f32_16x16x64_bf16 a[136:139], a[144:147], v[200:207], v251\n"
"ds_read_b128 a[32:35], v222 offset:17408\n"
"v_mfma_f32_16x16x32_bf16 v[64:67], a[64:67], v[200:203], v[218:221]\n"
"v_exp_f32 v196, v196\n"
"v_mfma_f32_16x16x32_bf16 v[64:67], a[68:71], v[204:207], v[64:67]\n"
"ds_read_b128 a[36:39], v222 offset:17472\n"
"ds_read_b128 a[40:43], v222 offset:17536\n"
"v_mfma_f32_16x16x32_bf16 v[68:71], a[76:79], v[204:207], v[218:221]\n"
"v_exp_f32 v197, v197\n"
"v_mfma_f32_16x16x32_bf16 v[68:71], a[72:75], v[200:203], v[68:71]\n"
"ds_read_b128 a[44:47], v222 offset:17600\n"
"v_perm_b32 v210, v197, v196, s72\n"
"v_mfma_f32_16x16x32_bf16 v[72:75], a[80:83], v[200:203], v[218:221]\n"
"s_add_u32 m0, s54, 34960\n"
"v_exp_f32 v198, v198\n"
"v_mfma_f32_16x16x32_bf16 v[72:75], a[84:87], v[204:207], v[72:75]\n"
"buffer_load_dwordx4 v248, s[40:43], s52 offen offset:128 lds\n"
"v_mfma_f32_16x16x32_bf16 v[76:79], a[92:95], v[204:207], v[218:221]\n"
"v_exp_f32 v199, v199\n"
"v_mfma_f32_16x16x32_bf16 v[76:79], a[88:91], v[200:203], v[76:79]\n"
"ds_read_b128 a[60:63], v222 offset:21952\n"
"v_perm_b32 v211, v199, v198, s72\n"
"v_mfma_f32_16x16x32_bf16 v[80:83], a[96:99], v[200:203], v[218:221]\n"
"v_exp_f32 v224, v224\n"
"v_mfma_f32_16x16x32_bf16 v[80:83], a[100:103], v[204:207], v[80:83]\n"
"ds_read_b128 a[56:59], v222 offset:21888\n"
"ds_read_b128 a[52:55], v222 offset:21824\n"
"v_mfma_f32_16x16x32_bf16 v[84:87], a[108:111], v[204:207], v[218:221]\n"
"v_exp_f32 v225, v225\n"
"v_mfma_f32_16x16x32_bf16 v[84:87], a[104:107], v[200:203], v[84:87]\n"
"ds_read_b128 a[48:51], v222 offset:21760\n"
"v_perm_b32 v212, v225, v224, s72\n"
"v_mfma_f32_16x16x32_bf16 v[88:91], a[112:115], v[200:203], v[218:221]\n"
"s_add_u32 m0, s54, 34968\n"
"v_exp_f32 v226, v226\n"
"v_mfma_f32_16x16x32_bf16 v[88:91], a[116:119], v[204:207], v[88:91]\n"
"buffer_load_dwordx4 v248, s[40:43], s52 offen offset:1152 lds\n"
"v_mfma_f32_16x16x32_bf16 v[92:95], a[124:127], v[204:207], v[218:221]\n"
"s_add_u32 s52, s52, 16384\n"
"v_exp_f32 v227, v227\n"
"v_mfma_f32_16x16x32_bf16 v[92:95], a[120:123], v[200:203], v[92:95]\n"
"s_waitcnt lgkmcnt(4)\n"
"v_perm_b32 v213, v227, v226, s72\n"
"s_getreg_b32 s73, hwreg(HW_REG_XCC_ID, 0, 3)\n s_lshl_b32 s73, s73, 2\n s_lshr_b32 s73, 0x9050905, s73\n s_and_b32 s73, s73, 15\n s_cbranch_scc0 Lgoxt3N%=\n Lslxt3N%=:\n s_sleep 4\n s_sub_u32 s73, s73, 1\n s_cmp_lg_u32 s73, 0\n s_cbranch_scc1 Lslxt3N%=\n Lgoxt3N%=:\n"
"v_mfma_f32_16x16x32_bf16 v[192:195], a[0:3], v[128:131], -4.0\n"
"v_exp_f32 v228, v228\n"
"v_mfma_f32_16x16x32_bf16 v[192:195], a[4:7], v[132:135], v[192:195]\n"
"v_mfma_f32_16x16x32_bf16 v[192:195], a[8:11], v[136:139], v[192:195]\n"
"v_exp_f32 v229, v229\n"
"v_mfma_f32_16x16x32_bf16 v[192:195], a[12:15], v[140:143], v[192:195]\n"
"s_waitcnt vmcnt(8)\n"
"v_perm_b32 v214, v229, v228, s72\n"
"v_mfma_f32_16x16x32_bf16 v[196:199], a[28:31], v[140:143], -4.0\n"
"s_add_u32 m0, s55, 0\n"
"v_exp_f32 v230, v230\n"
"v_mfma_f32_16x16x32_bf16 v[196:199], a[24:27], v[136:139], v[196:199]\n"
"s_waitcnt lgkmcnt(14)\n"
"buffer_load_dwordx4 v249, s[44:47], s76 offen offset:0 nt lds\n"
"v_mfma_f32_16x16x32_bf16 v[196:199], a[20:23], v[132:135], v[196:199]\n"
"v_exp_f32 v231, v231\n"
"v_mfma_f32_16x16x32_bf16 v[196:199], a[16:19], v[128:131], v[196:199]\n"
"s_barrier\n"
"v_perm_b32 v215, v231, v230, s72\n"
"v_mfma_f32_16x16x32_bf16 v[224:227], a[32:35], v[128:131], -4.0\n"
"v_exp_f32 v192, v192\n"
"v_mfma_f32_16x16x32_bf16 v[224:227], a[36:39], v[132:135], v[224:227]\n"
"v_mfma_f32_16x16x32_bf16 v[224:227], a[40:43], v[136:139], v[224:227]\n"
"v_exp_f32 v193, v193\n"
"v_mfma_f32_16x16x32_bf16 v[224:227], a[44:47], v[140:143], v[224:227]\n"
"s_waitcnt lgkmcnt(0)\n"
"v_perm_b32 v200, v193, v192, s72\n"
"v_mfma_f32_16x16x32_bf16 v[228:231], a[60:63], v[140:143], -4.0\n"
"s_add_u32 m0, s55, 8\n"
"v_exp_f32 v194, v194\n"
"v_mfma_f32_16x16x32_bf16 v[228:231], a[56:59], v[136:139], v[228:231]\n"
"buffer_load_dwordx4 v249, s[44:47], s76 offen offset:1024 nt lds\n"
"v_mfma_f32_16x16x32_bf16 v[228:231], a[52:55], v[132:135], v[228:231]\n"
"v_exp_f32 v195, v195\n"
"v_mfma_f32_16x16x32_bf16 v[228:231], a[48:51], v[128:131], v[228:231]\n"
"v_perm_b32 v201, v195, v194, s72\n"
"v_smfmac_f32_16x16x64_bf16 a[140:143], a[144:147], v[208:215], v251\n"
"v_mfma_f32_16x16x32_bf16 v[96:99], a[64:67], v[208:211], v[218:221]\n"
"v_exp_f32 v196, v196\n"
"v_mfma_f32_16x16x32_bf16 v[96:99], a[68:71], v[212:215], v[96:99]\n"
"ds_read_b64_tr_b16 a[64:65], v245 offset:16512\n"
"ds_read_b64_tr_b16 a[66:67], v245 offset:20640\n"
"v_mfma_f32_16x16x32_bf16 v[100:103], a[76:79], v[212:215], v[218:221]\n"
"v_exp_f32 v197, v197\n"
"v_mfma_f32_16x16x32_bf16 v[100:103], a[72:75], v[208:211], v[100:103]\n"
"ds_read_b64_tr_b16 a[68:69], v255 offset:16512\n"
"v_perm_b32 v202, v197, v196, s72\n"
"v_mfma_f32_16x16x32_bf16 v[104:107], a[80:83], v[208:211], v[218:221]\n"
"s_add_u32 m0, s55, 16\n"
"v_exp_f32 v198, v198\n"
"v_mfma_f32_16x16x32_bf16 v[104:107], a[84:87], v[212:215], v[104:107]\n"
"buffer_load_dwordx4 v249, s[44:47], s76 offen offset:2048 nt lds\n"
"v_mfma_f32_16x16x32_bf16 v[108:111], a[92:95], v[212:215], v[218:221]\n"
"v_exp_f32 v199, v199\n"
"v_mfma_f32_16x16x32_bf16 v[108:111], a[88:91], v[208:211], v[108:111]\n"
"ds_read_b64_tr_b16 a[70:71], v255 offset:20640\n"
"v_perm_b32 v203, v199, v198, s72\n"
"v_mfma_f32_16x16x32_bf16 v[112:115], a[96:99], v[208:211], v[218:221]\n"
"v_exp_f32 v224, v224\n"
"v_mfma_f32_16x16x32_bf16 v[112:115], a[100:103], v[212:215], v[112:115]\n"
"ds_read_b64_tr_b16 a[76:77], v255 offset:16520\n"
"ds_read_b64_tr_b16 a[78:79], v255 offset:20648\n"
"v_mfma_f32_16x16x32_bf16 v[116:119], a[108:111], v[212:215], v[218:221]\n"
"v_exp_f32 v225, v225\n"
"v_mfma_f32_16x16x32_bf16 v[116:119], a[104:107], v[208:211], v[116:119]\n"
"ds_read_b64_tr_b16 a[72:73], v245 offset:16520\n"
"v_perm_b32 v204, v225, v224, s72\n"
"v_mfma_f32_16x16x32_bf16 v[120:123], a[112:115], v[208:211], v[218:221]\n"
"s_add_u32 m0, s55, 24\n"
"v_exp_f32 v226, v226\n"
"v_mfma_f32_16x16x32_bf16 v[120:123], a[116:119], v[212:215], v[120:123]\n"
"buffer_load_dwordx4 v249, s[44:47], s76 offen offset:3072 nt lds\n"
"v_mfma_f32_16x16x32_bf16 v[124:127], a[124:127], v[212:215], v[218:221]\n"
"s_add_u32 s76, s76, 4096\n"
"v_exp_f32 v227, v227\n"
"v_mfma_f32_16x16x32_bf16 v[124:127], a[120:123], v[208:211], v[124:127]\n"
"ds_read_b64_tr_b16 a[74:75], v245 offset:20648\n"
"v_perm_b32 v205, v227, v226, s72\n"
"v_mfma_f32_16x16x32_bf16 v[192:195], a[0:3], v[144:147], -4.0\n"
"v_exp_f32 v228, v228\n"
"v_mfma_f32_16x16x32_bf16 v[192:195], a[4:7], v[148:151], v[192:195]\n"
"ds_read_b64_tr_b16 a[80:81], v245 offset:16768\n"
"ds_read_b64_tr_b16 a[82:83], v245 offset:20896\n"
"v_mfma_f32_16x16x32_bf16 v[192:195], a[8:11], v[152:155], v[192:195]\n"
"v_exp_f32 v229, v229\n"
"v_mfma_f32_16x16x32_bf16 v[192:195], a[12:15], v[156:159], v[192:195]\n"
"ds_read_b64_tr_b16 a[84:85], v255 offset:16768\n"
"v_perm_b32 v206, v229, v228, s72\n"
"v_mfma_f32_16x16x32_bf16 v[196:199], a[28:31], v[156:159], -4.0\n"
"s_add_u32 m0, s53, 52224\n"
"v_exp_f32 v230, v230\n"
"v_mfma_f32_16x16x32_bf16 v[196:199], a[24:27], v[152:155], v[196:199]\n"
"buffer_load_dwordx4 v247, s[36:39], s52 offen offset:0 lds\n"
"v_mfma_f32_16x16x32_bf16 v[196:199], a[20:23], v[148:151], v[196:199]\n"
"v_exp_f32 v231, v231\n"
"v_mfma_f32_16x16x32_bf16 v[196:199], a[16:19], v[144:147], v[196:199]\n"
"ds_read_b64_tr_b16 a[86:87], v255 offset:20896\n"
"v_perm_b32 v207, v231, v230, s72\n"
"v_mfma_f32_16x16x32_bf16 v[224:227], a[32:35], v[144:147], -4.0\n"
"v_exp_f32 v192, v192\n"
"v_mfma_f32_16x16x32_bf16 v[224:227], a[36:39], v[148:151], v[224:227]\n"
"ds_read_b64_tr_b16 a[92:93], v255 offset:16776\n"
"ds_read_b64_tr_b16 a[94:95], v255 offset:20904\n"
"v_mfma_f32_16x16x32_bf16 v[224:227], a[40:43], v[152:155], v[224:227]\n"
"v_exp_f32 v193, v193\n"
"v_mfma_f32_16x16x32_bf16 v[224:227], a[44:47], v[156:159], v[224:227]\n"
"ds_read_b64_tr_b16 a[88:89], v245 offset:16776\n"
"v_perm_b32 v208, v193, v192, s72\n"
"v_mfma_f32_16x16x32_bf16 v[228:231], a[60:63], v[156:159], -4.0\n"
"s_add_u32 m0, s53, 52288\n"
"v_exp_f32 v194, v194\n"
"v_mfma_f32_16x16x32_bf16 v[228:231], a[56:59], v[152:155], v[228:231]\n"
"buffer_load_dwordx4 v247, s[36:39], s52 offen offset:1024 lds\n"
"v_mfma_f32_16x16x32_bf16 v[228:231], a[52:55], v[148:151], v[228:231]\n"
"v_exp_f32 v195, v195\n"
"v_mfma_f32_16x16x32_bf16 v[228:231], a[48:51], v[144:147], v[228:231]\n"
"ds_read_b64_tr_b16 a[90:91], v245 offset:20904\n"
"v_perm_b32 v209, v195, v194, s72\n"
"v_smfmac_f32_16x16x64_bf16 a[128:131], a[144:147], v[200:207], v251\n"
"s_waitcnt lgkmcnt(5)\n"
"ds_read_b64_tr_b16 a[96:97], v245 offset:18576\n"
"v_mfma_f32_16x16x32_bf16 v[0:3], a[64:67], v[200:203], v[0:3]\n"
"v_exp_f32 v196, v196\n"
"v_mfma_f32_16x16x32_bf16 v[0:3], a[68:71], v[204:207], v[0:3]\n"
"ds_read_b64_tr_b16 a[98:99], v245 offset:22704\n"
"ds_read_b64_tr_b16 a[100:101], v255 offset:18576\n"
"v_mfma_f32_16x16x32_bf16 v[4:7], a[76:79], v[204:207], v[4:7]\n"
"v_exp_f32 v197, v197\n"
"v_mfma_f32_16x16x32_bf16 v[4:7], a[72:75], v[200:203], v[4:7]\n"
"s_waitcnt lgkmcnt(5)\n"
"ds_read_b64_tr_b16 a[102:103], v255 offset:22704\n"
"ds_read_b64_tr_b16 a[108:109], v255 offset:18584\n"
"ds_read_b64_tr_b16 a[110:111], v255 offset:22712\n"
"v_perm_b32 v210, v197, v196, s72\n"
"v_mfma_f32_16x16x32_bf16 v[8:11], a[80:83], v[200:203], v[8:11]\n"
"s_add_u32 m0, s53, 52352\n"
"v_exp_f32 v198, v198\n"
"v_mfma_f32_16x16x32_bf16 v[8:11], a[84:87], v[204:207], v[8:11]\n"
"buffer_load_dwordx4 v247, s[36:39], s52 offen offset:2048 lds\n"
"v_mfma_f32_16x16x32_bf16 v[12:15], a[92:95], v[204:207], v[12:15]\n"
"s_waitcnt lgkmcnt(6)\n"
"v_exp_f32 v199, v199\n"
"v_mfma_f32_16x16x32_bf16 v[12:15], a[88:91], v[200:203], v[12:15]\n"
"s_waitcnt lgkmcnt(4)\n"
"ds_read_b64_tr_b16 a[104:105], v245 offset:18584\n"
"ds_read_b64_tr_b16 a[106:107], v245 offset:22712\n"
"ds_read_b64_tr_b16 a[112:113], v245 offset:18832\n"
"ds_read_b64_tr_b16 a[114:115], v245 offset:22960\n"
"ds_read_b64_tr_b16 a[116:117], v255 offset:18832\n"
"ds_read_b64_tr_b16 a[118:119], v255 offset:22960\n"
"ds_read_b64_tr_b16 a[124:125], v255 offset:18840\n"
"ds_read_b64_tr_b16 a[126:127], v255 offset:22968\n"
"v_perm_b32 v211, v199, v198, s72\n"
"v_mfma_f32_16x16x32_bf16 v[16:19], a[96:99], v[200:203], v[16:19]\n"
"s_waitcnt lgkmcnt(10)\n"
"v_exp_f32 v224, v224\n"
"v_mfma_f32_16x16x32_bf16 v[16:19], a[100:103], v[204:207], v[16:19]\n"
"s_waitcnt lgkmcnt(8)\n"
"ds_read_b64_tr_b16 a[120:121], v245 offset:18840\n"
"ds_read_b64_tr_b16 a[122:123], v245 offset:22968\n"
"v_mfma_f32_16x16x32_bf16 v[20:23], a[108:111], v[204:207], v[20:23]\n"
"s_waitcnt lgkmcnt(8)\n"
"v_exp_f32 v225, v225\n"
"v_mfma_f32_16x16x32_bf16 v[20:23], a[104:107], v[200:203], v[20:23]\n"
"s_waitcnt lgkmcnt(4)\n"
"v_perm_b32 v212, v225, v224, s72\n"
"v_mfma_f32_16x16x32_bf16 v[24:27], a[112:115], v[200:203], v[24:27]\n"
"s_add_u32 m0, s53, 52416\n"
"v_exp_f32 v226, v226\n"
"v_mfma_f32_16x16x32_bf16 v[24:27], a[116:119], v[204:207], v[24:27]\n"
"s_waitcnt lgkmcnt(2)\n"
"buffer_load_dwordx4 v247, s[36:39], s52 offen offset:3072 lds\n"
"v_mfma_f32_16x16x32_bf16 v[28:31], a[124:127], v[204:207], v[28:31]\n"
"s_waitcnt lgkmcnt(0)\n"
"v_exp_f32 v227, v227\n"
"v_mfma_f32_16x16x32_bf16 v[28:31], a[120:123], v[200:203], v[28:31]\n"
"v_perm_b32 v213, v227, v226, s72\n"
"v_mfma_f32_16x16x32_bf16 v[192:195], a[0:3], v[160:163], -4.0\n"
"v_exp_f32 v228, v228\n"
"v_mfma_f32_16x16x32_bf16 v[192:195], a[4:7], v[164:167], v[192:195]\n"
"v_mfma_f32_16x16x32_bf16 v[192:195], a[8:11], v[168:171], v[192:195]\n"
"v_exp_f32 v229, v229\n"
"v_mfma_f32_16x16x32_bf16 v[192:195], a[12:15], v[172:175], v[192:195]\n"
"v_perm_b32 v214, v229, v228, s72\n"
"v_mfma_f32_16x16x32_bf16 v[196:199], a[28:31], v[172:175], -4.0\n"
"s_add_u32 m0, s54, 49536\n"
"v_exp_f32 v230, v230\n"
"v_mfma_f32_16x16x32_bf16 v[196:199], a[24:27], v[168:171], v[196:199]\n"
"buffer_load_dwordx4 v248, s[40:43], s52 offen offset:0 lds\n"
"v_mfma_f32_16x16x32_bf16 v[196:199], a[20:23], v[164:167], v[196:199]\n"
"v_exp_f32 v231, v231\n"
"v_mfma_f32_16x16x32_bf16 v[196:199], a[16:19], v[160:163], v[196:199]\n"
"v_perm_b32 v215, v231, v230, s72\n"
"v_mfma_f32_16x16x32_bf16 v[224:227], a[32:35], v[160:163], -4.0\n"
"v_exp_f32 v192, v192\n"
"v_mfma_f32_16x16x32_bf16 v[224:227], a[36:39], v[164:167], v[224:227]\n"
"v_mfma_f32_16x16x32_bf16 v[224:227], a[40:43], v[168:171], v[224:227]\n"
"v_exp_f32 v193, v193\n"
"v_mfma_f32_16x16x32_bf16 v[224:227], a[44:47], v[172:175], v[224:227]\n"
"v_perm_b32 v200, v193, v192, s72\n"
"v_mfma_f32_16x16x32_bf16 v[228:231], a[60:63], v[172:175], -4.0\n"
"s_add_u32 m0, s54, 49544\n"
"v_exp_f32 v194, v194\n"
"v_mfma_f32_16x16x32_bf16 v[228:231], a[56:59], v[168:171], v[228:231]\n"
"buffer_load_dwordx4 v248, s[40:43], s52 offen offset:1024 lds\n"
"v_mfma_f32_16x16x32_bf16 v[228:231], a[52:55], v[164:167], v[228:231]\n"
"v_exp_f32 v195, v195\n"
"v_mfma_f32_16x16x32_bf16 v[228:231], a[48:51], v[160:163], v[228:231]\n"
"s_waitcnt vmcnt(10) lgkmcnt(0)\n"
"v_perm_b32 v201, v195, v194, s72\n"
"v_smfmac_f32_16x16x64_bf16 a[132:135], a[144:147], v[208:215], v251\n"
"v_mfma_f32_16x16x32_bf16 v[32:35], a[64:67], v[208:211], v[32:35]\n"
"v_exp_f32 v196, v196\n"
"v_mfma_f32_16x16x32_bf16 v[32:35], a[68:71], v[212:215], v[32:35]\n"
"s_barrier\n"
"v_mfma_f32_16x16x32_bf16 v[36:39], a[76:79], v[212:215], v[36:39]\n"
"v_exp_f32 v197, v197\n"
"v_mfma_f32_16x16x32_bf16 v[36:39], a[72:75], v[208:211], v[36:39]\n"
"v_perm_b32 v202, v197, v196, s72\n"
"v_mfma_f32_16x16x32_bf16 v[40:43], a[80:83], v[208:211], v[40:43]\n"
"s_add_u32 m0, s54, 51472\n"
"v_exp_f32 v198, v198\n"
"v_mfma_f32_16x16x32_bf16 v[40:43], a[84:87], v[212:215], v[40:43]\n"
"buffer_load_dwordx4 v248, s[40:43], s52 offen offset:128 lds\n"
"v_mfma_f32_16x16x32_bf16 v[44:47], a[92:95], v[212:215], v[44:47]\n"
"v_exp_f32 v199, v199\n"
"v_mfma_f32_16x16x32_bf16 v[44:47], a[88:91], v[208:211], v[44:47]\n"
"v_perm_b32 v203, v199, v198, s72\n"
"v_mfma_f32_16x16x32_bf16 v[48:51], a[96:99], v[208:211], v[48:51]\n"
"v_exp_f32 v224, v224\n"
"v_mfma_f32_16x16x32_bf16 v[48:51], a[100:103], v[212:215], v[48:51]\n"
"v_mfma_f32_16x16x32_bf16 v[52:55], a[108:111], v[212:215], v[52:55]\n"
"v_exp_f32 v225, v225\n"
"v_mfma_f32_16x16x32_bf16 v[52:55], a[104:107], v[208:211], v[52:55]\n"
"v_perm_b32 v204, v225, v224, s72\n"
"v_mfma_f32_16x16x32_bf16 v[56:59], a[112:115], v[208:211], v[56:59]\n"
"s_add_u32 m0, s54, 51480\n"
"v_exp_f32 v226, v226\n"
"v_mfma_f32_16x16x32_bf16 v[56:59], a[116:119], v[212:215], v[56:59]\n"
"buffer_load_dwordx4 v248, s[40:43], s52 offen offset:1152 lds\n"
"v_mfma_f32_16x16x32_bf16 v[60:63], a[124:127], v[212:215], v[60:63]\n"
"s_add_u32 s52, s52, 16384\n"
"v_exp_f32 v227, v227\n"
"v_mfma_f32_16x16x32_bf16 v[60:63], a[120:123], v[208:211], v[60:63]\n"
"v_perm_b32 v205, v227, v226, s72\n"
"v_mfma_f32_16x16x32_bf16 v[192:195], a[0:3], v[176:179], -4.0\n"
"v_exp_f32 v228, v228\n"
"v_mfma_f32_16x16x32_bf16 v[192:195], a[4:7], v[180:183], v[192:195]\n"
"ds_read_b128 a[0:3], v244 offset:34816\n"
"ds_read_b128 a[4:7], v244 offset:34880\n"
"v_mfma_f32_16x16x32_bf16 v[192:195], a[8:11], v[184:187], v[192:195]\n"
"v_exp_f32 v229, v229\n"
"v_mfma_f32_16x16x32_bf16 v[192:195], a[12:15], v[188:191], v[192:195]\n"
"ds_read_b128 a[8:11], v244 offset:34944\n"
"v_perm_b32 v206, v229, v228, s72\n"
"v_mfma_f32_16x16x32_bf16 v[196:199], a[28:31], v[188:191], -4.0\n"
"s_add_u32 m0, s53, 0\n"
"v_exp_f32 v230, v230\n"
"v_mfma_f32_16x16x32_bf16 v[196:199], a[24:27], v[184:187], v[196:199]\n"
"buffer_load_dwordx4 v247, s[36:39], s52 offen offset:0 lds\n"
"v_mfma_f32_16x16x32_bf16 v[196:199], a[20:23], v[180:183], v[196:199]\n"
"v_exp_f32 v231, v231\n"
"v_mfma_f32_16x16x32_bf16 v[196:199], a[16:19], v[176:179], v[196:199]\n"
"ds_read_b128 a[12:15], v244 offset:35008\n"
"v_perm_b32 v207, v231, v230, s72\n"
"v_mfma_f32_16x16x32_bf16 v[224:227], a[32:35], v[176:179], -4.0\n"
"v_exp_f32 v192, v192\n"
"v_mfma_f32_16x16x32_bf16 v[224:227], a[36:39], v[180:183], v[224:227]\n"
"ds_read_b128 a[28:31], v244 offset:39360\n"
"ds_read_b128 a[24:27], v244 offset:39296\n"
"v_mfma_f32_16x16x32_bf16 v[224:227], a[40:43], v[184:187], v[224:227]\n"
"v_exp_f32 v193, v193\n"
"v_mfma_f32_16x16x32_bf16 v[224:227], a[44:47], v[188:191], v[224:227]\n"
"ds_read_b128 a[20:23], v244 offset:39232\n"
"v_perm_b32 v208, v193, v192, s72\n"
"v_mfma_f32_16x16x32_bf16 v[228:231], a[60:63], v[188:191], -4.0\n"
"s_add_u32 m0, s53, 64\n"
"v_exp_f32 v194, v194\n"
"v_mfma_f32_16x16x32_bf16 v[228:231], a[56:59], v[184:187], v[228:231]\n"
"buffer_load_dwordx4 v247, s[36:39], s52 offen offset:1024 lds\n"
"v_mfma_f32_16x16x32_bf16 v[228:231], a[52:55], v[180:183], v[228:231]\n"
"v_exp_f32 v195, v195\n"
"v_mfma_f32_16x16x32_bf16 v[228:231], a[48:51], v[176:179], v[228:231]\n"
"ds_read_b128 a[16:19], v244 offset:39168\n"
"v_perm_b32 v209, v195, v194, s72\n"
"v_smfmac_f32_16x16x64_bf16 a[136:139], a[144:147], v[200:207], v251\n"
"ds_read_b128 a[32:35], v222 offset:34816\n"
"v_mfma_f32_16x16x32_bf16 v[64:67], a[64:67], v[200:203], v[64:67]\n"
"v_exp_f32 v196, v196\n"
"v_mfma_f32_16x16x32_bf16 v[64:67], a[68:71], v[204:207], v[64:67]\n"
"ds_read_b128 a[36:39], v222 offset:34880\n"
"ds_read_b128 a[40:43], v222 offset:34944\n"
"v_mfma_f32_16x16x32_bf16 v[68:71], a[76:79], v[204:207], v[68:71]\n"
"v_exp_f32 v197, v197\n"
"v_mfma_f32_16x16x32_bf16 v[68:71], a[72:75], v[200:203], v[68:71]\n"
"ds_read_b128 a[44:47], v222 offset:35008\n"
"v_perm_b32 v210, v197, v196, s72\n"
"v_mfma_f32_16x16x32_bf16 v[72:75], a[80:83], v[200:203], v[72:75]\n"
"s_add_u32 m0, s53, 128\n"
"v_exp_f32 v198, v198\n"
"v_mfma_f32_16x16x32_bf16 v[72:75], a[84:87], v[204:207], v[72:75]\n"
"buffer_load_dwordx4 v247, s[36:39], s52 offen offset:2048 lds\n"
"v_mfma_f32_16x16x32_bf16 v[76:79], a[92:95], v[204:207], v[76:79]\n"
"v_exp_f32 v199, v199\n"
"v_mfma_f32_16x16x32_bf16 v[76:79], a[88:91], v[200:203], v[76:79]\n"
"ds_read_b128 a[60:63], v222 offset:39360\n"
"v_perm_b32 v211, v199, v198, s72\n"
"v_mfma_f32_16x16x32_bf16 v[80:83], a[96:99], v[200:203], v[80:83]\n"
"v_exp_f32 v224, v224\n"
"v_mfma_f32_16x16x32_bf16 v[80:83], a[100:103], v[204:207], v[80:83]\n"
"ds_read_b128 a[56:59], v222 offset:39296\n"
"ds_read_b128 a[52:55], v222 offset:39232\n"
"v_mfma_f32_16x16x32_bf16 v[84:87], a[108:111], v[204:207], v[84:87]\n"
"v_exp_f32 v225, v225\n"
"v_mfma_f32_16x16x32_bf16 v[84:87], a[104:107], v[200:203], v[84:87]\n"
"ds_read_b128 a[48:51], v222 offset:39168\n"
"v_perm_b32 v212, v225, v224, s72\n"
"v_mfma_f32_16x16x32_bf16 v[88:91], a[112:115], v[200:203], v[88:91]\n"
"s_add_u32 m0, s53, 192\n"
"v_exp_f32 v226, v226\n"
"v_mfma_f32_16x16x32_bf16 v[88:91], a[116:119], v[204:207], v[88:91]\n"
"buffer_load_dwordx4 v247, s[36:39], s52 offen offset:3072 lds\n"
"v_mfma_f32_16x16x32_bf16 v[92:95], a[124:127], v[204:207], v[92:95]\n"
"v_exp_f32 v227, v227\n"
"v_mfma_f32_16x16x32_bf16 v[92:95], a[120:123], v[200:203], v[92:95]\n"
"s_waitcnt lgkmcnt(4)\n"
"v_perm_b32 v213, v227, v226, s72\n"
"v_mfma_f32_16x16x32_bf16 v[192:195], a[0:3], v[128:131], -4.0\n"
"v_exp_f32 v228, v228\n"
"v_mfma_f32_16x16x32_bf16 v[192:195], a[4:7], v[132:135], v[192:195]\n"
"v_mfma_f32_16x16x32_bf16 v[192:195], a[8:11], v[136:139], v[192:195]\n"
"v_exp_f32 v229, v229\n"
"v_mfma_f32_16x16x32_bf16 v[192:195], a[12:15], v[140:143], v[192:195]\n"
"v_perm_b32 v214, v229, v228, s72\n"
"v_mfma_f32_16x16x32_bf16 v[196:199], a[28:31], v[140:143], -4.0\n"
"s_add_u32 m0, s54, 0\n"
"v_exp_f32 v230, v230\n"
"v_mfma_f32_16x16x32_bf16 v[196:199], a[24:27], v[136:139], v[196:199]\n"
"buffer_load_dwordx4 v248, s[40:43], s52 offen offset:0 lds\n"
"v_mfma_f32_16x16x32_bf16 v[196:199], a[20:23], v[132:135], v[196:199]\n"
"v_exp_f32 v231, v231\n"
"v_mfma_f32_16x16x32_bf16 v[196:199], a[16:19], v[128:131], v[196:199]\n"
"v_perm_b32 v215, v231, v230, s72\n"
"v_mfma_f32_16x16x32_bf16 v[224:227], a[32:35], v[128:131], -4.0\n"
"v_exp_f32 v192, v192\n"
"v_mfma_f32_16x16x32_bf16 v[224:227], a[36:39], v[132:135], v[224:227]\n"
"v_mfma_f32_16x16x32_bf16 v[224:227], a[40:43], v[136:139], v[224:227]\n"
"v_exp_f32 v193, v193\n"
"v_mfma_f32_16x16x32_bf16 v[224:227], a[44:47], v[140:143], v[224:227]\n"
"s_waitcnt lgkmcnt(0)\n"
"v_perm_b32 v200, v193, v192, s72\n"
"v_mfma_f32_16x16x32_bf16 v[228:231], a[60:63], v[140:143], -4.0\n"
"s_add_u32 m0, s54, 8\n"
"v_exp_f32 v194, v194\n"
"v_mfma_f32_16x16x32_bf16 v[228:231], a[56:59], v[136:139], v[228:231]\n"
"Lhop0_1_%=:\n s_cbranch_execz Lhop0_5_%=\n"
"buffer_load_dwordx4 v248, s[40:43], s52 offen offset:1024 lds\n"
"v_mfma_f32_16x16x32_bf16 v[228:231], a[52:55], v[132:135], v[228:231]\n"
"v_exp_f32 v195, v195\n"
"v_mfma_f32_16x16x32_bf16 v[228:231], a[48:51], v[128:131], v[228:231]\n"
"v_perm_b32 v201, v195, v194, s72\n"
"v_smfmac_f32_16x16x64_bf16 a[140:143], a[144:147], v[208:215], v251\n"
"v_mfma_f32_16x16x32_bf16 v[96:99], a[64:67], v[208:211], v[96:99]\n"
"v_exp_f32 v196, v196\n"
"v_mfma_f32_16x16x32_bf16 v[96:99], a[68:71], v[212:215], v[96:99]\n"
"ds_read_b64_tr_b16 a[64:65], v245 offset:33024\n"
"ds_read_b64_tr_b16 a[66:67], v245 offset:37152\n"
"v_mfma_f32_16x16x32_bf16 v[100:103], a[76:79], v[212:215], v[100:103]\n"
"v_exp_f32 v197, v197\n"
"v_mfma_f32_16x16x32_bf16 v[100:103], a[72:75], v[208:211], v[100:103]\n"
"ds_read_b64_tr_b16 a[68:69], v255 offset:33024\n"
"v_perm_b32 v202, v197, v196, s72\n"
"v_mfma_f32_16x16x32_bf16 v[104:107], a[80:83], v[208:211], v[104:107]\n"
"s_add_u32 m0, s54, 1936\n"
"v_exp_f32 v198, v198\n"
"v_mfma_f32_16x16x32_bf16 v[104:107], a[84:87], v[212:215], v[104:107]\n"
"buffer_load_dwordx4 v248, s[40:43], s52 offen offset:128 lds\n"
"v_mfma_f32_16x16x32_bf16 v[108:111], a[92:95], v[212:215], v[108:111]\n"
"v_exp_f32 v199, v199\n"
"v_mfma_f32_16x16x32_bf16 v[108:111], a[88:91], v[208:211], v[108:111]\n"
"ds_read_b64_tr_b16 a[70:71], v255 offset:37152\n"
"v_perm_b32 v203, v199, v198, s72\n"
"v_mfma_f32_16x16x32_bf16 v[112:115], a[96:99], v[208:211], v[112:115]\n"
"v_exp_f32 v224, v224\n"
"v_mfma_f32_16x16x32_bf16 v[112:115], a[100:103], v[212:215], v[112:115]\n"
"ds_read_b64_tr_b16 a[76:77], v255 offset:33032\n"
"ds_read_b64_tr_b16 a[78:79], v255 offset:37160\n"
"v_mfma_f32_16x16x32_bf16 v[116:119], a[108:111], v[212:215], v[116:119]\n"
"v_exp_f32 v225, v225\n"
"v_mfma_f32_16x16x32_bf16 v[116:119], a[104:107], v[208:211], v[116:119]\n"
"ds_read_b64_tr_b16 a[72:73], v245 offset:33032\n"
"v_perm_b32 v204, v225, v224, s72\n"
"v_mfma_f32_16x16x32_bf16 v[120:123], a[112:115], v[208:211], v[120:123]\n"
"s_add_u32 m0, s54, 1944\n"
"v_exp_f32 v226, v226\n"
"v_mfma_f32_16x16x32_bf16 v[120:123], a[116:119], v[212:215], v[120:123]\n"
"buffer_load_dwordx4 v248, s[40:43], s52 offen offset:1152 lds\n"
"v_mfma_f32_16x16x32_bf16 v[124:127], a[124:127], v[212:215], v[124:127]\n"
"s_add_u32 s52, s52, 16384\n"
"v_exp_f32 v227, v227\n"
"v_mfma_f32_16x16x32_bf16 v[124:127], a[120:123], v[208:211], v[124:127]\n"
"ds_read_b64_tr_b16 a[74:75], v245 offset:37160\n"
"v_perm_b32 v205, v227, v226, s72\n"
"v_mfma_f32_16x16x32_bf16 v[192:195], a[0:3], v[144:147], -4.0\n"
"v_exp_f32 v228, v228\n"
"v_mfma_f32_16x16x32_bf16 v[192:195], a[4:7], v[148:151], v[192:195]\n"
"ds_read_b64_tr_b16 a[80:81], v245 offset:33280\n"
"ds_read_b64_tr_b16 a[82:83], v245 offset:37408\n"
"v_mfma_f32_16x16x32_bf16 v[192:195], a[8:11], v[152:155], v[192:195]\n"
"v_exp_f32 v229, v229\n"
"v_mfma_f32_16x16x32_bf16 v[192:195], a[12:15], v[156:159], v[192:195]\n"
"ds_read_b64_tr_b16 a[84:85], v255 offset:33280\n"
"v_perm_b32 v206, v229, v228, s72\n"
"v_mfma_f32_16x16x32_bf16 v[196:199], a[28:31], v[156:159], -4.0\n"
"v_exp_f32 v230, v230\n"
"v_mfma_f32_16x16x32_bf16 v[196:199], a[24:27], v[152:155], v[196:199]\n"
"ds_read_b64_tr_b16 a[86:87], v255 offset:37408\n"
"ds_read_b64_tr_b16 a[92:93], v255 offset:33288\n"
"v_mfma_f32_16x16x32_bf16 v[196:199], a[20:23], v[148:151], v[196:199]\n"
"v_exp_f32 v231, v231\n"
"v_mfma_f32_16x16x32_bf16 v[196:199], a[16:19], v[144:147], v[196:199]\n"
"ds_read_b64_tr_b16 a[94:95], v255 offset:37416\n"
"v_perm_b32 v207, v231, v230, s72\n"
"v_mfma_f32_16x16x32_bf16 v[224:227], a[32:35], v[144:147], -4.0\n"
"v_exp_f32 v192, v192\n"
"v_mfma_f32_16x16x32_bf16 v[224:227], a[36:39], v[148:151], v[224:227]\n"
"ds_read_b64_tr_b16 a[88:89], v245 offset:33288\n"
"ds_read_b64_tr_b16 a[90:91], v245 offset:37416\n"
"v_mfma_f32_16x16x32_bf16 v[224:227], a[40:43], v[152:155], v[224:227]\n"
"v_exp_f32 v193, v193\n"
"v_mfma_f32_16x16x32_bf16 v[224:227], a[44:47], v[156:159], v[224:227]\n"
"ds_read_b64_tr_b16 a[96:97], v245 offset:35088\n"
"v_perm_b32 v208, v193, v192, s72\n"
"v_mfma_f32_16x16x32_bf16 v[228:231], a[60:63], v[156:159], -4.0\n"
"v_exp_f32 v194, v194\n"
"v_mfma_f32_16x16x32_bf16 v[228:231], a[56:59], v[152:155], v[228:231]\n"
"ds_read_b64_tr_b16 a[98:99], v245 offset:39216\n"
"ds_read_b64_tr_b16 a[100:101], v255 offset:35088\n"
"v_mfma_f32_16x16x32_bf16 v[228:231], a[52:55], v[148:151], v[228:231]\n"
"v_exp_f32 v195, v195\n"
"v_mfma_f32_16x16x32_bf16 v[228:231], a[48:51], v[144:147], v[228:231]\n"
"ds_read_b64_tr_b16 a[102:103], v255 offset:39216\n"
"v_perm_b32 v209, v195, v194, s72\n"
"v_smfmac_f32_16x16x64_bf16 a[128:131], a[144:147], v[200:207], v251\n"
"s_waitcnt lgkmcnt(7)\n"
"ds_read_b64_tr_b16 a[108:109], v255 offset:35096\n"
"v_mfma_f32_16x16x32_bf16 v[0:3], a[64:67], v[200:203], v[0:3]\n"
"v_exp_f32 v196, v196\n"
"v_mfma_f32_16x16x32_bf16 v[0:3], a[68:71], v[204:207], v[0:3]\n"
"ds_read_b64_tr_b16 a[110:111], v255 offset:39224\n"
"ds_read_b64_tr_b16 a[104:105], v245 offset:35096\n"
"v_mfma_f32_16x16x32_bf16 v[4:7], a[76:79], v[204:207], v[4:7]\n"
"v_exp_f32 v197, v197\n"
"v_mfma_f32_16x16x32_bf16 v[4:7], a[72:75], v[200:203], v[4:7]\n"
"ds_read_b64_tr_b16 a[106:107], v245 offset:39224\n"
"v_perm_b32 v210, v197, v196, s72\n"
"v_mfma_f32_16x16x32_bf16 v[8:11], a[80:83], v[200:203], v[8:11]\n"
"v_exp_f32 v198, v198\n"
"v_mfma_f32_16x16x32_bf16 v[8:11], a[84:87], v[204:207], v[8:11]\n"
"s_waitcnt lgkmcnt(7)\n"
"ds_read_b64_tr_b16 a[112:113], v245 offset:35344\n"
"ds_read_b64_tr_b16 a[114:115], v245 offset:39472\n"
"v_mfma_f32_16x16x32_bf16 v[12:15], a[92:95], v[204:207], v[12:15]\n"
"v_exp_f32 v199, v199\n"
"v_mfma_f32_16x16x32_bf16 v[12:15], a[88:91], v[200:203], v[12:15]\n"
"s_waitcnt vmcnt(16) lgkmcnt(7)\n"
"ds_read_b64_tr_b16 a[116:117], v255 offset:35344\n"
"ds_read_b64_tr_b16 a[118:119], v255 offset:39472\n"
"ds_read_b64_tr_b16 a[124:125], v255 offset:35352\n"
"ds_read_b64_tr_b16 a[126:127], v255 offset:39480\n"
"v_perm_b32 v211, v199, v198, s72\n"
"v_mfma_f32_16x16x32_bf16 v[16:19], a[96:99], v[200:203], v[16:19]\n"
"s_waitcnt lgkmcnt(10)\n"
"v_exp_f32 v224, v224\n"
"v_mfma_f32_16x16x32_bf16 v[16:19], a[100:103], v[204:207], v[16:19]\n"
"s_waitcnt lgkmcnt(8)\n"
"ds_read_b64_tr_b16 a[120:121], v245 offset:35352\n"
"ds_read_b64_tr_b16 a[122:123], v245 offset:39480\n"
"v_mfma_f32_16x16x32_bf16 v[20:23], a[108:111], v[204:207], v[20:23]\n"
"s_waitcnt lgkmcnt(8)\n"
"v_exp_f32 v225, v225\n"
"v_mfma_f32_16x16x32_bf16 v[20:23], a[104:107], v[200:203], v[20:23]\n"
"s_waitcnt lgkmcnt(4)\n"
"ds_read2_b64 a[152:155], v246 offset0:0 offset1:2\n"
"v_perm_b32 v212, v225, v224, s72\n"
"v_mfma_f32_16x16x32_bf16 v[24:27], a[112:115], v[200:203], v[24:27]\n"
"s_add_u32 m0, s53, 17408\n"
"v_exp_f32 v226, v226\n"
"v_mfma_f32_16x16x32_bf16 v[24:27], a[116:119], v[204:207], v[24:27]\n"
"s_waitcnt lgkmcnt(3)\n"
"buffer_load_dwordx4 v247, s[36:39], s52 offen offset:0 lds\n"
"v_mfma_f32_16x16x32_bf16 v[28:31], a[124:127], v[204:207], v[28:31]\n"
"s_waitcnt lgkmcnt(1)\n"
"v_exp_f32 v227, v227\n"
"v_mfma_f32_16x16x32_bf16 v[28:31], a[120:123], v[200:203], v[28:31]\n"
"ds_read2_b64 a[156:159], v246 offset0:1 offset1:3\n"
"v_perm_b32 v213, v227, v226, s72\n"
"v_mfma_f32_16x16x32_bf16 v[192:195], a[0:3], v[160:163], -4.0\n"
"v_exp_f32 v228, v228\n"
"v_mfma_f32_16x16x32_bf16 v[192:195], a[4:7], v[164:167], v[192:195]\n"
"ds_read2_b64 a[160:163], v246 offset0:32 offset1:34\n"
"ds_read2_b64 a[164:167], v246 offset0:33 offset1:35\n"
"v_mfma_f32_16x16x32_bf16 v[192:195], a[8:11], v[168:171], v[192:195]\n"
"v_exp_f32 v229, v229\n"
"v_mfma_f32_16x16x32_bf16 v[192:195], a[12:15], v[172:175], v[192:195]\n"
"ds_read2_b64 a[168:171], v246 offset0:64 offset1:66\n"
"v_perm_b32 v214, v229, v228, s72\n"
"v_mfma_f32_16x16x32_bf16 v[196:199], a[28:31], v[172:175], -4.0\n"
"s_add_u32 m0, s53, 17472\n"
"v_exp_f32 v230, v230\n"
"v_mfma_f32_16x16x32_bf16 v[196:199], a[24:27], v[168:171], v[196:199]\n"
"buffer_load_dwordx4 v247, s[36:39], s52 offen offset:1024 lds\n"
"v_mfma_f32_16x16x32_bf16 v[196:199], a[20:23], v[164:167], v[196:199]\n"
"v_exp_f32 v231, v231\n"
"v_mfma_f32_16x16x32_bf16 v[196:199], a[16:19], v[160:163], v[196:199]\n"
"ds_read2_b64 a[172:175], v246 offset0:65 offset1:67\n"
"v_perm_b32 v215, v231, v230, s72\n"
"v_mfma_f32_16x16x32_bf16 v[224:227], a[32:35], v[160:163], -4.0\n"
"v_exp_f32 v192, v192\n"
"v_mfma_f32_16x16x32_bf16 v[224:227], a[36:39], v[164:167], v[224:227]\n"
"ds_read2_b64 a[176:179], v246 offset0:96 offset1:98\n"
"ds_read2_b64 a[180:183], v246 offset0:97 offset1:99\n"
"v_mfma_f32_16x16x32_bf16 v[224:227], a[40:43], v[168:171], v[224:227]\n"
"v_exp_f32 v193, v193\n"
"v_mfma_f32_16x16x32_bf16 v[224:227], a[44:47], v[172:175], v[224:227]\n"
"v_perm_b32 v200, v193, v192, s72\n"
"v_mfma_f32_16x16x32_bf16 v[228:231], a[60:63], v[172:175], -4.0\n"
"s_add_u32 m0, s53, 17536\n"
"v_exp_f32 v194, v194\n"
"v_mfma_f32_16x16x32_bf16 v[228:231], a[56:59], v[168:171], v[228:231]\n"
"buffer_load_dwordx4 v247, s[36:39], s52 offen offset:2048 lds\n"
"v_mfma_f32_16x16x32_bf16 v[228:231], a[52:55], v[164:167], v[228:231]\n"
"v_exp_f32 v195, v195\n"
"v_mfma_f32_16x16x32_bf16 v[228:231], a[48:51], v[160:163], v[228:231]\n"
"s_waitcnt vmcnt(11) lgkmcnt(8)\n"
"v_perm_b32 v201, v195, v194, s72\n"
"v_smfmac_f32_16x16x64_bf16 a[132:135], a[144:147], v[208:215], v251\n"
"v_mfma_f32_16x16x32_bf16 v[32:35], a[64:67], v[208:211], v[32:35]\n"
"v_exp_f32 v196, v196\n"
"v_mfma_f32_16x16x32_bf16 v[32:35], a[68:71], v[212:215], v[32:35]\n"
"s_barrier\n"
"v_mfma_f32_16x16x32_bf16 v[36:39], a[76:79], v[212:215], v[36:39]\n"
"v_exp_f32 v197, v197\n"
"v_mfma_f32_16x16x32_bf16 v[36:39], a[72:75], v[208:211], v[36:39]\n"
"v_perm_b32 v202, v197, v196, s72\n"
"v_mfma_f32_16x16x32_bf16 v[40:43], a[80:83], v[208:211], v[40:43]\n"
"s_add_u32 m0, s53, 17600\n"
"v_exp_f32 v198, v198\n"
"v_mfma_f32_16x16x32_bf16 v[40:43], a[84:87], v[212:215], v[40:43]\n"
"buffer_load_dwordx4 v247, s[36:39], s52 offen offset:3072 lds\n"
"v_mfma_f32_16x16x32_bf16 v[44:47], a[92:95], v[212:215], v[44:47]\n"
"v_exp_f32 v199, v199\n"
"v_mfma_f32_16x16x32_bf16 v[44:47], a[88:91], v[208:211], v[44:47]\n"
"v_perm_b32 v203, v199, v198, s72\n"
"v_mfma_f32_16x16x32_bf16 v[48:51], a[96:99], v[208:211], v[48:51]\n"
"v_exp_f32 v224, v224\n"
"v_mfma_f32_16x16x32_bf16 v[48:51], a[100:103], v[212:215], v[48:51]\n"
"v_mfma_f32_16x16x32_bf16 v[52:55], a[108:111], v[212:215], v[52:55]\n"
"v_exp_f32 v225, v225\n"
"v_mfma_f32_16x16x32_bf16 v[52:55], a[104:107], v[208:211], v[52:55]\n"
"v_perm_b32 v204, v225, v224, s72\n"
"v_mfma_f32_16x16x32_bf16 v[56:59], a[112:115], v[208:211], v[56:59]\n"
"s_add_u32 m0, s54, 16512\n"
"v_exp_f32 v226, v226\n"
"v_mfma_f32_16x16x32_bf16 v[56:59], a[116:119], v[212:215], v[56:59]\n"
"buffer_load_dwordx4 v248, s[40:43], s52 offen offset:0 lds\n"
"v_mfma_f32_16x16x32_bf16 v[60:63], a[124:127], v[212:215], v[60:63]\n"
"v_exp_f32 v227, v227\n"
"v_mfma_f32_16x16x32_bf16 v[60:63], a[120:123], v[208:211], v[60:63]\n"
"v_perm_b32 v205, v227, v226, s72\n"
"s_waitcnt lgkmcnt(0)\n"
"s_branch LBN%=\n"
"LAN%=:\n"
"v_mfma_f32_16x16x32_bf16 v[192:195], a[0:3], v[176:179], -4.0\n"
"Lhop1_6_%=:\n s_cbranch_execz Lback1_%=\n"
"v_exp_f32 v228, v228\n"
"v_mfma_f32_16x16x32_bf16 v[192:195], a[4:7], v[180:183], v[192:195]\n"
"ds_read_b128 a[0:3], v244 offset:17408\n"
"ds_read_b128 a[4:7], v244 offset:17472\n"
"v_mfma_f32_16x16x32_bf16 v[192:195], a[8:11], v[184:187], v[192:195]\n"
"v_exp_f32 v229, v229\n"
"v_mfma_f32_16x16x32_bf16 v[192:195], a[12:15], v[188:191], v[192:195]\n"
"ds_read_b128 a[8:11], v244 offset:17536\n"
"v_perm_b32 v206, v229, v228, s72\n"
"v_mfma_f32_16x16x32_bf16 v[196:199], a[28:31], v[188:191], -4.0\n"
"s_add_u32 m0, s55, 0\n"
"v_exp_f32 v230, v230\n"
"v_mfma_f32_16x16x32_bf16 v[196:199], a[24:27], v[184:187], v[196:199]\n"
"s_waitcnt lgkmcnt(14)\n"
"buffer_load_dwordx4 v249, s[44:47], s76 offen offset:0 nt lds\n"
"v_mfma_f32_16x16x32_bf16 v[196:199], a[20:23], v[180:183], v[196:199]\n"
"v_exp_f32 v231, v231\n"
"v_mfma_f32_16x16x32_bf16 v[196:199], a[16:19], v[176:179], v[196:199]\n"
"ds_read_b128 a[12:15], v244 offset:17600\n"
"v_perm_b32 v207, v231, v230, s72\n"
"v_mfma_f32_16x16x32_bf16 v[224:227], a[32:35], v[176:179], -4.0\n"
"v_exp_f32 v192, v192\n"
"v_mfma_f32_16x16x32_bf16 v[224:227], a[36:39], v[180:183], v[224:227]\n"
"ds_read_b128 a[28:31], v244 offset:21952\n"
"ds_read_b128 a[24:27], v244 offset:21888\n"
"v_mfma_f32_16x16x32_bf16 v[224:227], a[40:43], v[184:187], v[224:227]\n"
"v_exp_f32 v193, v193\n"
"v_mfma_f32_16x16x32_bf16 v[224:227], a[44:47], v[188:191], v[224:227]\n"
"ds_read_b128 a[20:23], v244 offset:21824\n"
"v_perm_b32 v208, v193, v192, s72\n"
"v_mfma_f32_16x16x32_bf16 v[228:231], a[60:63], v[188:191], -4.0\n"
"s_add_u32 m0, s55, 8\n"
"v_exp_f32 v194, v194\n"
"v_mfma_f32_16x16x32_bf16 v[228:231], a[56:59], v[184:187], v[228:231]\n"
"buffer_load_dwordx4 v249, s[44:47], s76 offen offset:1024 nt lds\n"
"v_mfma_f32_16x16x32_bf16 v[228:231], a[52:55], v[180:183], v[228:231]\n"
"v_exp_f32 v195, v195\n"
"v_mfma_f32_16x16x32_bf16 v[228:231], a[48:51], v[176:179], v[228:231]\n"
"ds_read_b128 a[16:19], v244 offset:21760\n"
"v_perm_b32 v209, v195, v194, s72\n"
"v_smfmac_f32_16x16x64_bf16 a[136:139], a[144:147], v[200:207], v251\n"
"ds_read_b128 a[32:35], v222 offset:17408\n"
"v_mfma_f32_16x16x32_bf16 v[64:67], a[64:67], v[200:203], v[218:221]\n"
"v_exp_f32 v196, v196\n"
"v_mfma_f32_16x16x32_bf16 v[64:67], a[68:71], v[204:207], v[64:67]\n"
"ds_read_b128 a[36:39], v222 offset:17472\n"
"ds_read_b128 a[40:43], v222 offset:17536\n"
"v_mfma_f32_16x16x32_bf16 v[68:71], a[76:79], v[204:207], v[218:221]\n"
"v_exp_f32 v197, v197\n"
"v_mfma_f32_16x16x32_bf16 v[68:71], a[72:75], v[200:203], v[68:71]\n"
"ds_read_b128 a[44:47], v222 offset:17600\n"
"v_perm_b32 v210, v197, v196, s72\n"
"v_mfma_f32_16x16x32_bf16 v[72:75], a[80:83], v[200:203], v[218:221]\n"
"s_add_u32 m0, s55, 16\n"
"v_exp_f32 v198, v198\n"
"v_mfma_f32_16x16x32_bf16 v[72:75], a[84:87], v[204:207], v[72:75]\n"
"buffer_load_dwordx4 v249, s[44:47], s76 offen offset:2048 nt lds\n"
"v_mfma_f32_16x16x32_bf16 v[76:79], a[92:95], v[204:207], v[218:221]\n"
"v_exp_f32 v199, v199\n"
"v_mfma_f32_16x16x32_bf16 v[76:79], a[88:91], v[200:203], v[76:79]\n"
"ds_read_b128 a[60:63], v222 offset:21952\n"
"v_perm_b32 v211, v199, v198, s72\n"
"v_mfma_f32_16x16x32_bf16 v[80:83], a[96:99], v[200:203], v[218:221]\n"
"v_exp_f32 v224, v224\n"
"v_mfma_f32_16x16x32_bf16 v[80:83], a[100:103], v[204:207], v[80:83]\n"
"ds_read_b128 a[56:59], v222 offset:21888\n"
"ds_read_b128 a[52:55], v222 offset:21824\n"
"v_mfma_f32_16x16x32_bf16 v[84:87], a[108:111], v[204:207], v[218:221]\n"
"v_exp_f32 v225, v225\n"
"v_mfma_f32_16x16x32_bf16 v[84:87], a[104:107], v[200:203], v[84:87]\n"
"ds_read_b128 a[48:51], v222 offset:21760\n"
"v_perm_b32 v212, v225, v224, s72\n"
"v_mfma_f32_16x16x32_bf16 v[88:91], a[112:115], v[200:203], v[218:221]\n"
"s_add_u32 m0, s55, 24\n"
"v_exp_f32 v226, v226\n"
"v_mfma_f32_16x16x32_bf16 v[88:91], a[116:119], v[204:207], v[88:91]\n"
"buffer_load_dwordx4 v249, s[44:47], s76 offen offset:3072 nt lds\n"
"v_mfma_f32_16x16x32_bf16 v[92:95], a[124:127], v[204:207], v[218:221]\n"
"s_add_u32 s76, s76, 4096\n"
"v_exp_f32 v227, v227\n"
"v_mfma_f32_16x16x32_bf16 v[92:95], a[120:123], v[200:203], v[92:95]\n"
"s_waitcnt lgkmcnt(4)\n"
"v_perm_b32 v213, v227, v226, s72\n"
"v_mfma_f32_16x16x32_bf16 v[192:195], a[0:3], v[128:131], -4.0\n"
"v_exp_f32 v228, v228\n"
"v_mfma_f32_16x16x32_bf16 v[192:195], a[4:7], v[132:135], v[192:195]\n"
"v_mfma_f32_16x16x32_bf16 v[192:195], a[8:11], v[136:139], v[192:195]\n"
"v_exp_f32 v229, v229\n"
"v_mfma_f32_16x16x32_bf16 v[192:195], a[12:15], v[140:143], v[192:195]\n"
"v_perm_b32 v214, v229, v228, s72\n"
"v_mfma_f32_16x16x32_bf16 v[196:199], a[28:31], v[140:143], -4.0\n"
"s_add_u32 m0, s54, 49544\n"
"v_exp_f32 v230, v230\n"
"v_mfma_f32_16x16x32_bf16 v[196:199], a[24:27], v[136:139], v[196:199]\n"
"buffer_load_dwordx4 v248, s[40:43], s52 offen offset:1024 lds\n"
"v_mfma_f32_16x16x32_bf16 v[196:199], a[20:23], v[132:135], v[196:199]\n"
"v_exp_f32 v231, v231\n"
"v_mfma_f32_16x16x32_bf16 v[196:199], a[16:19], v[128:131], v[196:199]\n"
"v_perm_b32 v215, v231, v230, s72\n"
"v_mfma_f32_16x16x32_bf16 v[224:227], a[32:35], v[128:131], -4.0\n"
"v_exp_f32 v192, v192\n"
"v_mfma_f32_16x16x32_bf16 v[224:227], a[36:39], v[132:135], v[224:227]\n"
"v_mfma_f32_16x16x32_bf16 v[224:227], a[40:43], v[136:139], v[224:227]\n"
"v_exp_f32 v193, v193\n"
"v_mfma_f32_16x16x32_bf16 v[224:227], a[44:47], v[140:143], v[224:227]\n"
"s_waitcnt lgkmcnt(0)\n"
"v_perm_b32 v200, v193, v192, s72\n"
"v_mfma_f32_16x16x32_bf16 v[228:231], a[60:63], v[140:143], -4.0\n"
"s_add_u32 m0, s54, 51472\n"
"v_exp_f32 v194, v194\n"
"v_mfma_f32_16x16x32_bf16 v[228:231], a[56:59], v[136:139], v[228:231]\n"
"buffer_load_dwordx4 v248, s[40:43], s52 offen offset:128 lds\n"
"v_mfma_f32_16x16x32_bf16 v[228:231], a[52:55], v[132:135], v[228:231]\n"
"v_exp_f32 v195, v195\n"
"v_mfma_f32_16x16x32_bf16 v[228:231], a[48:51], v[128:131], v[228:231]\n"
"v_perm_b32 v201, v195, v194, s72\n"
"v_smfmac_f32_16x16x64_bf16 a[140:143], a[144:147], v[208:215], v251\n"
"v_mfma_f32_16x16x32_bf16 v[96:99], a[64:67], v[208:211], v[218:221]\n"
"v_exp_f32 v196, v196\n"
"v_mfma_f32_16x16x32_bf16 v[96:99], a[68:71], v[212:215], v[96:99]\n"
"ds_read_b64_tr_b16 a[64:65], v245 offset:16512\n"
"ds_read_b64_tr_b16 a[66:67], v245 offset:20640\n"
"v_mfma_f32_16x16x32_bf16 v[100:103], a[76:79], v[212:215], v[218:221]\n"
"v_exp_f32 v197, v197\n"
"v_mfma_f32_16x16x32_bf16 v[100:103], a[72:75], v[208:211], v[100:103]\n"
"ds_read_b64_tr_b16 a[68:69], v255 offset:16512\n"
"v_perm_b32 v202, v197, v196, s72\n"
"v_mfma_f32_16x16x32_bf16 v[104:107], a[80:83], v[208:211], v[218:221]\n"
"v_exp_f32 v198, v198\n"
"v_mfma_f32_16x16x32_bf16 v[104:107], a[84:87], v[212:215], v[104:107]\n"
"ds_read_b64_tr_b16 a[70:71], v255 offset:20640\n"
"ds_read_b64_tr_b16 a[76:77], v255 offset:16520\n"
"v_mfma_f32_16x16x32_bf16 v[108:111], a[92:95], v[212:215], v[218:221]\n"
"v_exp_f32 v199, v199\n"
"v_mfma_f32_16x16x32_bf16 v[108:111], a[88:91], v[208:211], v[108:111]\n"
"ds_read_b64_tr_b16 a[78:79], v255 offset:20648\n"
"v_perm_b32 v203, v199, v198, s72\n"
"v_mfma_f32_16x16x32_bf16 v[112:115], a[96:99], v[208:211], v[218:221]\n"
"v_exp_f32 v224, v224\n"
"v_mfma_f32_16x16x32_bf16 v[112:115], a[100:103], v[212:215], v[112:115]\n"
"ds_read_b64_tr_b16 a[72:73], v245 offset:16520\n"
"ds_read_b64_tr_b16 a[74:75], v245 offset:20648\n"
"v_mfma_f32_16x16x32_bf16 v[116:119], a[108:111], v[212:215], v[218:221]\n"
"v_exp_f32 v225, v225\n"
"v_mfma_f32_16x16x32_bf16 v[116:119], a[104:107], v[208:211], v[116:119]\n"
"ds_read_b64_tr_b16 a[80:81], v245 offset:16768\n"
"v_perm_b32 v204, v225, v224, s72\n"
"v_mfma_f32_16x16x32_bf16 v[120:123], a[112:115], v[208:211], v[218:221]\n"
"v_exp_f32 v226, v226\n"
"v_mfma_f32_16x16x32_bf16 v[120:123], a[116:119], v[212:215], v[120:123]\n"
"ds_read_b64_tr_b16 a[82:83], v245 offset:20896\n"
"ds_read_b64_tr_b16 a[84:85], v255 offset:16768\n"
"v_mfma_f32_16x16x32_bf16 v[124:127], a[124:127], v[212:215], v[218:221]\n"
"v_exp_f32 v227, v227\n"
"v_mfma_f32_16x16x32_bf16 v[124:127], a[120:123], v[208:211], v[124:127]\n"
"ds_read_b64_tr_b16 a[86:87], v255 offset:20896\n"
"v_perm_b32 v205, v227, v226, s72\n"
"v_mfma_f32_16x16x32_bf16 v[192:195], a[0:3], v[144:147], -4.0\n"
"v_exp_f32 v228, v228\n"
"v_mfma_f32_16x16x32_bf16 v[192:195], a[4:7], v[148:151], v[192:195]\n"
"ds_read_b64_tr_b16 a[92:93], v255 offset:16776\n"
"ds_read_b64_tr_b16 a[94:95], v255 offset:20904\n"
"v_mfma_f32_16x16x32_bf16 v[192:195], a[8:11], v[152:155], v[192:195]\n"
"v_exp_f32 v229, v229\n"
"v_mfma_f32_16x16x32_bf16 v[192:195], a[12:15], v[156:159], v[192:195]\n"
"ds_read_b64_tr_b16 a[88:89], v245 offset:16776\n"
"v_perm_b32 v206, v229, v228, s72\n"
"v_mfma_f32_16x16x32_bf16 v[196:199], a[28:31], v[156:159], -4.0\n"
"v_exp_f32 v230, v230\n"
"v_mfma_f32_16x16x32_bf16 v[196:199], a[24:27], v[152:155], v[196:199]\n"
"ds_read_b64_tr_b16 a[90:91], v245 offset:20904\n"
"ds_read_b64_tr_b16 a[96:97], v245 offset:18576\n"
"v_mfma_f32_16x16x32_bf16 v[196:199], a[20:23], v[148:151], v[196:199]\n"
"v_exp_f32 v231, v231\n"
"v_mfma_f32_16x16x32_bf16 v[196:199], a[16:19], v[144:147], v[196:199]\n"
"ds_read_b64_tr_b16 a[98:99], v245 offset:22704\n"
"v_perm_b32 v207, v231, v230, s72\n"
"v_mfma_f32_16x16x32_bf16 v[224:227], a[32:35], v[144:147], -4.0\n"
"v_exp_f32 v192, v192\n"
"v_mfma_f32_16x16x32_bf16 v[224:227], a[36:39], v[148:151], v[224:227]\n"
"ds_read_b64_tr_b16 a[100:101], v255 offset:18576\n"
"ds_read_b64_tr_b16 a[102:103], v255 offset:22704\n"
"v_mfma_f32_16x16x32_bf16 v[224:227], a[40:43], v[152:155], v[224:227]\n"
"v_exp_f32 v193, v193\n"
"v_mfma_f32_16x16x32_bf16 v[224:227], a[44:47], v[156:159], v[224:227]\n"
"ds_read_b64_tr_b16 a[108:109], v255 offset:18584\n"
"v_perm_b32 v208, v193, v192, s72\n"
"v_mfma_f32_16x16x32_bf16 v[228:231], a[60:63], v[156:159], -4.0\n"
"v_exp_f32 v194, v194\n"
"v_mfma_f32_16x16x32_bf16 v[228:231], a[56:59], v[152:155], v[228:231]\n"
"ds_read_b64_tr_b16 a[110:111], v255 offset:22712\n"
"ds_read_b64_tr_b16 a[104:105], v245 offset:18584\n"
"v_mfma_f32_16x16x32_bf16 v[228:231], a[52:55], v[148:151], v[228:231]\n"
"v_exp_f32 v195, v195\n"
"v_mfma_f32_16x16x32_bf16 v[228:231], a[48:51], v[144:147], v[228:231]\n"
"ds_read_b64_tr_b16 a[106:107], v245 offset:22712\n"
"v_perm_b32 v209, v195, v194, s72\n"
"v_smfmac_f32_16x16x64_bf16 a[128:131], a[144:147], v[200:207], v251\n"
"s_waitcnt lgkmcnt(7)\n"
"ds_read_b64_tr_b16 a[112:113], v245 offset:18832\n"
"v_mfma_f32_16x16x32_bf16 v[0:3], a[64:67], v[200:203], v[0:3]\n"
"v_exp_f32 v196, v196\n"
"v_mfma_f32_16x16x32_bf16 v[0:3], a[68:71], v[204:207], v[0:3]\n"
"ds_read_b64_tr_b16 a[114:115], v245 offset:22960\n"
"ds_read_b64_tr_b16 a[116:117], v255 offset:18832\n"
"v_mfma_f32_16x16x32_bf16 v[4:7], a[76:79], v[204:207], v[4:7]\n"
"v_exp_f32 v197, v197\n"
"v_mfma_f32_16x16x32_bf16 v[4:7], a[72:75], v[200:203], v[4:7]\n"
"ds_read_b64_tr_b16 a[118:119], v255 offset:22960\n"
"v_perm_b32 v210, v197, v196, s72\n"
"v_mfma_f32_16x16x32_bf16 v[8:11], a[80:83], v[200:203], v[8:11]\n"
"v_exp_f32 v198, v198\n"
"v_mfma_f32_16x16x32_bf16 v[8:11], a[84:87], v[204:207], v[8:11]\n"
"ds_read_b64_tr_b16 a[124:125], v255 offset:18840\n"
"ds_read_b64_tr_b16 a[126:127], v255 offset:22968\n"
"v_mfma_f32_16x16x32_bf16 v[12:15], a[92:95], v[204:207], v[12:15]\n"
"v_exp_f32 v199, v199\n"
"v_mfma_f32_16x16x32_bf16 v[12:15], a[88:91], v[200:203], v[12:15]\n"
"s_waitcnt lgkmcnt(7)\n"
"ds_read_b64_tr_b16 a[120:121], v245 offset:18840\n"
"v_perm_b32 v211, v199, v198, s72\n"
"v_mfma_f32_16x16x32_bf16 v[16:19], a[96:99], v[200:203], v[16:19]\n"
"v_exp_f32 v224, v224\n"
"v_mfma_f32_16x16x32_bf16 v[16:19], a[100:103], v[204:207], v[16:19]\n"
"ds_read_b64_tr_b16 a[122:123], v245 offset:22968\n"
"v_mfma_f32_16x16x32_bf16 v[20:23], a[108:111], v[204:207], v[20:23]\n"
"s_waitcnt lgkmcnt(7)\n"
"v_exp_f32 v225, v225\n"
"v_mfma_f32_16x16x32_bf16 v[20:23], a[104:107], v[200:203], v[20:23]\n"
"s_waitcnt lgkmcnt(6)\n"
"v_perm_b32 v212, v225, v224, s72\n"
"v_mfma_f32_16x16x32_bf16 v[24:27], a[112:115], v[200:203], v[24:27]\n"
"s_waitcnt lgkmcnt(4)\n"
"v_exp_f32 v226, v226\n"
"v_mfma_f32_16x16x32_bf16 v[24:27], a[116:119], v[204:207], v[24:27]\n"
"s_waitcnt lgkmcnt(2)\n"
"v_mfma_f32_16x16x32_bf16 v[28:31], a[124:127], v[204:207], v[28:31]\n"
"s_waitcnt lgkmcnt(0)\n"
"v_exp_f32 v227, v227\n"
"v_mfma_f32_16x16x32_bf16 v[28:31], a[120:123], v[200:203], v[28:31]\n"
"buffer_store_dwordx4 a[184:187], v250, s[48:51], s84 offen offset:0 sc1\n"
"v_perm_b32 v213, v227, v226, s72\n"
"v_mfma_f32_16x16x32_bf16 v[192:195], a[0:3], v[160:163], -4.0\n"
"v_exp_f32 v228, v228\n"
"v_mfma_f32_16x16x32_bf16 v[192:195], a[4:7], v[164:167], v[192:195]\n"
"v_mfma_f32_16x16x32_bf16 v[192:195], a[8:11], v[168:171], v[192:195]\n"
"v_exp_f32 v229, v229\n"
"v_mfma_f32_16x16x32_bf16 v[192:195], a[12:15], v[172:175], v[192:195]\n"
"v_perm_b32 v214, v229, v228, s72\n"
"v_mfma_f32_16x16x32_bf16 v[196:199], a[28:31], v[172:175], -4.0\n"
"s_add_u32 m0, s54, 51480\n"
"v_exp_f32 v230, v230\n"
"v_mfma_f32_16x16x32_bf16 v[196:199], a[24:27], v[168:171], v[196:199]\n"
"buffer_load_dwordx4 v248, s[40:43], s52 offen offset:1152 lds\n"
"v_mfma_f32_16x16x32_bf16 v[196:199], a[20:23], v[164:167], v[196:199]\n"
"s_add_u32 s52, s52, 16384\n"
"v_exp_f32 v231, v231\n"
"v_mfma_f32_16x16x32_bf16 v[196:199], a[16:19], v[160:163], v[196:199]\n"
"v_perm_b32 v215, v231, v230, s72\n"
"v_mfma_f32_16x16x32_bf16 v[224:227], a[32:35], v[160:163], -4.0\n"
"v_exp_f32 v192, v192\n"
"v_mfma_f32_16x16x32_bf16 v[224:227], a[36:39], v[164:167], v[224:227]\n"
"v_mfma_f32_16x16x32_bf16 v[224:227], a[40:43], v[168:171], v[224:227]\n"
"v_exp_f32 v193, v193\n"
"v_mfma_f32_16x16x32_bf16 v[224:227], a[44:47], v[172:175], v[224:227]\n"
"v_perm_b32 v200, v193, v192, s72\n"
"v_mfma_f32_16x16x32_bf16 v[228:231], a[60:63], v[172:175], -4.0\n"
"s_add_u32 m0, s53, 0\n"
"v_exp_f32 v194, v194\n"
"v_mfma_f32_16x16x32_bf16 v[228:231], a[56:59], v[168:171], v[228:231]\n"
"buffer_load_dwordx4 v247, s[36:39], s52 offen offset:0 lds\n"
"v_mfma_f32_16x16x32_bf16 v[228:231], a[52:55], v[164:167], v[228:231]\n"
"v_exp_f32 v195, v195\n"
"v_mfma_f32_16x16x32_bf16 v[228:231], a[48:51], v[160:163], v[228:231]\n"
"s_waitcnt vmcnt(13) lgkmcnt(0)\n"
"v_perm_b32 v201, v195, v194, s72\n"
"v_smfmac_f32_16x16x64_bf16 a[132:135], a[144:147], v[208:215], v251\n"
"v_mfma_f32_16x16x32_bf16 v[32:35], a[64:67], v[208:211], v[32:35]\n"
"v_exp_f32 v196, v196\n"
"v_mfma_f32_16x16x32_bf16 v[32:35], a[68:71], v[212:215], v[32:35]\n"
"s_barrier\n"
"v_mfma_f32_16x16x32_bf16 v[36:39], a[76:79], v[212:215], v[36:39]\n"
"v_exp_f32 v197, v197\n"
"v_mfma_f32_16x16x32_bf16 v[36:39], a[72:75], v[208:211], v[36:39]\n"
"v_perm_b32 v202, v197, v196, s72\n"
"v_mfma_f32_16x16x32_bf16 v[40:43], a[80:83], v[208:211], v[40:43]\n"
"s_add_u32 m0, s53, 64\n"
"v_exp_f32 v198, v198\n"
"v_mfma_f32_16x16x32_bf16 v[40:43], a[84:87], v[212:215], v[40:43]\n"
"buffer_load_dwordx4 v247, s[36:39], s52 offen offset:1024 lds\n"
"v_mfma_f32_16x16x32_bf16 v[44:47], a[92:95], v[212:215], v[44:47]\n"
"v_exp_f32 v199, v199\n"
"v_mfma_f32_16x16x32_bf16 v[44:47], a[88:91], v[208:211], v[44:47]\n"
"v_perm_b32 v203, v199, v198, s72\n"
"v_mfma_f32_16x16x32_bf16 v[48:51], a[96:99], v[208:211], v[48:51]\n"
"v_exp_f32 v224, v224\n"
"v_mfma_f32_16x16x32_bf16 v[48:51], a[100:103], v[212:215], v[48:51]\n"
"v_mfma_f32_16x16x32_bf16 v[52:55], a[108:111], v[212:215], v[52:55]\n"
"v_exp_f32 v225, v225\n"
"v_mfma_f32_16x16x32_bf16 v[52:55], a[104:107], v[208:211], v[52:55]\n"
"v_perm_b32 v204, v225, v224, s72\n"
"v_mfma_f32_16x16x32_bf16 v[56:59], a[112:115], v[208:211], v[56:59]\n"
"s_add_u32 m0, s53, 128\n"
"v_exp_f32 v226, v226\n"
"v_mfma_f32_16x16x32_bf16 v[56:59], a[116:119], v[212:215], v[56:59]\n"
"buffer_load_dwordx4 v247, s[36:39], s52 offen offset:2048 lds\n"
"v_mfma_f32_16x16x32_bf16 v[60:63], a[124:127], v[212:215], v[60:63]\n"
"v_exp_f32 v227, v227\n"
"v_mfma_f32_16x16x32_bf16 v[60:63], a[120:123], v[208:211], v[60:63]\n"
"v_perm_b32 v205, v227, v226, s72\n"
"v_mfma_f32_16x16x32_bf16 v[192:195], a[0:3], v[176:179], -4.0\n"
"v_exp_f32 v228, v228\n"
"v_mfma_f32_16x16x32_bf16 v[192:195], a[4:7], v[180:183], v[192:195]\n"
"ds_read_b128 a[0:3], v244 offset:34816\n"
"ds_read_b128 a[4:7], v244 offset:34880\n"
"v_mfma_f32_16x16x32_bf16 v[192:195], a[8:11], v[184:187], v[192:195]\n"
"v_exp_f32 v229, v229\n"
"v_mfma_f32_16x16x32_bf16 v[192:195], a[12:15], v[188:191], v[192:195]\n"
"ds_read_b128 a[8:11], v244 offset:34944\n"
"v_perm_b32 v206, v229, v228, s72\n"
"v_mfma_f32_16x16x32_bf16 v[196:199], a[28:31], v[188:191], -4.0\n"
"s_add_u32 m0, s53, 192\n"
"v_exp_f32 v230, v230\n"
"v_mfma_f32_16x16x32_bf16 v[196:199], a[24:27], v[184:187], v[196:199]\n"
"buffer_load_dwordx4 v247, s[36:39], s52 offen offset:3072 lds\n"
"v_mfma_f32_16x16x32_bf16 v[196:199], a[20:23], v[180:183], v[196:199]\n"
"v_exp_f32 v231, v231\n"
"v_mfma_f32_16x16x32_bf16 v[196:199], a[16:19], v[176:179], v[196:199]\n"
"buffer_store_dwordx4 a[188:191], v250, s[48:51], s84 offen offset:1024 sc1\n"
"v_perm_b32 v207, v231, v230, s72\n"
"v_mfma_f32_16x16x32_bf16 v[224:227], a[32:35], v[176:179], -4.0\n"
"v_exp_f32 v192, v192\n"
"v_mfma_f32_16x16x32_bf16 v[224:227], a[36:39], v[180:183], v[224:227]\n"
"ds_read_b128 a[12:15], v244 offset:35008\n"
"ds_read_b128 a[28:31], v244 offset:39360\n"
"v_mfma_f32_16x16x32_bf16 v[224:227], a[40:43], v[184:187], v[224:227]\n"
"v_exp_f32 v193, v193\n"
"v_mfma_f32_16x16x32_bf16 v[224:227], a[44:47], v[188:191], v[224:227]\n"
"ds_read_b128 a[24:27], v244 offset:39296\n"
"v_perm_b32 v208, v193, v192, s72\n"
"v_mfma_f32_16x16x32_bf16 v[228:231], a[60:63], v[188:191], -4.0\n"
"s_add_u32 m0, s54, 0\n"
"v_exp_f32 v194, v194\n"
"v_mfma_f32_16x16x32_bf16 v[228:231], a[56:59], v[184:187], v[228:231]\n"
"buffer_load_dwordx4 v248, s[40:43], s52 offen offset:0 lds\n"
"v_mfma_f32_16x16x32_bf16 v[228:231], a[52:55], v[180:183], v[228:231]\n"
"v_exp_f32 v195, v195\n"
"v_mfma_f32_16x16x32_bf16 v[228:231], a[48:51], v[176:179], v[228:231]\n"
"ds_read_b128 a[20:23], v244 offset:39232\n"
"v_perm_b32 v209, v195, v194, s72\n"
"v_smfmac_f32_16x16x64_bf16 a[136:139], a[144:147], v[200:207], v251\n"
"ds_read_b128 a[16:19], v244 offset:39168\n"
"v_mfma_f32_16x16x32_bf16 v[64:67], a[64:67], v[200:203], v[64:67]\n"
"v_exp_f32 v196, v196\n"
"v_mfma_f32_16x16x32_bf16 v[64:67], a[68:71], v[204:207], v[64:67]\n"
"ds_read_b128 a[32:35], v222 offset:34816\n"
"ds_read_b128 a[36:39], v222 offset:34880\n"
"v_mfma_f32_16x16x32_bf16 v[68:71], a[76:79], v[204:207], v[68:71]\n"
"v_exp_f32 v197, v197\n"
"v_mfma_f32_16x16x32_bf16 v[68:71], a[72:75], v[200:203], v[68:71]\n"
"ds_read_b128 a[40:43], v222 offset:34944\n"
"v_perm_b32 v210, v197, v196, s72\n"
"v_mfma_f32_16x16x32_bf16 v[72:75], a[80:83], v[200:203], v[72:75]\n"
"s_add_u32 m0, s54, 8\n"
"v_exp_f32 v198, v198\n"
"v_mfma_f32_16x16x32_bf16 v[72:75], a[84:87], v[204:207], v[72:75]\n"
"Lhop1_7_%=:\n s_cbranch_execz Lback1_%=\n"
"buffer_load_dwordx4 v248, s[40:43], s52 offen offset:1024 lds\n"
"v_mfma_f32_16x16x32_bf16 v[76:79], a[92:95], v[204:207], v[76:79]\n"
"v_exp_f32 v199, v199\n"
"v_mfma_f32_16x16x32_bf16 v[76:79], a[88:91], v[200:203], v[76:79]\n"
"ds_read_b128 a[44:47], v222 offset:35008\n"
"v_perm_b32 v211, v199, v198, s72\n"
"v_mfma_f32_16x16x32_bf16 v[80:83], a[96:99], v[200:203], v[80:83]\n"
"v_exp_f32 v224, v224\n"
"v_mfma_f32_16x16x32_bf16 v[80:83], a[100:103], v[204:207], v[80:83]\n"
"ds_read_b128 a[60:63], v222 offset:39360\n"
"ds_read_b128 a[56:59], v222 offset:39296\n"
"v_mfma_f32_16x16x32_bf16 v[84:87], a[108:111], v[204:207], v[84:87]\n"
"v_exp_f32 v225, v225\n"
"v_mfma_f32_16x16x32_bf16 v[84:87], a[104:107], v[200:203], v[84:87]\n"
"ds_read_b128 a[52:55], v222 offset:39232\n"
"v_perm_b32 v212, v225, v224, s72\n"
"v_mfma_f32_16x16x32_bf16 v[88:91], a[112:115], v[200:203], v[88:91]\n"
"s_add_u32 m0, s54, 1936\n"
"v_exp_f32 v226, v226\n"
"v_mfma_f32_16x16x32_bf16 v[88:91], a[116:119], v[204:207], v[88:91]\n"
"buffer_load_dwordx4 v248, s[40:43], s52 offen offset:128 lds\n"
"v_mfma_f32_16x16x32_bf16 v[92:95], a[124:127], v[204:207], v[92:95]\n"
"v_exp_f32 v227, v227\n"
"v_mfma_f32_16x16x32_bf16 v[92:95], a[120:123], v[200:203], v[92:95]\n"
"s_waitcnt lgkmcnt(4)\n"
"ds_read_b128 a[48:51], v222 offset:39168\n"
"v_perm_b32 v213, v227, v226, s72\n"
"v_mfma_f32_16x16x32_bf16 v[192:195], a[0:3], v[128:131], -4.0\n"
"v_exp_f32 v228, v228\n"
"v_mfma_f32_16x16x32_bf16 v[192:195], a[4:7], v[132:135], v[192:195]\n"
"v_mfma_f32_16x16x32_bf16 v[192:195], a[8:11], v[136:139], v[192:195]\n"
"v_exp_f32 v229, v229\n"
"v_mfma_f32_16x16x32_bf16 v[192:195], a[12:15], v[140:143], v[192:195]\n"
"v_perm_b32 v214, v229, v228, s72\n"
"v_mfma_f32_16x16x32_bf16 v[196:199], a[28:31], v[140:143], -4.0\n"
"s_add_u32 m0, s54, 1944\n"
"v_exp_f32 v230, v230\n"
"v_mfma_f32_16x16x32_bf16 v[196:199], a[24:27], v[136:139], v[196:199]\n"
"buffer_load_dwordx4 v248, s[40:43], s52 offen offset:1152 lds\n"
"v_mfma_f32_16x16x32_bf16 v[196:199], a[20:23], v[132:135], v[196:199]\n"
"s_add_u32 s52, s52, 16384\n"
"v_exp_f32 v231, v231\n"
"v_mfma_f32_16x16x32_bf16 v[196:199], a[16:19], v[128:131], v[196:199]\n"
"v_perm_b32 v215, v231, v230, s72\n"
"v_mfma_f32_16x16x32_bf16 v[224:227], a[32:35], v[128:131], -4.0\n"
"v_exp_f32 v192, v192\n"
"v_mfma_f32_16x16x32_bf16 v[224:227], a[36:39], v[132:135], v[224:227]\n"
"v_mfma_f32_16x16x32_bf16 v[224:227], a[40:43], v[136:139], v[224:227]\n"
"s_waitcnt lgkmcnt(0)\n"
"v_exp_f32 v193, v193\n"
"v_mfma_f32_16x16x32_bf16 v[224:227], a[44:47], v[140:143], v[224:227]\n"
"v_perm_b32 v200, v193, v192, s72\n"
"v_mfma_f32_16x16x32_bf16 v[228:231], a[60:63], v[140:143], -4.0\n"
"v_exp_f32 v194, v194\n"
"v_mfma_f32_16x16x32_bf16 v[228:231], a[56:59], v[136:139], v[228:231]\n"
"v_mfma_f32_16x16x32_bf16 v[228:231], a[52:55], v[132:135], v[228:231]\n"
"v_exp_f32 v195, v195\n"
"v_mfma_f32_16x16x32_bf16 v[228:231], a[48:51], v[128:131], v[228:231]\n"
"buffer_store_dwordx4 a[192:195], v250, s[48:51], s84 offen offset:128 sc1\n"
"v_perm_b32 v201, v195, v194, s72\n"
"v_smfmac_f32_16x16x64_bf16 a[140:143], a[144:147], v[208:215], v251\n"
"v_mfma_f32_16x16x32_bf16 v[96:99], a[64:67], v[208:211], v[96:99]\n"
"v_exp_f32 v196, v196\n"
"v_mfma_f32_16x16x32_bf16 v[96:99], a[68:71], v[212:215], v[96:99]\n"
"ds_read_b64_tr_b16 a[64:65], v245 offset:33024\n"
"ds_read_b64_tr_b16 a[66:67], v245 offset:37152\n"
"v_mfma_f32_16x16x32_bf16 v[100:103], a[76:79], v[212:215], v[100:103]\n"
"v_exp_f32 v197, v197\n"
"v_mfma_f32_16x16x32_bf16 v[100:103], a[72:75], v[208:211], v[100:103]\n"
"ds_read_b64_tr_b16 a[68:69], v255 offset:33024\n"
"v_perm_b32 v202, v197, v196, s72\n"
"v_mfma_f32_16x16x32_bf16 v[104:107], a[80:83], v[208:211], v[104:107]\n"
"v_exp_f32 v198, v198\n"
"v_mfma_f32_16x16x32_bf16 v[104:107], a[84:87], v[212:215], v[104:107]\n"
"ds_read_b64_tr_b16 a[70:71], v255 offset:37152\n"
"ds_read_b64_tr_b16 a[76:77], v255 offset:33032\n"
"v_mfma_f32_16x16x32_bf16 v[108:111], a[92:95], v[212:215], v[108:111]\n"
"v_exp_f32 v199, v199\n"
"v_mfma_f32_16x16x32_bf16 v[108:111], a[88:91], v[208:211], v[108:111]\n"
"ds_read_b64_tr_b16 a[78:79], v255 offset:37160\n"
"v_perm_b32 v203, v199, v198, s72\n"
"v_mfma_f32_16x16x32_bf16 v[112:115], a[96:99], v[208:211], v[112:115]\n"
"v_exp_f32 v224, v224\n"
"v_mfma_f32_16x16x32_bf16 v[112:115], a[100:103], v[212:215], v[112:115]\n"
"ds_read_b64_tr_b16 a[72:73], v245 offset:33032\n"
"ds_read_b64_tr_b16 a[74:75], v245 offset:37160\n"
"v_mfma_f32_16x16x32_bf16 v[116:119], a[108:111], v[212:215], v[116:119]\n"
"v_exp_f32 v225, v225\n"
"v_mfma_f32_16x16x32_bf16 v[116:119], a[104:107], v[208:211], v[116:119]\n"
"ds_read_b64_tr_b16 a[80:81], v245 offset:33280\n"
"v_perm_b32 v204, v225, v224, s72\n"
"v_mfma_f32_16x16x32_bf16 v[120:123], a[112:115], v[208:211], v[120:123]\n"
"v_exp_f32 v226, v226\n"
"v_mfma_f32_16x16x32_bf16 v[120:123], a[116:119], v[212:215], v[120:123]\n"
"ds_read_b64_tr_b16 a[82:83], v245 offset:37408\n"
"ds_read_b64_tr_b16 a[84:85], v255 offset:33280\n"
"v_mfma_f32_16x16x32_bf16 v[124:127], a[124:127], v[212:215], v[124:127]\n"
"v_exp_f32 v227, v227\n"
"v_mfma_f32_16x16x32_bf16 v[124:127], a[120:123], v[208:211], v[124:127]\n"
"ds_read_b64_tr_b16 a[86:87], v255 offset:37408\n"
"v_perm_b32 v205, v227, v226, s72\n"
"v_mfma_f32_16x16x32_bf16 v[192:195], a[0:3], v[144:147], -4.0\n"
"v_exp_f32 v228, v228\n"
"v_mfma_f32_16x16x32_bf16 v[192:195], a[4:7], v[148:151], v[192:195]\n"
"ds_read_b64_tr_b16 a[92:93], v255 offset:33288\n"
"ds_read_b64_tr_b16 a[94:95], v255 offset:37416\n"
"v_mfma_f32_16x16x32_bf16 v[192:195], a[8:11], v[152:155], v[192:195]\n"
"v_exp_f32 v229, v229\n"
"v_mfma_f32_16x16x32_bf16 v[192:195], a[12:15], v[156:159], v[192:195]\n"
"ds_read_b64_tr_b16 a[88:89], v245 offset:33288\n"
"v_perm_b32 v206, v229, v228, s72\n"
"v_mfma_f32_16x16x32_bf16 v[196:199], a[28:31], v[156:159], -4.0\n"
"v_exp_f32 v230, v230\n"
"v_mfma_f32_16x16x32_bf16 v[196:199], a[24:27], v[152:155], v[196:199]\n"
"ds_read_b64_tr_b16 a[90:91], v245 offset:37416\n"
"ds_read_b64_tr_b16 a[96:97], v245 offset:35088\n"
"v_mfma_f32_16x16x32_bf16 v[196:199], a[20:23], v[148:151], v[196:199]\n"
"v_exp_f32 v231, v231\n"
"v_mfma_f32_16x16x32_bf16 v[196:199], a[16:19], v[144:147], v[196:199]\n"
"buffer_store_dwordx4 a[196:199], v250, s[48:51], s84 offen offset:1152 sc1\n"
"v_perm_b32 v207, v231, v230, s72\n"
"v_mfma_f32_16x16x32_bf16 v[224:227], a[32:35], v[144:147], -4.0\n"
"s_add_u32 s84, s84, 4096\n"
"v_exp_f32 v192, v192\n"
"v_mfma_f32_16x16x32_bf16 v[224:227], a[36:39], v[148:151], v[224:227]\n"
"ds_read_b64_tr_b16 a[98:99], v245 offset:39216\n"
"ds_read_b64_tr_b16 a[100:101], v255 offset:35088\n"
"v_mfma_f32_16x16x32_bf16 v[224:227], a[40:43], v[152:155], v[224:227]\n"
"v_exp_f32 v193, v193\n"
"v_mfma_f32_16x16x32_bf16 v[224:227], a[44:47], v[156:159], v[224:227]\n"
"ds_read_b64_tr_b16 a[102:103], v255 offset:39216\n"
"v_perm_b32 v208, v193, v192, s72\n"
"v_mfma_f32_16x16x32_bf16 v[228:231], a[60:63], v[156:159], -4.0\n"
"v_exp_f32 v194, v194\n"
"v_mfma_f32_16x16x32_bf16 v[228:231], a[56:59], v[152:155], v[228:231]\n"
"ds_read_b64_tr_b16 a[108:109], v255 offset:35096\n"
"ds_read_b64_tr_b16 a[110:111], v255 offset:39224\n"
"v_mfma_f32_16x16x32_bf16 v[228:231], a[52:55], v[148:151], v[228:231]\n"
"v_exp_f32 v195, v195\n"
"v_mfma_f32_16x16x32_bf16 v[228:231], a[48:51], v[144:147], v[228:231]\n"
"ds_read_b64_tr_b16 a[104:105], v245 offset:35096\n"
"v_perm_b32 v209, v195, v194, s72\n"
"v_smfmac_f32_16x16x64_bf16 a[128:131], a[144:147], v[200:207], v251\n"
"s_waitcnt lgkmcnt(6)\n"
"ds_read_b64_tr_b16 a[106:107], v245 offset:39224\n"
"v_mfma_f32_16x16x32_bf16 v[0:3], a[64:67], v[200:203], v[0:3]\n"
"v_exp_f32 v196, v196\n"
"v_mfma_f32_16x16x32_bf16 v[0:3], a[68:71], v[204:207], v[0:3]\n"
"ds_read_b64_tr_b16 a[112:113], v245 offset:35344\n"
"ds_read_b64_tr_b16 a[114:115], v245 offset:39472\n"
"v_mfma_f32_16x16x32_bf16 v[4:7], a[76:79], v[204:207], v[4:7]\n"
"v_exp_f32 v197, v197\n"
"v_mfma_f32_16x16x32_bf16 v[4:7], a[72:75], v[200:203], v[4:7]\n"
"ds_read_b64_tr_b16 a[116:117], v255 offset:35344\n"
"v_perm_b32 v210, v197, v196, s72\n"
"v_mfma_f32_16x16x32_bf16 v[8:11], a[80:83], v[200:203], v[8:11]\n"
"v_exp_f32 v198, v198\n"
"v_mfma_f32_16x16x32_bf16 v[8:11], a[84:87], v[204:207], v[8:11]\n"
"ds_read_b64_tr_b16 a[118:119], v255 offset:39472\n"
"ds_read_b64_tr_b16 a[124:125], v255 offset:35352\n"
"v_mfma_f32_16x16x32_bf16 v[12:15], a[92:95], v[204:207], v[12:15]\n"
"v_exp_f32 v199, v199\n"
"v_mfma_f32_16x16x32_bf16 v[12:15], a[88:91], v[200:203], v[12:15]\n"
"s_waitcnt vmcnt(11) lgkmcnt(7)\n"
"ds_read_b64_tr_b16 a[126:127], v255 offset:39480\n"
"v_perm_b32 v211, v199, v198, s72\n"
"v_mfma_f32_16x16x32_bf16 v[16:19], a[96:99], v[200:203], v[16:19]\n"
"v_exp_f32 v224, v224\n"
"v_mfma_f32_16x16x32_bf16 v[16:19], a[100:103], v[204:207], v[16:19]\n"
"ds_read_b64_tr_b16 a[120:121], v245 offset:35352\n"
"ds_read_b64_tr_b16 a[122:123], v245 offset:39480\n"
"v_mfma_f32_16x16x32_bf16 v[20:23], a[108:111], v[204:207], v[20:23]\n"
"s_waitcnt lgkmcnt(8)\n"
"v_exp_f32 v225, v225\n"
"v_mfma_f32_16x16x32_bf16 v[20:23], a[104:107], v[200:203], v[20:23]\n"
"s_waitcnt lgkmcnt(4)\n"
"ds_read2_b64 a[152:155], v246 offset0:0 offset1:2\n"
"v_perm_b32 v212, v225, v224, s72\n"
"v_mfma_f32_16x16x32_bf16 v[24:27], a[112:115], v[200:203], v[24:27]\n"
"s_add_u32 m0, s53, 17408\n"
"v_exp_f32 v226, v226\n"
"v_mfma_f32_16x16x32_bf16 v[24:27], a[116:119], v[204:207], v[24:27]\n"
"s_waitcnt lgkmcnt(3)\n"
"buffer_load_dwordx4 v247, s[36:39], s52 offen offset:0 lds\n"
"v_mfma_f32_16x16x32_bf16 v[28:31], a[124:127], v[204:207], v[28:31]\n"
"s_waitcnt lgkmcnt(1)\n"
"v_exp_f32 v227, v227\n"
"v_mfma_f32_16x16x32_bf16 v[28:31], a[120:123], v[200:203], v[28:31]\n"
"ds_read2_b64 a[156:159], v246 offset0:1 offset1:3\n"
"v_perm_b32 v213, v227, v226, s72\n"
"v_mfma_f32_16x16x32_bf16 v[192:195], a[0:3], v[160:163], -4.0\n"
"v_exp_f32 v228, v228\n"
"v_mfma_f32_16x16x32_bf16 v[192:195], a[4:7], v[164:167], v[192:195]\n"
"ds_read2_b64 a[160:163], v246 offset0:32 offset1:34\n"
"ds_read2_b64 a[164:167], v246 offset0:33 offset1:35\n"
"v_mfma_f32_16x16x32_bf16 v[192:195], a[8:11], v[168:171], v[192:195]\n"
"v_exp_f32 v229, v229\n"
"v_mfma_f32_16x16x32_bf16 v[192:195], a[12:15], v[172:175], v[192:195]\n"
"ds_read2_b64 a[168:171], v246 offset0:64 offset1:66\n"
"v_perm_b32 v214, v229, v228, s72\n"
"v_mfma_f32_16x16x32_bf16 v[196:199], a[28:31], v[172:175], -4.0\n"
"s_add_u32 m0, s53, 17472\n"
"v_exp_f32 v230, v230\n"
"v_mfma_f32_16x16x32_bf16 v[196:199], a[24:27], v[168:171], v[196:199]\n"
"buffer_load_dwordx4 v247, s[36:39], s52 offen offset:1024 lds\n"
"v_mfma_f32_16x16x32_bf16 v[196:199], a[20:23], v[164:167], v[196:199]\n"
"v_exp_f32 v231, v231\n"
"v_mfma_f32_16x16x32_bf16 v[196:199], a[16:19], v[160:163], v[196:199]\n"
"ds_read2_b64 a[172:175], v246 offset0:65 offset1:67\n"
"v_perm_b32 v215, v231, v230, s72\n"
"v_mfma_f32_16x16x32_bf16 v[224:227], a[32:35], v[160:163], -4.0\n"
"v_exp_f32 v192, v192\n"
"v_mfma_f32_16x16x32_bf16 v[224:227], a[36:39], v[164:167], v[224:227]\n"
"ds_read2_b64 a[176:179], v246 offset0:96 offset1:98\n"
"ds_read2_b64 a[180:183], v246 offset0:97 offset1:99\n"
"v_mfma_f32_16x16x32_bf16 v[224:227], a[40:43], v[168:171], v[224:227]\n"
"v_exp_f32 v193, v193\n"
"v_mfma_f32_16x16x32_bf16 v[224:227], a[44:47], v[172:175], v[224:227]\n"
"v_perm_b32 v200, v193, v192, s72\n"
"v_mfma_f32_16x16x32_bf16 v[228:231], a[60:63], v[172:175], -4.0\n"
"s_add_u32 m0, s53, 17536\n"
"v_exp_f32 v194, v194\n"
"v_mfma_f32_16x16x32_bf16 v[228:231], a[56:59], v[168:171], v[228:231]\n"
"buffer_load_dwordx4 v247, s[36:39], s52 offen offset:2048 lds\n"
"v_mfma_f32_16x16x32_bf16 v[228:231], a[52:55], v[164:167], v[228:231]\n"
"v_exp_f32 v195, v195\n"
"v_mfma_f32_16x16x32_bf16 v[228:231], a[48:51], v[160:163], v[228:231]\n"
"s_waitcnt vmcnt(11) lgkmcnt(8)\n"
"v_perm_b32 v201, v195, v194, s72\n"
"v_smfmac_f32_16x16x64_bf16 a[132:135], a[144:147], v[208:215], v251\n"
"v_mfma_f32_16x16x32_bf16 v[32:35], a[64:67], v[208:211], v[32:35]\n"
"v_exp_f32 v196, v196\n"
"v_mfma_f32_16x16x32_bf16 v[32:35], a[68:71], v[212:215], v[32:35]\n"
"s_barrier\n"
"v_mfma_f32_16x16x32_bf16 v[36:39], a[76:79], v[212:215], v[36:39]\n"
"v_exp_f32 v197, v197\n"
"v_mfma_f32_16x16x32_bf16 v[36:39], a[72:75], v[208:211], v[36:39]\n"
"v_perm_b32 v202, v197, v196, s72\n"
"v_mfma_f32_16x16x32_bf16 v[40:43], a[80:83], v[208:211], v[40:43]\n"
"s_add_u32 m0, s53, 17600\n"
"v_exp_f32 v198, v198\n"
"v_mfma_f32_16x16x32_bf16 v[40:43], a[84:87], v[212:215], v[40:43]\n"
"buffer_load_dwordx4 v247, s[36:39], s52 offen offset:3072 lds\n"
"v_mfma_f32_16x16x32_bf16 v[44:47], a[92:95], v[212:215], v[44:47]\n"
"v_exp_f32 v199, v199\n"
"v_mfma_f32_16x16x32_bf16 v[44:47], a[88:91], v[208:211], v[44:47]\n"
"v_perm_b32 v203, v199, v198, s72\n"
"v_mfma_f32_16x16x32_bf16 v[48:51], a[96:99], v[208:211], v[48:51]\n"
"v_exp_f32 v224, v224\n"
"v_mfma_f32_16x16x32_bf16 v[48:51], a[100:103], v[212:215], v[48:51]\n"
"v_mfma_f32_16x16x32_bf16 v[52:55], a[108:111], v[212:215], v[52:55]\n"
"v_exp_f32 v225, v225\n"
"v_mfma_f32_16x16x32_bf16 v[52:55], a[104:107], v[208:211], v[52:55]\n"
"v_perm_b32 v204, v225, v224, s72\n"
"v_mfma_f32_16x16x32_bf16 v[56:59], a[112:115], v[208:211], v[56:59]\n"
"s_add_u32 m0, s54, 16512\n"
"v_exp_f32 v226, v226\n"
"v_mfma_f32_16x16x32_bf16 v[56:59], a[116:119], v[212:215], v[56:59]\n"
"buffer_load_dwordx4 v248, s[40:43], s52 offen offset:0 lds\n"
"v_mfma_f32_16x16x32_bf16 v[60:63], a[124:127], v[212:215], v[60:63]\n"
"v_exp_f32 v227, v227\n"
"v_mfma_f32_16x16x32_bf16 v[60:63], a[120:123], v[208:211], v[60:63]\n"
"v_perm_b32 v205, v227, v226, s72\n"
"s_waitcnt lgkmcnt(0)\n"
"LBN%=:\n"
"v_mfma_f32_16x16x32_bf16 v[192:195], a[0:3], v[176:179], -4.0\n"
"Lhop0_4_%=:\n s_cbranch_execz Lback0_%=\n"
"v_exp_f32 v228, v228\n"
"v_mfma_f32_16x16x32_bf16 v[192:195], a[4:7], v[180:183], v[192:195]\n"
"ds_read_b128 a[0:3], v244 offset:52224\n"
"ds_read_b128 a[4:7], v244 offset:52288\n"
"v_mfma_f32_16x16x32_bf16 v[192:195], a[8:11], v[184:187], v[192:195]\n"
"v_exp_f32 v229, v229\n"
"v_mfma_f32_16x16x32_bf16 v[192:195], a[12:15], v[188:191], v[192:195]\n"
"ds_read_b128 a[8:11], v244 offset:52352\n"
"v_perm_b32 v206, v229, v228, s72\n"
"v_mfma_f32_16x16x32_bf16 v[196:199], a[28:31], v[188:191], -4.0\n"
"s_add_u32 m0, s55, 0\n"
"v_exp_f32 v230, v230\n"
"v_mfma_f32_16x16x32_bf16 v[196:199], a[24:27], v[184:187], v[196:199]\n"
"s_waitcnt lgkmcnt(3)\n"
"buffer_load_dwordx4 v249, s[44:47], s76 offen offset:0 nt lds\n"
"v_mfma_f32_16x16x32_bf16 v[196:199], a[20:23], v[180:183], v[196:199]\n"
"v_exp_f32 v231, v231\n"
"v_mfma_f32_16x16x32_bf16 v[196:199], a[16:19], v[176:179], v[196:199]\n"
"ds_read_b128 a[12:15], v244 offset:52416\n"
"v_perm_b32 v207, v231, v230, s72\n"
"v_mfma_f32_16x16x32_bf16 v[224:227], a[32:35], v[176:179], -4.0\n"
"v_exp_f32 v192, v192\n"
"v_mfma_f32_16x16x32_bf16 v[224:227], a[36:39], v[180:183], v[224:227]\n"
"ds_read_b128 a[28:31], v244 offset:56768\n"
"ds_read_b128 a[24:27], v244 offset:56704\n"
"v_mfma_f32_16x16x32_bf16 v[224:227], a[40:43], v[184:187], v[224:227]\n"
"v_exp_f32 v193, v193\n"
"v_mfma_f32_16x16x32_bf16 v[224:227], a[44:47], v[188:191], v[224:227]\n"
"ds_read_b128 a[20:23], v244 offset:56640\n"
"v_perm_b32 v208, v193, v192, s72\n"
"v_mfma_f32_16x16x32_bf16 v[228:231], a[60:63], v[188:191], -4.0\n"
"s_add_u32 m0, s55, 8\n"
"v_exp_f32 v194, v194\n"
"v_mfma_f32_16x16x32_bf16 v[228:231], a[56:59], v[184:187], v[228:231]\n"
"buffer_load_dwordx4 v249, s[44:47], s76 offen offset:1024 nt lds\n"
"v_mfma_f32_16x16x32_bf16 v[228:231], a[52:55], v[180:183], v[228:231]\n"
"v_exp_f32 v195, v195\n"
"v_mfma_f32_16x16x32_bf16 v[228:231], a[48:51], v[176:179], v[228:231]\n"
"ds_read_b128 a[16:19], v244 offset:56576\n"
"v_perm_b32 v209, v195, v194, s72\n"
"v_smfmac_f32_16x16x64_bf16 a[136:139], a[144:147], v[200:207], v251\n"
"ds_read_b128 a[32:35], v222 offset:52224\n"
"v_mfma_f32_16x16x32_bf16 v[64:67], a[64:67], v[200:203], v[64:67]\n"
"v_exp_f32 v196, v196\n"
"v_mfma_f32_16x16x32_bf16 v[64:67], a[68:71], v[204:207], v[64:67]\n"
"ds_read_b128 a[36:39], v222 offset:52288\n"
"ds_read_b128 a[40:43], v222 offset:52352\n"
"v_mfma_f32_16x16x32_bf16 v[68:71], a[76:79], v[204:207], v[68:71]\n"
"v_exp_f32 v197, v197\n"
"v_mfma_f32_16x16x32_bf16 v[68:71], a[72:75], v[200:203], v[68:71]\n"
"ds_read_b128 a[44:47], v222 offset:52416\n"
"v_perm_b32 v210, v197, v196, s72\n"
"v_mfma_f32_16x16x32_bf16 v[72:75], a[80:83], v[200:203], v[72:75]\n"
"s_add_u32 m0, s55, 16\n"
"v_exp_f32 v198, v198\n"
"v_mfma_f32_16x16x32_bf16 v[72:75], a[84:87], v[204:207], v[72:75]\n"
"buffer_load_dwordx4 v249, s[44:47], s76 offen offset:2048 nt lds\n"
"v_mfma_f32_16x16x32_bf16 v[76:79], a[92:95], v[204:207], v[76:79]\n"
"v_exp_f32 v199, v199\n"
"v_mfma_f32_16x16x32_bf16 v[76:79], a[88:91], v[200:203], v[76:79]\n"
"ds_read_b128 a[60:63], v222 offset:56768\n"
"v_perm_b32 v211, v199, v198, s72\n"
"v_mfma_f32_16x16x32_bf16 v[80:83], a[96:99], v[200:203], v[80:83]\n"
"v_exp_f32 v224, v224\n"
"v_mfma_f32_16x16x32_bf16 v[80:83], a[100:103], v[204:207], v[80:83]\n"
"ds_read_b128 a[56:59], v222 offset:56704\n"
"ds_read_b128 a[52:55], v222 offset:56640\n"
"v_mfma_f32_16x16x32_bf16 v[84:87], a[108:111], v[204:207], v[84:87]\n"
"v_exp_f32 v225, v225\n"
"v_mfma_f32_16x16x32_bf16 v[84:87], a[104:107], v[200:203], v[84:87]\n"
"ds_read_b128 a[48:51], v222 offset:56576\n"
"v_perm_b32 v212, v225, v224, s72\n"
"v_mfma_f32_16x16x32_bf16 v[88:91], a[112:115], v[200:203], v[88:91]\n"
"s_add_u32 m0, s55, 24\n"
"v_exp_f32 v226, v226\n"
"v_mfma_f32_16x16x32_bf16 v[88:91], a[116:119], v[204:207], v[88:91]\n"
"buffer_load_dwordx4 v249, s[44:47], s76 offen offset:3072 nt lds\n"
"v_mfma_f32_16x16x32_bf16 v[92:95], a[124:127], v[204:207], v[92:95]\n"
"s_add_u32 s76, s76, 4096\n"
"v_exp_f32 v227, v227\n"
"v_mfma_f32_16x16x32_bf16 v[92:95], a[120:123], v[200:203], v[92:95]\n"
"s_waitcnt lgkmcnt(4)\n"
"v_perm_b32 v213, v227, v226, s72\n"
"v_mfma_f32_16x16x32_bf16 v[192:195], a[0:3], v[128:131], -4.0\n"
"v_exp_f32 v228, v228\n"
"v_mfma_f32_16x16x32_bf16 v[192:195], a[4:7], v[132:135], v[192:195]\n"
"v_mfma_f32_16x16x32_bf16 v[192:195], a[8:11], v[136:139], v[192:195]\n"
"v_exp_f32 v229, v229\n"
"v_mfma_f32_16x16x32_bf16 v[192:195], a[12:15], v[140:143], v[192:195]\n"
"v_perm_b32 v214, v229, v228, s72\n"
"v_mfma_f32_16x16x32_bf16 v[196:199], a[28:31], v[140:143], -4.0\n"
"s_add_u32 m0, s54, 16520\n"
"v_exp_f32 v230, v230\n"
"v_mfma_f32_16x16x32_bf16 v[196:199], a[24:27], v[136:139], v[196:199]\n"
"buffer_load_dwordx4 v248, s[40:43], s52 offen offset:1024 lds\n"
"v_mfma_f32_16x16x32_bf16 v[196:199], a[20:23], v[132:135], v[196:199]\n"
"v_exp_f32 v231, v231\n"
"v_mfma_f32_16x16x32_bf16 v[196:199], a[16:19], v[128:131], v[196:199]\n"
"v_perm_b32 v215, v231, v230, s72\n"
"v_mfma_f32_16x16x32_bf16 v[224:227], a[32:35], v[128:131], -4.0\n"
"v_exp_f32 v192, v192\n"
"v_mfma_f32_16x16x32_bf16 v[224:227], a[36:39], v[132:135], v[224:227]\n"
"v_mfma_f32_16x16x32_bf16 v[224:227], a[40:43], v[136:139], v[224:227]\n"
"v_exp_f32 v193, v193\n"
"v_mfma_f32_16x16x32_bf16 v[224:227], a[44:47], v[140:143], v[224:227]\n"
"s_waitcnt lgkmcnt(0)\n"
"v_perm_b32 v200, v193, v192, s72\n"
"v_mfma_f32_16x16x32_bf16 v[228:231], a[60:63], v[140:143], -4.0\n"
"s_add_u32 m0, s54, 18448\n"
"v_exp_f32 v194, v194\n"
"v_mfma_f32_16x16x32_bf16 v[228:231], a[56:59], v[136:139], v[228:231]\n"
"buffer_load_dwordx4 v248, s[40:43], s52 offen offset:128 lds\n"
"v_mfma_f32_16x16x32_bf16 v[228:231], a[52:55], v[132:135], v[228:231]\n"
"v_exp_f32 v195, v195\n"
"v_mfma_f32_16x16x32_bf16 v[228:231], a[48:51], v[128:131], v[228:231]\n"
"v_perm_b32 v201, v195, v194, s72\n"
"v_smfmac_f32_16x16x64_bf16 a[140:143], a[144:147], v[208:215], v251\n"
"v_mfma_f32_16x16x32_bf16 v[96:99], a[64:67], v[208:211], v[96:99]\n"
"v_exp_f32 v196, v196\n"
"v_mfma_f32_16x16x32_bf16 v[96:99], a[68:71], v[212:215], v[96:99]\n"
"ds_read_b64_tr_b16 a[64:65], v245 offset:49536\n"
"ds_read_b64_tr_b16 a[66:67], v245 offset:53664\n"
"v_mfma_f32_16x16x32_bf16 v[100:103], a[76:79], v[212:215], v[100:103]\n"
"v_exp_f32 v197, v197\n"
"v_mfma_f32_16x16x32_bf16 v[100:103], a[72:75], v[208:211], v[100:103]\n"
"ds_read_b64_tr_b16 a[68:69], v255 offset:49536\n"
"v_perm_b32 v202, v197, v196, s72\n"
"v_mfma_f32_16x16x32_bf16 v[104:107], a[80:83], v[208:211], v[104:107]\n"
"v_exp_f32 v198, v198\n"
"v_mfma_f32_16x16x32_bf16 v[104:107], a[84:87], v[212:215], v[104:107]\n"
"ds_read_b64_tr_b16 a[70:71], v255 offset:53664\n"
"ds_read_b64_tr_b16 a[76:77], v255 offset:49544\n"
"v_mfma_f32_16x16x32_bf16 v[108:111], a[92:95], v[212:215], v[108:111]\n"
"v_exp_f32 v199, v199\n"
"v_mfma_f32_16x16x32_bf16 v[108:111], a[88:91], v[208:211], v[108:111]\n"
"ds_read_b64_tr_b16 a[78:79], v255 offset:53672\n"
"v_perm_b32 v203, v199, v198, s72\n"
"v_mfma_f32_16x16x32_bf16 v[112:115], a[96:99], v[208:211], v[112:115]\n"
"v_exp_f32 v224, v224\n"
"v_mfma_f32_16x16x32_bf16 v[112:115], a[100:103], v[212:215], v[112:115]\n"
"ds_read_b64_tr_b16 a[72:73], v245 offset:49544\n"
"ds_read_b64_tr_b16 a[74:75], v245 offset:53672\n"
"v_mfma_f32_16x16x32_bf16 v[116:119], a[108:111], v[212:215], v[116:119]\n"
"v_exp_f32 v225, v225\n"
"v_mfma_f32_16x16x32_bf16 v[116:119], a[104:107], v[208:211], v[116:119]\n"
"ds_read_b64_tr_b16 a[80:81], v245 offset:49792\n"
"v_perm_b32 v204, v225, v224, s72\n"
"v_mfma_f32_16x16x32_bf16 v[120:123], a[112:115], v[208:211], v[120:123]\n"
"v_exp_f32 v226, v226\n"
"v_mfma_f32_16x16x32_bf16 v[120:123], a[116:119], v[212:215], v[120:123]\n"
"ds_read_b64_tr_b16 a[82:83], v245 offset:53920\n"
"ds_read_b64_tr_b16 a[84:85], v255 offset:49792\n"
"v_mfma_f32_16x16x32_bf16 v[124:127], a[124:127], v[212:215], v[124:127]\n"
"v_exp_f32 v227, v227\n"
"v_mfma_f32_16x16x32_bf16 v[124:127], a[120:123], v[208:211], v[124:127]\n"
"ds_read_b64_tr_b16 a[86:87], v255 offset:53920\n"
"v_perm_b32 v205, v227, v226, s72\n"
"v_mfma_f32_16x16x32_bf16 v[192:195], a[0:3], v[144:147], -4.0\n"
"v_exp_f32 v228, v228\n"
"v_mfma_f32_16x16x32_bf16 v[192:195], a[4:7], v[148:151], v[192:195]\n"
"ds_read_b64_tr_b16 a[92:93], v255 offset:49800\n"
"ds_read_b64_tr_b16 a[94:95], v255 offset:53928\n"
"v_mfma_f32_16x16x32_bf16 v[192:195], a[8:11], v[152:155], v[192:195]\n"
"v_exp_f32 v229, v229\n"
"v_mfma_f32_16x16x32_bf16 v[192:195], a[12:15], v[156:159], v[192:195]\n"
"ds_read_b64_tr_b16 a[88:89], v245 offset:49800\n"
"v_perm_b32 v206, v229, v228, s72\n"
"v_mfma_f32_16x16x32_bf16 v[196:199], a[28:31], v[156:159], -4.0\n"
"v_exp_f32 v230, v230\n"
"v_mfma_f32_16x16x32_bf16 v[196:199], a[24:27], v[152:155], v[196:199]\n"
"ds_read_b64_tr_b16 a[90:91], v245 offset:53928\n"
"ds_read_b64_tr_b16 a[96:97], v245 offset:51600\n"
"v_mfma_f32_16x16x32_bf16 v[196:199], a[20:23], v[148:151], v[196:199]\n"
"v_exp_f32 v231, v231\n"
"v_mfma_f32_16x16x32_bf16 v[196:199], a[16:19], v[144:147], v[196:199]\n"
"ds_read_b64_tr_b16 a[98:99], v245 offset:55728\n"
"v_perm_b32 v207, v231, v230, s72\n"
"v_mfma_f32_16x16x32_bf16 v[224:227], a[32:35], v[144:147], -4.0\n"
"v_exp_f32 v192, v192\n"
"v_mfma_f32_16x16x32_bf16 v[224:227], a[36:39], v[148:151], v[224:227]\n"
"ds_read_b64_tr_b16 a[100:101], v255 offset:51600\n"
"ds_read_b64_tr_b16 a[102:103], v255 offset:55728\n"
"v_mfma_f32_16x16x32_bf16 v[224:227], a[40:43], v[152:155], v[224:227]\n"
"v_exp_f32 v193, v193\n"
"v_mfma_f32_16x16x32_bf16 v[224:227], a[44:47], v[156:159], v[224:227]\n"
"ds_read_b64_tr_b16 a[108:109], v255 offset:51608\n"
"v_perm_b32 v208, v193, v192, s72\n"
"v_mfma_f32_16x16x32_bf16 v[228:231], a[60:63], v[156:159], -4.0\n"
"v_exp_f32 v194, v194\n"
"v_mfma_f32_16x16x32_bf16 v[228:231], a[56:59], v[152:155], v[228:231]\n"
"ds_read_b64_tr_b16 a[110:111], v255 offset:55736\n"
"ds_read_b64_tr_b16 a[104:105], v245 offset:51608\n"
"v_mfma_f32_16x16x32_bf16 v[228:231], a[52:55], v[148:151], v[228:231]\n"
"v_exp_f32 v195, v195\n"
"v_mfma_f32_16x16x32_bf16 v[228:231], a[48:51], v[144:147], v[228:231]\n"
"ds_read_b64_tr_b16 a[106:107], v245 offset:55736\n"
"v_perm_b32 v209, v195, v194, s72\n"
"v_smfmac_f32_16x16x64_bf16 a[128:131], a[144:147], v[200:207], v251\n"
"s_waitcnt lgkmcnt(7)\n"
"ds_read_b64_tr_b16 a[112:113], v245 offset:51856\n"
"v_mfma_f32_16x16x32_bf16 v[0:3], a[64:67], v[200:203], v[0:3]\n"
"v_exp_f32 v196, v196\n"
"v_mfma_f32_16x16x32_bf16 v[0:3], a[68:71], v[204:207], v[0:3]\n"
"ds_read_b64_tr_b16 a[114:115], v245 offset:55984\n"
"ds_read_b64_tr_b16 a[116:117], v255 offset:51856\n"
"v_mfma_f32_16x16x32_bf16 v[4:7], a[76:79], v[204:207], v[4:7]\n"
"v_exp_f32 v197, v197\n"
"v_mfma_f32_16x16x32_bf16 v[4:7], a[72:75], v[200:203], v[4:7]\n"
"ds_read_b64_tr_b16 a[118:119], v255 offset:55984\n"
"v_perm_b32 v210, v197, v196, s72\n"
"v_mfma_f32_16x16x32_bf16 v[8:11], a[80:83], v[200:203], v[8:11]\n"
"v_exp_f32 v198, v198\n"
"v_mfma_f32_16x16x32_bf16 v[8:11], a[84:87], v[204:207], v[8:11]\n"
"ds_read_b64_tr_b16 a[124:125], v255 offset:51864\n"
"ds_read_b64_tr_b16 a[126:127], v255 offset:55992\n"
"v_mfma_f32_16x16x32_bf16 v[12:15], a[92:95], v[204:207], v[12:15]\n"
"v_exp_f32 v199, v199\n"
"v_mfma_f32_16x16x32_bf16 v[12:15], a[88:91], v[200:203], v[12:15]\n"
"s_waitcnt lgkmcnt(7)\n"
"ds_read_b64_tr_b16 a[120:121], v245 offset:51864\n"
"v_perm_b32 v211, v199, v198, s72\n"
"v_mfma_f32_16x16x32_bf16 v[16:19], a[96:99], v[200:203], v[16:19]\n"
"v_exp_f32 v224, v224\n"
"v_mfma_f32_16x16x32_bf16 v[16:19], a[100:103], v[204:207], v[16:19]\n"
"ds_read_b64_tr_b16 a[122:123], v245 offset:55992\n"
"v_mfma_f32_16x16x32_bf16 v[20:23], a[108:111], v[204:207], v[20:23]\n"
"s_waitcnt lgkmcnt(7)\n"
"v_exp_f32 v225, v225\n"
"v_mfma_f32_16x16x32_bf16 v[20:23], a[104:107], v[200:203], v[20:23]\n"
"s_waitcnt lgkmcnt(6)\n"
"v_perm_b32 v212, v225, v224, s72\n"
"v_mfma_f32_16x16x32_bf16 v[24:27], a[112:115], v[200:203], v[24:27]\n"
"s_waitcnt lgkmcnt(4)\n"
"v_exp_f32 v226, v226\n"
"v_mfma_f32_16x16x32_bf16 v[24:27], a[116:119], v[204:207], v[24:27]\n"
"s_waitcnt lgkmcnt(2)\n"
"v_mfma_f32_16x16x32_bf16 v[28:31], a[124:127], v[204:207], v[28:31]\n"
"s_waitcnt lgkmcnt(0)\n"
"v_exp_f32 v227, v227\n"
"v_mfma_f32_16x16x32_bf16 v[28:31], a[120:123], v[200:203], v[28:31]\n"
"buffer_store_dwordx4 a[216:219], v250, s[48:51], s84 offen offset:0 sc1\n"
"v_perm_b32 v213, v227, v226, s72\n"
"v_mfma_f32_16x16x32_bf16 v[192:195], a[0:3], v[160:163], -4.0\n"
"v_exp_f32 v228, v228\n"
"v_mfma_f32_16x16x32_bf16 v[192:195], a[4:7], v[164:167], v[192:195]\n"
"v_mfma_f32_16x16x32_bf16 v[192:195], a[8:11], v[168:171], v[192:195]\n"
"v_exp_f32 v229, v229\n"
"v_mfma_f32_16x16x32_bf16 v[192:195], a[12:15], v[172:175], v[192:195]\n"
"v_perm_b32 v214, v229, v228, s72\n"
"v_mfma_f32_16x16x32_bf16 v[196:199], a[28:31], v[172:175], -4.0\n"
"s_add_u32 m0, s54, 18456\n"
"v_exp_f32 v230, v230\n"
"v_mfma_f32_16x16x32_bf16 v[196:199], a[24:27], v[168:171], v[196:199]\n"
"buffer_load_dwordx4 v248, s[40:43], s52 offen offset:1152 lds\n"
"v_mfma_f32_16x16x32_bf16 v[196:199], a[20:23], v[164:167], v[196:199]\n"
"s_add_u32 s52, s52, 16384\n"
"v_exp_f32 v231, v231\n"
"v_mfma_f32_16x16x32_bf16 v[196:199], a[16:19], v[160:163], v[196:199]\n"
"v_perm_b32 v215, v231, v230, s72\n"
"v_mfma_f32_16x16x32_bf16 v[224:227], a[32:35], v[160:163], -4.0\n"
"v_exp_f32 v192, v192\n"
"v_mfma_f32_16x16x32_bf16 v[224:227], a[36:39], v[164:167], v[224:227]\n"
"v_mfma_f32_16x16x32_bf16 v[224:227], a[40:43], v[168:171], v[224:227]\n"
"v_exp_f32 v193, v193\n"
"v_mfma_f32_16x16x32_bf16 v[224:227], a[44:47], v[172:175], v[224:227]\n"
"v_perm_b32 v200, v193, v192, s72\n"
"v_mfma_f32_16x16x32_bf16 v[228:231], a[60:63], v[172:175], -4.0\n"
"s_add_u32 m0, s53, 34816\n"
"v_exp_f32 v194, v194\n"
"v_mfma_f32_16x16x32_bf16 v[228:231], a[56:59], v[168:171], v[228:231]\n"
"buffer_load_dwordx4 v247, s[36:39], s52 offen offset:0 lds\n"
"v_mfma_f32_16x16x32_bf16 v[228:231], a[52:55], v[164:167], v[228:231]\n"
"v_exp_f32 v195, v195\n"
"v_mfma_f32_16x16x32_bf16 v[228:231], a[48:51], v[160:163], v[228:231]\n"
"s_waitcnt vmcnt(13) lgkmcnt(0)\n"
"v_perm_b32 v201, v195, v194, s72\n"
"v_smfmac_f32_16x16x64_bf16 a[132:135], a[144:147], v[208:215], v251\n"
"v_mfma_f32_16x16x32_bf16 v[32:35], a[64:67], v[208:211], v[32:35]\n"
"v_exp_f32 v196, v196\n"
"v_mfma_f32_16x16x32_bf16 v[32:35], a[68:71], v[212:215], v[32:35]\n"
"s_barrier\n"
"v_mfma_f32_16x16x32_bf16 v[36:39], a[76:79], v[212:215], v[36:39]\n"
"v_exp_f32 v197, v197\n"
"v_mfma_f32_16x16x32_bf16 v[36:39], a[72:75], v[208:211], v[36:39]\n"
"v_perm_b32 v202, v197, v196, s72\n"
"v_mfma_f32_16x16x32_bf16 v[40:43], a[80:83], v[208:211], v[40:43]\n"
"s_add_u32 m0, s53, 34880\n"
"v_exp_f32 v198, v198\n"
"v_mfma_f32_16x16x32_bf16 v[40:43], a[84:87], v[212:215], v[40:43]\n"
"buffer_load_dwordx4 v247, s[36:39], s52 offen offset:1024 lds\n"
"v_mfma_f32_16x16x32_bf16 v[44:47], a[92:95], v[212:215], v[44:47]\n"
"v_exp_f32 v199, v199\n"
"v_mfma_f32_16x16x32_bf16 v[44:47], a[88:91], v[208:211], v[44:47]\n"
"v_perm_b32 v203, v199, v198, s72\n"
"v_mfma_f32_16x16x32_bf16 v[48:51], a[96:99], v[208:211], v[48:51]\n"
"v_exp_f32 v224, v224\n"
"v_mfma_f32_16x16x32_bf16 v[48:51], a[100:103], v[212:215], v[48:51]\n"
"v_mfma_f32_16x16x32_bf16 v[52:55], a[108:111], v[212:215], v[52:55]\n"
"v_exp_f32 v225, v225\n"
"v_mfma_f32_16x16x32_bf16 v[52:55], a[104:107], v[208:211], v[52:55]\n"
"v_perm_b32 v204, v225, v224, s72\n"
"v_mfma_f32_16x16x32_bf16 v[56:59], a[112:115], v[208:211], v[56:59]\n"
"s_add_u32 m0, s53, 34944\n"
"v_exp_f32 v226, v226\n"
"v_mfma_f32_16x16x32_bf16 v[56:59], a[116:119], v[212:215], v[56:59]\n"
"buffer_load_dwordx4 v247, s[36:39], s52 offen offset:2048 lds\n"
"v_mfma_f32_16x16x32_bf16 v[60:63], a[124:127], v[212:215], v[60:63]\n"
"v_exp_f32 v227, v227\n"
"v_mfma_f32_16x16x32_bf16 v[60:63], a[120:123], v[208:211], v[60:63]\n"
"v_perm_b32 v205, v227, v226, s72\n"
"v_mfma_f32_16x16x32_bf16 v[192:195], a[0:3], v[176:179], -4.0\n"
"v_exp_f32 v228, v228\n"
"v_mfma_f32_16x16x32_bf16 v[192:195], a[4:7], v[180:183], v[192:195]\n"
"ds_read_b128 a[0:3], v244 offset:0\n"
"ds_read_b128 a[4:7], v244 offset:64\n"
"v_mfma_f32_16x16x32_bf16 v[192:195], a[8:11], v[184:187], v[192:195]\n"
"v_exp_f32 v229, v229\n"
"v_mfma_f32_16x16x32_bf16 v[192:195], a[12:15], v[188:191], v[192:195]\n"
"Lhop0_5_%=:\n s_cbranch_execz Lback0_%=\n"
"ds_read_b128 a[8:11], v244 offset:128\n"
"v_perm_b32 v206, v229, v228, s72\n"
"v_mfma_f32_16x16x32_bf16 v[196:199], a[28:31], v[188:191], -4.0\n"
"s_add_u32 m0, s53, 35008\n"
"v_exp_f32 v230, v230\n"
"v_mfma_f32_16x16x32_bf16 v[196:199], a[24:27], v[184:187], v[196:199]\n"
"buffer_load_dwordx4 v247, s[36:39], s52 offen offset:3072 lds\n"
"v_mfma_f32_16x16x32_bf16 v[196:199], a[20:23], v[180:183], v[196:199]\n"
"v_exp_f32 v231, v231\n"
"v_mfma_f32_16x16x32_bf16 v[196:199], a[16:19], v[176:179], v[196:199]\n"
"buffer_store_dwordx4 a[220:223], v250, s[48:51], s84 offen offset:1024 sc1\n"
"v_perm_b32 v207, v231, v230, s72\n"
"v_mfma_f32_16x16x32_bf16 v[224:227], a[32:35], v[176:179], -4.0\n"
"v_exp_f32 v192, v192\n"
"v_mfma_f32_16x16x32_bf16 v[224:227], a[36:39], v[180:183], v[224:227]\n"
"ds_read_b128 a[12:15], v244 offset:192\n"
"ds_read_b128 a[28:31], v244 offset:4544\n"
"v_mfma_f32_16x16x32_bf16 v[224:227], a[40:43], v[184:187], v[224:227]\n"
"v_exp_f32 v193, v193\n"
"v_mfma_f32_16x16x32_bf16 v[224:227], a[44:47], v[188:191], v[224:227]\n"
"ds_read_b128 a[24:27], v244 offset:4480\n"
"v_perm_b32 v208, v193, v192, s72\n"
"v_mfma_f32_16x16x32_bf16 v[228:231], a[60:63], v[188:191], -4.0\n"
"s_add_u32 m0, s54, 33024\n"
"v_exp_f32 v194, v194\n"
"v_mfma_f32_16x16x32_bf16 v[228:231], a[56:59], v[184:187], v[228:231]\n"
"buffer_load_dwordx4 v248, s[40:43], s52 offen offset:0 lds\n"
"v_mfma_f32_16x16x32_bf16 v[228:231], a[52:55], v[180:183], v[228:231]\n"
"v_exp_f32 v195, v195\n"
"v_mfma_f32_16x16x32_bf16 v[228:231], a[48:51], v[176:179], v[228:231]\n"
"ds_read_b128 a[20:23], v244 offset:4416\n"
"v_perm_b32 v209, v195, v194, s72\n"
"v_smfmac_f32_16x16x64_bf16 a[136:139], a[144:147], v[200:207], v251\n"
"ds_read_b128 a[16:19], v244 offset:4352\n"
"v_mfma_f32_16x16x32_bf16 v[64:67], a[64:67], v[200:203], v[64:67]\n"
"v_exp_f32 v196, v196\n"
"v_mfma_f32_16x16x32_bf16 v[64:67], a[68:71], v[204:207], v[64:67]\n"
"ds_read_b128 a[32:35], v222 offset:0\n"
"ds_read_b128 a[36:39], v222 offset:64\n"
"v_mfma_f32_16x16x32_bf16 v[68:71], a[76:79], v[204:207], v[68:71]\n"
"v_exp_f32 v197, v197\n"
"v_mfma_f32_16x16x32_bf16 v[68:71], a[72:75], v[200:203], v[68:71]\n"
"ds_read_b128 a[40:43], v222 offset:128\n"
"v_perm_b32 v210, v197, v196, s72\n"
"v_mfma_f32_16x16x32_bf16 v[72:75], a[80:83], v[200:203], v[72:75]\n"
"s_add_u32 m0, s54, 33032\n"
"v_exp_f32 v198, v198\n"
"v_mfma_f32_16x16x32_bf16 v[72:75], a[84:87], v[204:207], v[72:75]\n"
"buffer_load_dwordx4 v248, s[40:43], s52 offen offset:1024 lds\n"
"v_mfma_f32_16x16x32_bf16 v[76:79], a[92:95], v[204:207], v[76:79]\n"
"v_exp_f32 v199, v199\n"
"v_mfma_f32_16x16x32_bf16 v[76:79], a[88:91], v[200:203], v[76:79]\n"
"ds_read_b128 a[44:47], v222 offset:192\n"
"v_perm_b32 v211, v199, v198, s72\n"
"v_mfma_f32_16x16x32_bf16 v[80:83], a[96:99], v[200:203], v[80:83]\n"
"v_exp_f32 v224, v224\n"
"v_mfma_f32_16x16x32_bf16 v[80:83], a[100:103], v[204:207], v[80:83]\n"
"ds_read_b128 a[60:63], v222 offset:4544\n"
"ds_read_b128 a[56:59], v222 offset:4480\n"
"v_mfma_f32_16x16x32_bf16 v[84:87], a[108:111], v[204:207], v[84:87]\n"
"v_exp_f32 v225, v225\n"
"v_mfma_f32_16x16x32_bf16 v[84:87], a[104:107], v[200:203], v[84:87]\n"
"ds_read_b128 a[52:55], v222 offset:4416\n"
"v_perm_b32 v212, v225, v224, s72\n"
"v_mfma_f32_16x16x32_bf16 v[88:91], a[112:115], v[200:203], v[88:91]\n"
"s_add_u32 m0, s54, 34960\n"
"v_exp_f32 v226, v226\n"
"v_mfma_f32_16x16x32_bf16 v[88:91], a[116:119], v[204:207], v[88:91]\n"
"buffer_load_dwordx4 v248, s[40:43], s52 offen offset:128 lds\n"
"v_mfma_f32_16x16x32_bf16 v[92:95], a[124:127], v[204:207], v[92:95]\n"
"v_exp_f32 v227, v227\n"
"v_mfma_f32_16x16x32_bf16 v[92:95], a[120:123], v[200:203], v[92:95]\n"
"s_waitcnt lgkmcnt(4)\n"
"ds_read_b128 a[48:51], v222 offset:4352\n"
"v_perm_b32 v213, v227, v226, s72\n"
"v_mfma_f32_16x16x32_bf16 v[192:195], a[0:3], v[128:131], -4.0\n"
"v_exp_f32 v228, v228\n"
"v_mfma_f32_16x16x32_bf16 v[192:195], a[4:7], v[132:135], v[192:195]\n"
"v_mfma_f32_16x16x32_bf16 v[192:195], a[8:11], v[136:139], v[192:195]\n"
"v_exp_f32 v229, v229\n"
"v_mfma_f32_16x16x32_bf16 v[192:195], a[12:15], v[140:143], v[192:195]\n"
"v_perm_b32 v214, v229, v228, s72\n"
"v_mfma_f32_16x16x32_bf16 v[196:199], a[28:31], v[140:143], -4.0\n"
"s_add_u32 m0, s54, 34968\n"
"v_exp_f32 v230, v230\n"
"v_mfma_f32_16x16x32_bf16 v[196:199], a[24:27], v[136:139], v[196:199]\n"
"buffer_load_dwordx4 v248, s[40:43], s52 offen offset:1152 lds\n"
"v_mfma_f32_16x16x32_bf16 v[196:199], a[20:23], v[132:135], v[196:199]\n"
"s_add_u32 s52, s52, 16384\n"
"v_exp_f32 v231, v231\n"
"v_mfma_f32_16x16x32_bf16 v[196:199], a[16:19], v[128:131], v[196:199]\n"
"v_perm_b32 v215, v231, v230, s72\n"
"v_mfma_f32_16x16x32_bf16 v[224:227], a[32:35], v[128:131], -4.0\n"
"v_exp_f32 v192, v192\n"
"v_mfma_f32_16x16x32_bf16 v[224:227], a[36:39], v[132:135], v[224:227]\n"
"v_mfma_f32_16x16x32_bf16 v[224:227], a[40:43], v[136:139], v[224:227]\n"
"s_waitcnt lgkmcnt(0)\n"
"v_exp_f32 v193, v193\n"
"v_mfma_f32_16x16x32_bf16 v[224:227], a[44:47], v[140:143], v[224:227]\n"
"v_perm_b32 v200, v193, v192, s72\n"
"v_mfma_f32_16x16x32_bf16 v[228:231], a[60:63], v[140:143], -4.0\n"
"v_exp_f32 v194, v194\n"
"v_mfma_f32_16x16x32_bf16 v[228:231], a[56:59], v[136:139], v[228:231]\n"
"v_mfma_f32_16x16x32_bf16 v[228:231], a[52:55], v[132:135], v[228:231]\n"
"v_exp_f32 v195, v195\n"
"v_mfma_f32_16x16x32_bf16 v[228:231], a[48:51], v[128:131], v[228:231]\n"
"buffer_store_dwordx4 a[224:227], v250, s[48:51], s84 offen offset:128 sc1\n"
"v_perm_b32 v201, v195, v194, s72\n"
"v_smfmac_f32_16x16x64_bf16 a[140:143], a[144:147], v[208:215], v251\n"
"v_mfma_f32_16x16x32_bf16 v[96:99], a[64:67], v[208:211], v[96:99]\n"
"v_exp_f32 v196, v196\n"
"v_mfma_f32_16x16x32_bf16 v[96:99], a[68:71], v[212:215], v[96:99]\n"
"ds_read_b64_tr_b16 a[64:65], v245 offset:0\n"
"ds_read_b64_tr_b16 a[66:67], v245 offset:4128\n"
"v_mfma_f32_16x16x32_bf16 v[100:103], a[76:79], v[212:215], v[100:103]\n"
"v_exp_f32 v197, v197\n"
"v_mfma_f32_16x16x32_bf16 v[100:103], a[72:75], v[208:211], v[100:103]\n"
"ds_read_b64_tr_b16 a[68:69], v255 offset:0\n"
"v_perm_b32 v202, v197, v196, s72\n"
"v_mfma_f32_16x16x32_bf16 v[104:107], a[80:83], v[208:211], v[104:107]\n"
"v_exp_f32 v198, v198\n"
"v_mfma_f32_16x16x32_bf16 v[104:107], a[84:87], v[212:215], v[104:107]\n"
"ds_read_b64_tr_b16 a[70:71], v255 offset:4128\n"
"ds_read_b64_tr_b16 a[76:77], v255 offset:8\n"
"v_mfma_f32_16x16x32_bf16 v[108:111], a[92:95], v[212:215], v[108:111]\n"
"v_exp_f32 v199, v199\n"
"v_mfma_f32_16x16x32_bf16 v[108:111], a[88:91], v[208:211], v[108:111]\n"
"ds_read_b64_tr_b16 a[78:79], v255 offset:4136\n"
"v_perm_b32 v203, v199, v198, s72\n"
"v_mfma_f32_16x16x32_bf16 v[112:115], a[96:99], v[208:211], v[112:115]\n"
"v_exp_f32 v224, v224\n"
"v_mfma_f32_16x16x32_bf16 v[112:115], a[100:103], v[212:215], v[112:115]\n"
"ds_read_b64_tr_b16 a[72:73], v245 offset:8\n"
"ds_read_b64_tr_b16 a[74:75], v245 offset:4136\n"
"v_mfma_f32_16x16x32_bf16 v[116:119], a[108:111], v[212:215], v[116:119]\n"
"v_exp_f32 v225, v225\n"
"v_mfma_f32_16x16x32_bf16 v[116:119], a[104:107], v[208:211], v[116:119]\n"
"ds_read_b64_tr_b16 a[80:81], v245 offset:256\n"
"v_perm_b32 v204, v225, v224, s72\n"
"v_mfma_f32_16x16x32_bf16 v[120:123], a[112:115], v[208:211], v[120:123]\n"
"v_exp_f32 v226, v226\n"
"v_mfma_f32_16x16x32_bf16 v[120:123], a[116:119], v[212:215], v[120:123]\n"
"ds_read_b64_tr_b16 a[82:83], v245 offset:4384\n"
"ds_read_b64_tr_b16 a[84:85], v255 offset:256\n"
"v_mfma_f32_16x16x32_bf16 v[124:127], a[124:127], v[212:215], v[124:127]\n"
"v_exp_f32 v227, v227\n"
"v_mfma_f32_16x16x32_bf16 v[124:127], a[120:123], v[208:211], v[124:127]\n"
"ds_read_b64_tr_b16 a[86:87], v255 offset:4384\n"
"v_perm_b32 v205, v227, v226, s72\n"
"v_mfma_f32_16x16x32_bf16 v[192:195], a[0:3], v[144:147], -4.0\n"
"v_exp_f32 v228, v228\n"
"v_mfma_f32_16x16x32_bf16 v[192:195], a[4:7], v[148:151], v[192:195]\n"
"ds_read_b64_tr_b16 a[92:93], v255 offset:264\n"
"ds_read_b64_tr_b16 a[94:95], v255 offset:4392\n"
"v_mfma_f32_16x16x32_bf16 v[192:195], a[8:11], v[152:155], v[192:195]\n"
"v_exp_f32 v229, v229\n"
"v_mfma_f32_16x16x32_bf16 v[192:195], a[12:15], v[156:159], v[192:195]\n"
"ds_read_b64_tr_b16 a[88:89], v245 offset:264\n"
"v_perm_b32 v206, v229, v228, s72\n"
"v_mfma_f32_16x16x32_bf16 v[196:199], a[28:31], v[156:159], -4.0\n"
"v_exp_f32 v230, v230\n"
"v_mfma_f32_16x16x32_bf16 v[196:199], a[24:27], v[152:155], v[196:199]\n"
"ds_read_b64_tr_b16 a[90:91], v245 offset:4392\n"
"ds_read_b64_tr_b16 a[96:97], v245 offset:2064\n"
"v_mfma_f32_16x16x32_bf16 v[196:199], a[20:23], v[148:151], v[196:199]\n"
"v_exp_f32 v231, v231\n"
"v_mfma_f32_16x16x32_bf16 v[196:199], a[16:19], v[144:147], v[196:199]\n"
"buffer_store_dwordx4 a[228:231], v250, s[48:51], s84 offen offset:1152 sc1\n"
"v_perm_b32 v207, v231, v230, s72\n"
"v_mfma_f32_16x16x32_bf16 v[224:227], a[32:35], v[144:147], -4.0\n"
"s_mov_b32 s84, s85\n"
"v_exp_f32 v192, v192\n"
"v_mfma_f32_16x16x32_bf16 v[224:227], a[36:39], v[148:151], v[224:227]\n"
"ds_read_b64_tr_b16 a[98:99], v245 offset:6192\n"
"ds_read_b64_tr_b16 a[100:101], v255 offset:2064\n"
"v_mfma_f32_16x16x32_bf16 v[224:227], a[40:43], v[152:155], v[224:227]\n"
"v_exp_f32 v193, v193\n"
"v_mfma_f32_16x16x32_bf16 v[224:227], a[44:47], v[156:159], v[224:227]\n"
"ds_read_b64_tr_b16 a[102:103], v255 offset:6192\n"
"v_perm_b32 v208, v193, v192, s72\n"
"v_mfma_f32_16x16x32_bf16 v[228:231], a[60:63], v[156:159], -4.0\n"
"v_exp_f32 v194, v194\n"
"v_mfma_f32_16x16x32_bf16 v[228:231], a[56:59], v[152:155], v[228:231]\n"
"ds_read_b64_tr_b16 a[108:109], v255 offset:2072\n"
"ds_read_b64_tr_b16 a[110:111], v255 offset:6200\n"
"v_mfma_f32_16x16x32_bf16 v[228:231], a[52:55], v[148:151], v[228:231]\n"
"v_exp_f32 v195, v195\n"
"v_mfma_f32_16x16x32_bf16 v[228:231], a[48:51], v[144:147], v[228:231]\n"
"ds_read_b64_tr_b16 a[104:105], v245 offset:2072\n"
"v_perm_b32 v209, v195, v194, s72\n"
"v_smfmac_f32_16x16x64_bf16 a[128:131], a[144:147], v[200:207], v251\n"
"s_waitcnt lgkmcnt(6)\n"
"ds_read_b64_tr_b16 a[106:107], v245 offset:6200\n"
"v_mfma_f32_16x16x32_bf16 v[0:3], a[64:67], v[200:203], v[0:3]\n"
"v_exp_f32 v196, v196\n"
"v_mfma_f32_16x16x32_bf16 v[0:3], a[68:71], v[204:207], v[0:3]\n"
"ds_read_b64_tr_b16 a[112:113], v245 offset:2320\n"
"ds_read_b64_tr_b16 a[114:115], v245 offset:6448\n"
"v_mfma_f32_16x16x32_bf16 v[4:7], a[76:79], v[204:207], v[4:7]\n"
"v_exp_f32 v197, v197\n"
"v_mfma_f32_16x16x32_bf16 v[4:7], a[72:75], v[200:203], v[4:7]\n"
"ds_read_b64_tr_b16 a[116:117], v255 offset:2320\n"
"v_perm_b32 v210, v197, v196, s72\n"
"v_mfma_f32_16x16x32_bf16 v[8:11], a[80:83], v[200:203], v[8:11]\n"
"v_exp_f32 v198, v198\n"
"v_mfma_f32_16x16x32_bf16 v[8:11], a[84:87], v[204:207], v[8:11]\n"
"ds_read_b64_tr_b16 a[118:119], v255 offset:6448\n"
"ds_read_b64_tr_b16 a[124:125], v255 offset:2328\n"
"v_mfma_f32_16x16x32_bf16 v[12:15], a[92:95], v[204:207], v[12:15]\n"
"v_exp_f32 v199, v199\n"
"v_mfma_f32_16x16x32_bf16 v[12:15], a[88:91], v[200:203], v[12:15]\n"
"s_waitcnt vmcnt(11) lgkmcnt(7)\n"
"ds_read_b64_tr_b16 a[126:127], v255 offset:6456\n"
"v_perm_b32 v211, v199, v198, s72\n"
"v_mfma_f32_16x16x32_bf16 v[16:19], a[96:99], v[200:203], v[16:19]\n"
"v_exp_f32 v224, v224\n"
"v_mfma_f32_16x16x32_bf16 v[16:19], a[100:103], v[204:207], v[16:19]\n"
"ds_read_b64_tr_b16 a[120:121], v245 offset:2328\n"
"ds_read_b64_tr_b16 a[122:123], v245 offset:6456\n"
"v_mfma_f32_16x16x32_bf16 v[20:23], a[108:111], v[204:207], v[20:23]\n"
"s_waitcnt lgkmcnt(8)\n"
"v_exp_f32 v225, v225\n"
"v_mfma_f32_16x16x32_bf16 v[20:23], a[104:107], v[200:203], v[20:23]\n"
"s_waitcnt lgkmcnt(4)\n"
"ds_read2_b64 a[184:187], v246 offset0:0 offset1:2\n"
"v_perm_b32 v212, v225, v224, s72\n"
"v_mfma_f32_16x16x32_bf16 v[24:27], a[112:115], v[200:203], v[24:27]\n"
"s_add_u32 m0, s53, 52224\n"
"v_exp_f32 v226, v226\n"
"v_mfma_f32_16x16x32_bf16 v[24:27], a[116:119], v[204:207], v[24:27]\n"
"s_waitcnt lgkmcnt(3)\n"
"buffer_load_dwordx4 v247, s[36:39], s52 offen offset:0 lds\n"
"v_mfma_f32_16x16x32_bf16 v[28:31], a[124:127], v[204:207], v[28:31]\n"
"s_waitcnt lgkmcnt(1)\n"
"v_exp_f32 v227, v227\n"
"v_mfma_f32_16x16x32_bf16 v[28:31], a[120:123], v[200:203], v[28:31]\n"
"ds_read2_b64 a[188:191], v246 offset0:1 offset1:3\n"
"v_perm_b32 v213, v227, v226, s72\n"
"v_mfma_f32_16x16x32_bf16 v[192:195], a[0:3], v[160:163], -4.0\n"
"v_exp_f32 v228, v228\n"
"v_mfma_f32_16x16x32_bf16 v[192:195], a[4:7], v[164:167], v[192:195]\n"
"ds_read2_b64 a[192:195], v246 offset0:32 offset1:34\n"
"ds_read2_b64 a[196:199], v246 offset0:33 offset1:35\n"
"v_mfma_f32_16x16x32_bf16 v[192:195], a[8:11], v[168:171], v[192:195]\n"
"v_exp_f32 v229, v229\n"
"v_mfma_f32_16x16x32_bf16 v[192:195], a[12:15], v[172:175], v[192:195]\n"
"ds_read2_b64 a[200:203], v246 offset0:64 offset1:66\n"
"v_perm_b32 v214, v229, v228, s72\n"
"v_mfma_f32_16x16x32_bf16 v[196:199], a[28:31], v[172:175], -4.0\n"
"s_add_u32 m0, s53, 52288\n"
"v_exp_f32 v230, v230\n"
"v_mfma_f32_16x16x32_bf16 v[196:199], a[24:27], v[168:171], v[196:199]\n"
"buffer_load_dwordx4 v247, s[36:39], s52 offen offset:1024 lds\n"
"v_mfma_f32_16x16x32_bf16 v[196:199], a[20:23], v[164:167], v[196:199]\n"
"v_exp_f32 v231, v231\n"
"v_mfma_f32_16x16x32_bf16 v[196:199], a[16:19], v[160:163], v[196:199]\n"
"ds_read2_b64 a[204:207], v246 offset0:65 offset1:67\n"
"v_perm_b32 v215, v231, v230, s72\n"
"v_mfma_f32_16x16x32_bf16 v[224:227], a[32:35], v[160:163], -4.0\n"
"v_exp_f32 v192, v192\n"
"v_mfma_f32_16x16x32_bf16 v[224:227], a[36:39], v[164:167], v[224:227]\n"
"ds_read2_b64 a[208:211], v246 offset0:96 offset1:98\n"
"ds_read2_b64 a[212:215], v246 offset0:97 offset1:99\n"
"v_mfma_f32_16x16x32_bf16 v[224:227], a[40:43], v[168:171], v[224:227]\n"
"v_exp_f32 v193, v193\n"
"v_mfma_f32_16x16x32_bf16 v[224:227], a[44:47], v[172:175], v[224:227]\n"
"v_perm_b32 v200, v193, v192, s72\n"
"v_mfma_f32_16x16x32_bf16 v[228:231], a[60:63], v[172:175], -4.0\n"
"s_add_u32 m0, s53, 52352\n"
"v_exp_f32 v194, v194\n"
"v_mfma_f32_16x16x32_bf16 v[228:231], a[56:59], v[168:171], v[228:231]\n"
"buffer_load_dwordx4 v247, s[36:39], s52 offen offset:2048 lds\n"
"v_mfma_f32_16x16x32_bf16 v[228:231], a[52:55], v[164:167], v[228:231]\n"
"v_exp_f32 v195, v195\n"
"v_mfma_f32_16x16x32_bf16 v[228:231], a[48:51], v[160:163], v[228:231]\n"
"s_waitcnt vmcnt(11) lgkmcnt(8)\n"
"v_perm_b32 v201, v195, v194, s72\n"
"v_smfmac_f32_16x16x64_bf16 a[132:135], a[144:147], v[208:215], v251\n"
"v_mfma_f32_16x16x32_bf16 v[32:35], a[64:67], v[208:211], v[32:35]\n"
"v_exp_f32 v196, v196\n"
"v_mfma_f32_16x16x32_bf16 v[32:35], a[68:71], v[212:215], v[32:35]\n"
"s_barrier\n"
"v_mfma_f32_16x16x32_bf16 v[36:39], a[76:79], v[212:215], v[36:39]\n"
"v_exp_f32 v197, v197\n"
"v_mfma_f32_16x16x32_bf16 v[36:39], a[72:75], v[208:211], v[36:39]\n"
"v_perm_b32 v202, v197, v196, s72\n"
"v_mfma_f32_16x16x32_bf16 v[40:43], a[80:83], v[208:211], v[40:43]\n"
"s_add_u32 m0, s53, 52416\n"
"v_exp_f32 v198, v198\n"
"v_mfma_f32_16x16x32_bf16 v[40:43], a[84:87], v[212:215], v[40:43]\n"
"buffer_load_dwordx4 v247, s[36:39], s52 offen offset:3072 lds\n"
"v_mfma_f32_16x16x32_bf16 v[44:47], a[92:95], v[212:215], v[44:47]\n"
"s_cmp_lt_u32 s56, 3\n"
"v_exp_f32 v199, v199\n"
"v_mfma_f32_16x16x32_bf16 v[44:47], a[88:91], v[208:211], v[44:47]\n"
"s_cselect_b32 s38, s38, 0\n"
"v_perm_b32 v203, v199, v198, s72\n"
"v_mfma_f32_16x16x32_bf16 v[48:51], a[96:99], v[208:211], v[48:51]\n"
"v_exp_f32 v224, v224\n"
"v_mfma_f32_16x16x32_bf16 v[48:51], a[100:103], v[212:215], v[48:51]\n"
"v_mfma_f32_16x16x32_bf16 v[52:55], a[108:111], v[212:215], v[52:55]\n"
"v_exp_f32 v225, v225\n"
"v_mfma_f32_16x16x32_bf16 v[52:55], a[104:107], v[208:211], v[52:55]\n"
"v_perm_b32 v204, v225, v224, s72\n"
"v_mfma_f32_16x16x32_bf16 v[56:59], a[112:115], v[208:211], v[56:59]\n"
"s_add_u32 m0, s54, 49536\n"
"v_exp_f32 v226, v226\n"
"v_mfma_f32_16x16x32_bf16 v[56:59], a[116:119], v[212:215], v[56:59]\n"
"buffer_load_dwordx4 v248, s[40:43], s52 offen offset:0 lds\n"
"v_mfma_f32_16x16x32_bf16 v[60:63], a[124:127], v[212:215], v[60:63]\n"
"v_exp_f32 v227, v227\n"
"v_mfma_f32_16x16x32_bf16 v[60:63], a[120:123], v[208:211], v[60:63]\n"
"v_perm_b32 v205, v227, v226, s72\n"
"v_mfma_f32_16x16x32_bf16 v[192:195], a[0:3], v[176:179], -4.0\n"
"v_exp_f32 v228, v228\n"
"v_mfma_f32_16x16x32_bf16 v[192:195], a[4:7], v[180:183], v[192:195]\n"
"ds_read_b128 a[0:3], v244 offset:17408\n"
"ds_read_b128 a[4:7], v244 offset:17472\n"
"v_mfma_f32_16x16x32_bf16 v[192:195], a[8:11], v[184:187], v[192:195]\n"
"v_exp_f32 v229, v229\n"
"v_mfma_f32_16x16x32_bf16 v[192:195], a[12:15], v[188:191], v[192:195]\n"
"ds_read_b128 a[8:11], v244 offset:17536\n"
"v_perm_b32 v206, v229, v228, s72\n"
"v_mfma_f32_16x16x32_bf16 v[196:199], a[28:31], v[188:191], -4.0\n"
"s_add_u32 m0, s55, 0\n"
"v_exp_f32 v230, v230\n"
"v_mfma_f32_16x16x32_bf16 v[196:199], a[24:27], v[184:187], v[196:199]\n"
"s_waitcnt lgkmcnt(3)\n"
"buffer_load_dwordx4 v249, s[44:47], s76 offen offset:0 nt lds\n"
"v_mfma_f32_16x16x32_bf16 v[196:199], a[20:23], v[180:183], v[196:199]\n"
"v_exp_f32 v231, v231\n"
"v_mfma_f32_16x16x32_bf16 v[196:199], a[16:19], v[176:179], v[196:199]\n"
"ds_read_b128 a[12:15], v244 offset:17600\n"
"v_perm_b32 v207, v231, v230, s72\n"
"v_mfma_f32_16x16x32_bf16 v[224:227], a[32:35], v[176:179], -4.0\n"
"v_exp_f32 v192, v192\n"
"v_mfma_f32_16x16x32_bf16 v[224:227], a[36:39], v[180:183], v[224:227]\n"
"ds_read_b128 a[28:31], v244 offset:21952\n"
"ds_read_b128 a[24:27], v244 offset:21888\n"
"v_mfma_f32_16x16x32_bf16 v[224:227], a[40:43], v[184:187], v[224:227]\n"
"v_exp_f32 v193, v193\n"
"v_mfma_f32_16x16x32_bf16 v[224:227], a[44:47], v[188:191], v[224:227]\n"
"ds_read_b128 a[20:23], v244 offset:21824\n"
"v_perm_b32 v208, v193, v192, s72\n"
"v_mfma_f32_16x16x32_bf16 v[228:231], a[60:63], v[188:191], -4.0\n"
"s_add_u32 m0, s55, 8\n"
"v_exp_f32 v194, v194\n"
"v_mfma_f32_16x16x32_bf16 v[228:231], a[56:59], v[184:187], v[228:231]\n"
"buffer_load_dwordx4 v249, s[44:47], s76 offen offset:1024 nt lds\n"
"v_mfma_f32_16x16x32_bf16 v[228:231], a[52:55], v[180:183], v[228:231]\n"
"v_exp_f32 v195, v195\n"
"v_mfma_f32_16x16x32_bf16 v[228:231], a[48:51], v[176:179], v[228:231]\n"
"ds_read_b128 a[16:19], v244 offset:21760\n"
"v_perm_b32 v209, v195, v194, s72\n"
"v_smfmac_f32_16x16x64_bf16 a[136:139], a[144:147], v[200:207], v251\n"
"ds_read_b128 a[32:35], v222 offset:17408\n"
"v_mfma_f32_16x16x32_bf16 v[64:67], a[64:67], v[200:203], v[64:67]\n"
"v_exp_f32 v196, v196\n"
"v_mfma_f32_16x16x32_bf16 v[64:67], a[68:71], v[204:207], v[64:67]\n"
"ds_read_b128 a[36:39], v222 offset:17472\n"
"ds_read_b128 a[40:43], v222 offset:17536\n"
"v_mfma_f32_16x16x32_bf16 v[68:71], a[76:79], v[204:207], v[68:71]\n"
"v_exp_f32 v197, v197\n"
"v_mfma_f32_16x16x32_bf16 v[68:71], a[72:75], v[200:203], v[68:71]\n"
"ds_read_b128 a[44:47], v222 offset:17600\n"
"v_perm_b32 v210, v197, v196, s72\n"
"v_mfma_f32_16x16x32_bf16 v[72:75], a[80:83], v[200:203], v[72:75]\n"
"s_add_u32 m0, s55, 16\n"
"v_exp_f32 v198, v198\n"
"v_mfma_f32_16x16x32_bf16 v[72:75], a[84:87], v[204:207], v[72:75]\n"
"buffer_load_dwordx4 v249, s[44:47], s76 offen offset:2048 nt lds\n"
"v_mfma_f32_16x16x32_bf16 v[76:79], a[92:95], v[204:207], v[76:79]\n"
"v_exp_f32 v199, v199\n"
"v_mfma_f32_16x16x32_bf16 v[76:79], a[88:91], v[200:203], v[76:79]\n"
"ds_read_b128 a[60:63], v222 offset:21952\n"
"v_perm_b32 v211, v199, v198, s72\n"
"v_mfma_f32_16x16x32_bf16 v[80:83], a[96:99], v[200:203], v[80:83]\n"
"v_exp_f32 v224, v224\n"
"v_mfma_f32_16x16x32_bf16 v[80:83], a[100:103], v[204:207], v[80:83]\n"
"ds_read_b128 a[56:59], v222 offset:21888\n"
"ds_read_b128 a[52:55], v222 offset:21824\n"
"v_mfma_f32_16x16x32_bf16 v[84:87], a[108:111], v[204:207], v[84:87]\n"
"v_exp_f32 v225, v225\n"
"v_mfma_f32_16x16x32_bf16 v[84:87], a[104:107], v[200:203], v[84:87]\n"
"ds_read_b128 a[48:51], v222 offset:21760\n"
"v_perm_b32 v212, v225, v224, s72\n"
"v_mfma_f32_16x16x32_bf16 v[88:91], a[112:115], v[200:203], v[88:91]\n"
"s_add_u32 m0, s55, 24\n"
"v_exp_f32 v226, v226\n"
"v_mfma_f32_16x16x32_bf16 v[88:91], a[116:119], v[204:207], v[88:91]\n"
"buffer_load_dwordx4 v249, s[44:47], s76 offen offset:3072 nt lds\n"
"v_mfma_f32_16x16x32_bf16 v[92:95], a[124:127], v[204:207], v[92:95]\n"
"s_mov_b32 s76, s77\n"
"v_exp_f32 v227, v227\n"
"v_mfma_f32_16x16x32_bf16 v[92:95], a[120:123], v[200:203], v[92:95]\n"
"s_waitcnt lgkmcnt(4)\n"
"v_perm_b32 v213, v227, v226, s72\n"
"v_mfma_f32_16x16x32_bf16 v[192:195], a[0:3], v[128:131], -4.0\n"
"v_exp_f32 v228, v228\n"
"v_mfma_f32_16x16x32_bf16 v[192:195], a[4:7], v[132:135], v[192:195]\n"
"v_mfma_f32_16x16x32_bf16 v[192:195], a[8:11], v[136:139], v[192:195]\n"
"v_exp_f32 v229, v229\n"
"v_mfma_f32_16x16x32_bf16 v[192:195], a[12:15], v[140:143], v[192:195]\n"
"v_perm_b32 v214, v229, v228, s72\n"
"v_mfma_f32_16x16x32_bf16 v[196:199], a[28:31], v[140:143], -4.0\n"
"s_add_u32 m0, s54, 49544\n"
"v_exp_f32 v230, v230\n"
"v_mfma_f32_16x16x32_bf16 v[196:199], a[24:27], v[136:139], v[196:199]\n"
"buffer_load_dwordx4 v248, s[40:43], s52 offen offset:1024 lds\n"
"v_mfma_f32_16x16x32_bf16 v[196:199], a[20:23], v[132:135], v[196:199]\n"
"v_exp_f32 v231, v231\n"
"v_mfma_f32_16x16x32_bf16 v[196:199], a[16:19], v[128:131], v[196:199]\n"
"v_perm_b32 v215, v231, v230, s72\n"
"v_mfma_f32_16x16x32_bf16 v[224:227], a[32:35], v[128:131], -4.0\n"
"v_exp_f32 v192, v192\n"
"v_mfma_f32_16x16x32_bf16 v[224:227], a[36:39], v[132:135], v[224:227]\n"
"v_mfma_f32_16x16x32_bf16 v[224:227], a[40:43], v[136:139], v[224:227]\n"
"v_exp_f32 v193, v193\n"
"v_mfma_f32_16x16x32_bf16 v[224:227], a[44:47], v[140:143], v[224:227]\n"
"s_waitcnt lgkmcnt(0)\n"
"v_perm_b32 v200, v193, v192, s72\n"
"v_mfma_f32_16x16x32_bf16 v[228:231], a[60:63], v[140:143], -4.0\n"
"s_add_u32 m0, s54, 51472\n"
"v_exp_f32 v194, v194\n"
"v_mfma_f32_16x16x32_bf16 v[228:231], a[56:59], v[136:139], v[228:231]\n"
"buffer_load_dwordx4 v248, s[40:43], s52 offen offset:128 lds\n"
"v_mfma_f32_16x16x32_bf16 v[228:231], a[52:55], v[132:135], v[228:231]\n"
"v_exp_f32 v195, v195\n"
"v_mfma_f32_16x16x32_bf16 v[228:231], a[48:51], v[128:131], v[228:231]\n"
"v_perm_b32 v201, v195, v194, s72\n"
"v_smfmac_f32_16x16x64_bf16 a[140:143], a[144:147], v[208:215], v251\n"
"v_mfma_f32_16x16x32_bf16 v[96:99], a[64:67], v[208:211], v[96:99]\n"
"v_exp_f32 v196, v196\n"
"v_mfma_f32_16x16x32_bf16 v[96:99], a[68:71], v[212:215], v[96:99]\n"
"ds_read_b64_tr_b16 a[64:65], v245 offset:16512\n"
"ds_read_b64_tr_b16 a[66:67], v245 offset:20640\n"
"v_mfma_f32_16x16x32_bf16 v[100:103], a[76:79], v[212:215], v[100:103]\n"
"v_exp_f32 v197, v197\n"
"v_mfma_f32_16x16x32_bf16 v[100:103], a[72:75], v[208:211], v[100:103]\n"
"ds_read_b64_tr_b16 a[68:69], v255 offset:16512\n"
"v_perm_b32 v202, v197, v196, s72\n"
"v_mfma_f32_16x16x32_bf16 v[104:107], a[80:83], v[208:211], v[104:107]\n"
"v_exp_f32 v198, v198\n"
"v_mfma_f32_16x16x32_bf16 v[104:107], a[84:87], v[212:215], v[104:107]\n"
"ds_read_b64_tr_b16 a[70:71], v255 offset:20640\n"
"ds_read_b64_tr_b16 a[76:77], v255 offset:16520\n"
"v_mfma_f32_16x16x32_bf16 v[108:111], a[92:95], v[212:215], v[108:111]\n"
"v_exp_f32 v199, v199\n"
"v_mfma_f32_16x16x32_bf16 v[108:111], a[88:91], v[208:211], v[108:111]\n"
"ds_read_b64_tr_b16 a[78:79], v255 offset:20648\n"
"v_perm_b32 v203, v199, v198, s72\n"
"v_mfma_f32_16x16x32_bf16 v[112:115], a[96:99], v[208:211], v[112:115]\n"
"v_exp_f32 v224, v224\n"
"v_mfma_f32_16x16x32_bf16 v[112:115], a[100:103], v[212:215], v[112:115]\n"
"ds_read_b64_tr_b16 a[72:73], v245 offset:16520\n"
"ds_read_b64_tr_b16 a[74:75], v245 offset:20648\n"
"v_mfma_f32_16x16x32_bf16 v[116:119], a[108:111], v[212:215], v[116:119]\n"
"v_exp_f32 v225, v225\n"
"v_mfma_f32_16x16x32_bf16 v[116:119], a[104:107], v[208:211], v[116:119]\n"
"ds_read_b64_tr_b16 a[80:81], v245 offset:16768\n"
"v_perm_b32 v204, v225, v224, s72\n"
"v_mfma_f32_16x16x32_bf16 v[120:123], a[112:115], v[208:211], v[120:123]\n"
"v_exp_f32 v226, v226\n"
"v_mfma_f32_16x16x32_bf16 v[120:123], a[116:119], v[212:215], v[120:123]\n"
"ds_read_b64_tr_b16 a[82:83], v245 offset:20896\n"
"ds_read_b64_tr_b16 a[84:85], v255 offset:16768\n"
"v_mfma_f32_16x16x32_bf16 v[124:127], a[124:127], v[212:215], v[124:127]\n"
"v_exp_f32 v227, v227\n"
"v_mfma_f32_16x16x32_bf16 v[124:127], a[120:123], v[208:211], v[124:127]\n"
"ds_read_b64_tr_b16 a[86:87], v255 offset:20896\n"
"v_perm_b32 v205, v227, v226, s72\n"
"v_mfma_f32_16x16x32_bf16 v[192:195], a[0:3], v[144:147], -4.0\n"
"v_exp_f32 v228, v228\n"
"v_mfma_f32_16x16x32_bf16 v[192:195], a[4:7], v[148:151], v[192:195]\n"
"ds_read_b64_tr_b16 a[92:93], v255 offset:16776\n"
"ds_read_b64_tr_b16 a[94:95], v255 offset:20904\n"
"v_mfma_f32_16x16x32_bf16 v[192:195], a[8:11], v[152:155], v[192:195]\n"
"v_exp_f32 v229, v229\n"
"v_mfma_f32_16x16x32_bf16 v[192:195], a[12:15], v[156:159], v[192:195]\n"
"ds_read_b64_tr_b16 a[88:89], v245 offset:16776\n"
"v_perm_b32 v206, v229, v228, s72\n"
"v_mfma_f32_16x16x32_bf16 v[196:199], a[28:31], v[156:159], -4.0\n"
"v_exp_f32 v230, v230\n"
"v_mfma_f32_16x16x32_bf16 v[196:199], a[24:27], v[152:155], v[196:199]\n"
"ds_read_b64_tr_b16 a[90:91], v245 offset:20904\n"
"ds_read_b64_tr_b16 a[96:97], v245 offset:18576\n"
"v_mfma_f32_16x16x32_bf16 v[196:199], a[20:23], v[148:151], v[196:199]\n"
"v_exp_f32 v231, v231\n"
"v_mfma_f32_16x16x32_bf16 v[196:199], a[16:19], v[144:147], v[196:199]\n"
"ds_read_b64_tr_b16 a[98:99], v245 offset:22704\n"
"v_perm_b32 v207, v231, v230, s72\n"
"v_mfma_f32_16x16x32_bf16 v[224:227], a[32:35], v[144:147], -4.0\n"
"v_exp_f32 v192, v192\n"
"v_mfma_f32_16x16x32_bf16 v[224:227], a[36:39], v[148:151], v[224:227]\n"
"ds_read_b64_tr_b16 a[100:101], v255 offset:18576\n"
"ds_read_b64_tr_b16 a[102:103], v255 offset:22704\n"
"v_mfma_f32_16x16x32_bf16 v[224:227], a[40:43], v[152:155], v[224:227]\n"
"v_exp_f32 v193, v193\n"
"v_mfma_f32_16x16x32_bf16 v[224:227], a[44:47], v[156:159], v[224:227]\n"
"ds_read_b64_tr_b16 a[108:109], v255 offset:18584\n"
"v_perm_b32 v208, v193, v192, s72\n"
"v_mfma_f32_16x16x32_bf16 v[228:231], a[60:63], v[156:159], -4.0\n"
"v_exp_f32 v194, v194\n"
"v_mfma_f32_16x16x32_bf16 v[228:231], a[56:59], v[152:155], v[228:231]\n"
"ds_read_b64_tr_b16 a[110:111], v255 offset:22712\n"
"ds_read_b64_tr_b16 a[104:105], v245 offset:18584\n"
"v_mfma_f32_16x16x32_bf16 v[228:231], a[52:55], v[148:151], v[228:231]\n"
"v_exp_f32 v195, v195\n"
"v_mfma_f32_16x16x32_bf16 v[228:231], a[48:51], v[144:147], v[228:231]\n"
"ds_read_b64_tr_b16 a[106:107], v245 offset:22712\n"
"v_perm_b32 v209, v195, v194, s72\n"
"v_smfmac_f32_16x16x64_bf16 a[128:131], a[144:147], v[200:207], v251\n"
"s_waitcnt lgkmcnt(7)\n"
"ds_read_b64_tr_b16 a[112:113], v245 offset:18832\n"
"v_mfma_f32_16x16x32_bf16 v[0:3], a[64:67], v[200:203], v[0:3]\n"
"v_exp_f32 v196, v196\n"
"v_mfma_f32_16x16x32_bf16 v[0:3], a[68:71], v[204:207], v[0:3]\n"
"ds_read_b64_tr_b16 a[114:115], v245 offset:22960\n"
"ds_read_b64_tr_b16 a[116:117], v255 offset:18832\n"
"v_mfma_f32_16x16x32_bf16 v[4:7], a[76:79], v[204:207], v[4:7]\n"
"v_exp_f32 v197, v197\n"
"v_mfma_f32_16x16x32_bf16 v[4:7], a[72:75], v[200:203], v[4:7]\n"
"ds_read_b64_tr_b16 a[118:119], v255 offset:22960\n"
"v_perm_b32 v210, v197, v196, s72\n"
"v_mfma_f32_16x16x32_bf16 v[8:11], a[80:83], v[200:203], v[8:11]\n"
"v_exp_f32 v198, v198\n"
"v_mfma_f32_16x16x32_bf16 v[8:11], a[84:87], v[204:207], v[8:11]\n"
"ds_read_b64_tr_b16 a[124:125], v255 offset:18840\n"
"ds_read_b64_tr_b16 a[126:127], v255 offset:22968\n"
"v_mfma_f32_16x16x32_bf16 v[12:15], a[92:95], v[204:207], v[12:15]\n"
"v_exp_f32 v199, v199\n"
"v_mfma_f32_16x16x32_bf16 v[12:15], a[88:91], v[200:203], v[12:15]\n"
"s_waitcnt lgkmcnt(7)\n"
"ds_read_b64_tr_b16 a[120:121], v245 offset:18840\n"
"v_perm_b32 v211, v199, v198, s72\n"
"v_mfma_f32_16x16x32_bf16 v[16:19], a[96:99], v[200:203], v[16:19]\n"
"v_exp_f32 v224, v224\n"
"v_mfma_f32_16x16x32_bf16 v[16:19], a[100:103], v[204:207], v[16:19]\n"
"ds_read_b64_tr_b16 a[122:123], v245 offset:22968\n"
"v_mfma_f32_16x16x32_bf16 v[20:23], a[108:111], v[204:207], v[20:23]\n"
"s_waitcnt lgkmcnt(7)\n"
"v_exp_f32 v225, v225\n"
"v_mfma_f32_16x16x32_bf16 v[20:23], a[104:107], v[200:203], v[20:23]\n"
"s_waitcnt lgkmcnt(6)\n"
"v_perm_b32 v212, v225, v224, s72\n"
"v_mfma_f32_16x16x32_bf16 v[24:27], a[112:115], v[200:203], v[24:27]\n"
"s_waitcnt lgkmcnt(4)\n"
"v_exp_f32 v226, v226\n"
"v_mfma_f32_16x16x32_bf16 v[24:27], a[116:119], v[204:207], v[24:27]\n"
"s_waitcnt lgkmcnt(2)\n"
"v_mfma_f32_16x16x32_bf16 v[28:31], a[124:127], v[204:207], v[28:31]\n"
"s_waitcnt lgkmcnt(0)\n"
"v_exp_f32 v227, v227\n"
"v_mfma_f32_16x16x32_bf16 v[28:31], a[120:123], v[200:203], v[28:31]\n"
"buffer_store_dwordx4 a[232:235], v250, s[48:51], s84 offen offset:0 sc1\n"
"v_perm_b32 v213, v227, v226, s72\n"
"v_mfma_f32_16x16x32_bf16 v[192:195], a[0:3], v[160:163], -4.0\n"
"v_exp_f32 v228, v228\n"
"v_mfma_f32_16x16x32_bf16 v[192:195], a[4:7], v[164:167], v[192:195]\n"
"v_mfma_f32_16x16x32_bf16 v[192:195], a[8:11], v[168:171], v[192:195]\n"
"v_exp_f32 v229, v229\n"
"v_mfma_f32_16x16x32_bf16 v[192:195], a[12:15], v[172:175], v[192:195]\n"
"v_perm_b32 v214, v229, v228, s72\n"
"v_mfma_f32_16x16x32_bf16 v[196:199], a[28:31], v[172:175], -4.0\n"
"s_add_u32 m0, s54, 51480\n"
"v_exp_f32 v230, v230\n"
"v_mfma_f32_16x16x32_bf16 v[196:199], a[24:27], v[168:171], v[196:199]\n"
"buffer_load_dwordx4 v248, s[40:43], s52 offen offset:1152 lds\n"
"v_mfma_f32_16x16x32_bf16 v[196:199], a[20:23], v[164:167], v[196:199]\n"
"s_add_u32 s52, s52, 4286464000\n"
"v_exp_f32 v231, v231\n"
"v_mfma_f32_16x16x32_bf16 v[196:199], a[16:19], v[160:163], v[196:199]\n"
"s_mov_b32 s42, s38\n"
"v_perm_b32 v215, v231, v230, s72\n"
"v_mfma_f32_16x16x32_bf16 v[224:227], a[32:35], v[160:163], -4.0\n"
"v_exp_f32 v192, v192\n"
"v_mfma_f32_16x16x32_bf16 v[224:227], a[36:39], v[164:167], v[224:227]\n"
"v_mfma_f32_16x16x32_bf16 v[224:227], a[40:43], v[168:171], v[224:227]\n"
"v_exp_f32 v193, v193\n"
"v_mfma_f32_16x16x32_bf16 v[224:227], a[44:47], v[172:175], v[224:227]\n"
"v_perm_b32 v200, v193, v192, s72\n"
"v_mfma_f32_16x16x32_bf16 v[228:231], a[60:63], v[172:175], -4.0\n"
"s_add_u32 m0, s53, 0\n"
"v_exp_f32 v194, v194\n"
"v_mfma_f32_16x16x32_bf16 v[228:231], a[56:59], v[168:171], v[228:231]\n"
"Lhop0_6_%=:\n s_cbranch_execz Lback0_%=\n"
"buffer_load_dwordx4 v247, s[36:39], s52 offen offset:0 lds\n"
"v_mfma_f32_16x16x32_bf16 v[228:231], a[52:55], v[164:167], v[228:231]\n"
"v_exp_f32 v195, v195\n"
"v_mfma_f32_16x16x32_bf16 v[228:231], a[48:51], v[160:163], v[228:231]\n"
"s_waitcnt vmcnt(13) lgkmcnt(0)\n"
"v_perm_b32 v201, v195, v194, s72\n"
"v_smfmac_f32_16x16x64_bf16 a[132:135], a[144:147], v[208:215], v251\n"
"v_mfma_f32_16x16x32_bf16 v[32:35], a[64:67], v[208:211], v[32:35]\n"
"v_exp_f32 v196, v196\n"
"v_mfma_f32_16x16x32_bf16 v[32:35], a[68:71], v[212:215], v[32:35]\n"
"s_barrier\n"
"v_mfma_f32_16x16x32_bf16 v[36:39], a[76:79], v[212:215], v[36:39]\n"
"v_exp_f32 v197, v197\n"
"v_mfma_f32_16x16x32_bf16 v[36:39], a[72:75], v[208:211], v[36:39]\n"
"v_perm_b32 v202, v197, v196, s72\n"
"v_mfma_f32_16x16x32_bf16 v[40:43], a[80:83], v[208:211], v[40:43]\n"
"s_add_u32 m0, s53, 64\n"
"v_exp_f32 v198, v198\n"
"v_mfma_f32_16x16x32_bf16 v[40:43], a[84:87], v[212:215], v[40:43]\n"
"buffer_load_dwordx4 v247, s[36:39], s52 offen offset:1024 lds\n"
"v_mfma_f32_16x16x32_bf16 v[44:47], a[92:95], v[212:215], v[44:47]\n"
"v_exp_f32 v199, v199\n"
"v_mfma_f32_16x16x32_bf16 v[44:47], a[88:91], v[208:211], v[44:47]\n"
"v_perm_b32 v203, v199, v198, s72\n"
"v_mfma_f32_16x16x32_bf16 v[48:51], a[96:99], v[208:211], v[48:51]\n"
"v_exp_f32 v224, v224\n"
"v_mfma_f32_16x16x32_bf16 v[48:51], a[100:103], v[212:215], v[48:51]\n"
"v_mfma_f32_16x16x32_bf16 v[52:55], a[108:111], v[212:215], v[52:55]\n"
"v_exp_f32 v225, v225\n"
"v_mfma_f32_16x16x32_bf16 v[52:55], a[104:107], v[208:211], v[52:55]\n"
"v_perm_b32 v204, v225, v224, s72\n"
"v_mfma_f32_16x16x32_bf16 v[56:59], a[112:115], v[208:211], v[56:59]\n"
"s_add_u32 m0, s53, 128\n"
"v_exp_f32 v226, v226\n"
"v_mfma_f32_16x16x32_bf16 v[56:59], a[116:119], v[212:215], v[56:59]\n"
"buffer_load_dwordx4 v247, s[36:39], s52 offen offset:2048 lds\n"
"v_mfma_f32_16x16x32_bf16 v[60:63], a[124:127], v[212:215], v[60:63]\n"
"v_exp_f32 v227, v227\n"
"v_mfma_f32_16x16x32_bf16 v[60:63], a[120:123], v[208:211], v[60:63]\n"
"v_perm_b32 v205, v227, v226, s72\n"
"v_mfma_f32_16x16x32_bf16 v[192:195], a[0:3], v[176:179], -4.0\n"
"v_exp_f32 v228, v228\n"
"v_mfma_f32_16x16x32_bf16 v[192:195], a[4:7], v[180:183], v[192:195]\n"
"ds_read_b128 a[0:3], v244 offset:34816\n"
"ds_read_b128 a[4:7], v244 offset:34880\n"
"v_mfma_f32_16x16x32_bf16 v[192:195], a[8:11], v[184:187], v[192:195]\n"
"v_exp_f32 v229, v229\n"
"v_mfma_f32_16x16x32_bf16 v[192:195], a[12:15], v[188:191], v[192:195]\n"
"ds_read_b128 a[8:11], v244 offset:34944\n"
"v_perm_b32 v206, v229, v228, s72\n"
"v_mfma_f32_16x16x32_bf16 v[196:199], a[28:31], v[188:191], -4.0\n"
"s_add_u32 m0, s53, 192\n"
"v_exp_f32 v230, v230\n"
"v_mfma_f32_16x16x32_bf16 v[196:199], a[24:27], v[184:187], v[196:199]\n"
"buffer_load_dwordx4 v247, s[36:39], s52 offen offset:3072 lds\n"
"v_mfma_f32_16x16x32_bf16 v[196:199], a[20:23], v[180:183], v[196:199]\n"
"v_exp_f32 v231, v231\n"
"v_mfma_f32_16x16x32_bf16 v[196:199], a[16:19], v[176:179], v[196:199]\n"
"buffer_store_dwordx4 a[236:239], v250, s[48:51], s84 offen offset:1024 sc1\n"
"v_perm_b32 v207, v231, v230, s72\n"
"v_mfma_f32_16x16x32_bf16 v[224:227], a[32:35], v[176:179], -4.0\n"
"s_cmp_eq_u32 s70, s56\n"
"v_exp_f32 v192, v192\n"
"v_mfma_f32_16x16x32_bf16 v[224:227], a[36:39], v[180:183], v[224:227]\n"
"s_cselect_b32 s50, 0, s50\n"
"ds_read_b128 a[12:15], v244 offset:35008\n"
"ds_read_b128 a[28:31], v244 offset:39360\n"
"v_mfma_f32_16x16x32_bf16 v[224:227], a[40:43], v[184:187], v[224:227]\n"
"v_exp_f32 v193, v193\n"
"v_mfma_f32_16x16x32_bf16 v[224:227], a[44:47], v[188:191], v[224:227]\n"
"ds_read_b128 a[24:27], v244 offset:39296\n"
"v_perm_b32 v208, v193, v192, s72\n"
"v_mfma_f32_16x16x32_bf16 v[228:231], a[60:63], v[188:191], -4.0\n"
"s_add_u32 m0, s54, 0\n"
"v_exp_f32 v194, v194\n"
"v_mfma_f32_16x16x32_bf16 v[228:231], a[56:59], v[184:187], v[228:231]\n"
"buffer_load_dwordx4 v248, s[40:43], s52 offen offset:0 lds\n"
"v_mfma_f32_16x16x32_bf16 v[228:231], a[52:55], v[180:183], v[228:231]\n"
"v_exp_f32 v195, v195\n"
"v_mfma_f32_16x16x32_bf16 v[228:231], a[48:51], v[176:179], v[228:231]\n"
"ds_read_b128 a[20:23], v244 offset:39232\n"
"v_perm_b32 v209, v195, v194, s72\n"
"v_smfmac_f32_16x16x64_bf16 a[136:139], a[144:147], v[200:207], v251\n"
"ds_read_b128 a[16:19], v244 offset:39168\n"
"v_mfma_f32_16x16x32_bf16 v[64:67], a[64:67], v[200:203], v[64:67]\n"
"v_exp_f32 v196, v196\n"
"v_mfma_f32_16x16x32_bf16 v[64:67], a[68:71], v[204:207], v[64:67]\n"
"ds_read_b128 a[32:35], v222 offset:34816\n"
"ds_read_b128 a[36:39], v222 offset:34880\n"
"v_mfma_f32_16x16x32_bf16 v[68:71], a[76:79], v[204:207], v[68:71]\n"
"v_exp_f32 v197, v197\n"
"v_mfma_f32_16x16x32_bf16 v[68:71], a[72:75], v[200:203], v[68:71]\n"
"ds_read_b128 a[40:43], v222 offset:34944\n"
"v_perm_b32 v210, v197, v196, s72\n"
"v_mfma_f32_16x16x32_bf16 v[72:75], a[80:83], v[200:203], v[72:75]\n"
"s_add_u32 m0, s54, 8\n"
"v_exp_f32 v198, v198\n"
"v_mfma_f32_16x16x32_bf16 v[72:75], a[84:87], v[204:207], v[72:75]\n"
"buffer_load_dwordx4 v248, s[40:43], s52 offen offset:1024 lds\n"
"v_mfma_f32_16x16x32_bf16 v[76:79], a[92:95], v[204:207], v[76:79]\n"
"v_exp_f32 v199, v199\n"
"v_mfma_f32_16x16x32_bf16 v[76:79], a[88:91], v[200:203], v[76:79]\n"
"ds_read_b128 a[44:47], v222 offset:35008\n"
"v_perm_b32 v211, v199, v198, s72\n"
"v_mfma_f32_16x16x32_bf16 v[80:83], a[96:99], v[200:203], v[80:83]\n"
"v_exp_f32 v224, v224\n"
"v_mfma_f32_16x16x32_bf16 v[80:83], a[100:103], v[204:207], v[80:83]\n"
"ds_read_b128 a[60:63], v222 offset:39360\n"
"ds_read_b128 a[56:59], v222 offset:39296\n"
"v_mfma_f32_16x16x32_bf16 v[84:87], a[108:111], v[204:207], v[84:87]\n"
"v_exp_f32 v225, v225\n"
"v_mfma_f32_16x16x32_bf16 v[84:87], a[104:107], v[200:203], v[84:87]\n"
"ds_read_b128 a[52:55], v222 offset:39232\n"
"v_perm_b32 v212, v225, v224, s72\n"
"v_mfma_f32_16x16x32_bf16 v[88:91], a[112:115], v[200:203], v[88:91]\n"
"s_add_u32 m0, s54, 1936\n"
"v_exp_f32 v226, v226\n"
"v_mfma_f32_16x16x32_bf16 v[88:91], a[116:119], v[204:207], v[88:91]\n"
"buffer_load_dwordx4 v248, s[40:43], s52 offen offset:128 lds\n"
"v_mfma_f32_16x16x32_bf16 v[92:95], a[124:127], v[204:207], v[92:95]\n"
"v_exp_f32 v227, v227\n"
"v_mfma_f32_16x16x32_bf16 v[92:95], a[120:123], v[200:203], v[92:95]\n"
"s_waitcnt lgkmcnt(4)\n"
"ds_read_b128 a[48:51], v222 offset:39168\n"
"v_perm_b32 v213, v227, v226, s72\n"
"v_mfma_f32_16x16x32_bf16 v[192:195], a[0:3], v[128:131], -4.0\n"
"v_exp_f32 v228, v228\n"
"v_mfma_f32_16x16x32_bf16 v[192:195], a[4:7], v[132:135], v[192:195]\n"
"v_mfma_f32_16x16x32_bf16 v[192:195], a[8:11], v[136:139], v[192:195]\n"
"v_exp_f32 v229, v229\n"
"v_mfma_f32_16x16x32_bf16 v[192:195], a[12:15], v[140:143], v[192:195]\n"
"v_perm_b32 v214, v229, v228, s72\n"
"v_mfma_f32_16x16x32_bf16 v[196:199], a[28:31], v[140:143], -4.0\n"
"s_add_u32 m0, s54, 1944\n"
"v_exp_f32 v230, v230\n"
"v_mfma_f32_16x16x32_bf16 v[196:199], a[24:27], v[136:139], v[196:199]\n"
"buffer_load_dwordx4 v248, s[40:43], s52 offen offset:1152 lds\n"
"v_mfma_f32_16x16x32_bf16 v[196:199], a[20:23], v[132:135], v[196:199]\n"
"s_add_u32 s52, s52, 16384\n"
"v_exp_f32 v231, v231\n"
"v_mfma_f32_16x16x32_bf16 v[196:199], a[16:19], v[128:131], v[196:199]\n"
"v_perm_b32 v215, v231, v230, s72\n"
"v_mfma_f32_16x16x32_bf16 v[224:227], a[32:35], v[128:131], -4.0\n"
"v_exp_f32 v192, v192\n"
"v_mfma_f32_16x16x32_bf16 v[224:227], a[36:39], v[132:135], v[224:227]\n"
"v_mfma_f32_16x16x32_bf16 v[224:227], a[40:43], v[136:139], v[224:227]\n"
"s_waitcnt lgkmcnt(0)\n"
"v_exp_f32 v193, v193\n"
"v_mfma_f32_16x16x32_bf16 v[224:227], a[44:47], v[140:143], v[224:227]\n"
"v_perm_b32 v200, v193, v192, s72\n"
"v_mfma_f32_16x16x32_bf16 v[228:231], a[60:63], v[140:143], -4.0\n"
"v_exp_f32 v194, v194\n"
"v_mfma_f32_16x16x32_bf16 v[228:231], a[56:59], v[136:139], v[228:231]\n"
"v_mfma_f32_16x16x32_bf16 v[228:231], a[52:55], v[132:135], v[228:231]\n"
"v_exp_f32 v195, v195\n"
"v_mfma_f32_16x16x32_bf16 v[228:231], a[48:51], v[128:131], v[228:231]\n"
"buffer_store_dwordx4 a[240:243], v250, s[48:51], s84 offen offset:128 sc1\n"
"v_perm_b32 v201, v195, v194, s72\n"
"v_smfmac_f32_16x16x64_bf16 a[140:143], a[144:147], v[208:215], v251\n"
"v_mfma_f32_16x16x32_bf16 v[96:99], a[64:67], v[208:211], v[96:99]\n"
"v_exp_f32 v196, v196\n"
"v_mfma_f32_16x16x32_bf16 v[96:99], a[68:71], v[212:215], v[96:99]\n"
"ds_read_b64_tr_b16 a[64:65], v245 offset:33024\n"
"ds_read_b64_tr_b16 a[66:67], v245 offset:37152\n"
"v_mfma_f32_16x16x32_bf16 v[100:103], a[76:79], v[212:215], v[100:103]\n"
"v_exp_f32 v197, v197\n"
"v_mfma_f32_16x16x32_bf16 v[100:103], a[72:75], v[208:211], v[100:103]\n"
"ds_read_b64_tr_b16 a[68:69], v255 offset:33024\n"
"v_perm_b32 v202, v197, v196, s72\n"
"v_mfma_f32_16x16x32_bf16 v[104:107], a[80:83], v[208:211], v[104:107]\n"
"v_exp_f32 v198, v198\n"
"v_mfma_f32_16x16x32_bf16 v[104:107], a[84:87], v[212:215], v[104:107]\n"
"ds_read_b64_tr_b16 a[70:71], v255 offset:37152\n"
"ds_read_b64_tr_b16 a[76:77], v255 offset:33032\n"
"v_mfma_f32_16x16x32_bf16 v[108:111], a[92:95], v[212:215], v[108:111]\n"
"v_exp_f32 v199, v199\n"
"v_mfma_f32_16x16x32_bf16 v[108:111], a[88:91], v[208:211], v[108:111]\n"
"ds_read_b64_tr_b16 a[78:79], v255 offset:37160\n"
"v_perm_b32 v203, v199, v198, s72\n"
"v_mfma_f32_16x16x32_bf16 v[112:115], a[96:99], v[208:211], v[112:115]\n"
"v_exp_f32 v224, v224\n"
"v_mfma_f32_16x16x32_bf16 v[112:115], a[100:103], v[212:215], v[112:115]\n"
"ds_read_b64_tr_b16 a[72:73], v245 offset:33032\n"
"ds_read_b64_tr_b16 a[74:75], v245 offset:37160\n"
"v_mfma_f32_16x16x32_bf16 v[116:119], a[108:111], v[212:215], v[116:119]\n"
"v_exp_f32 v225, v225\n"
"v_mfma_f32_16x16x32_bf16 v[116:119], a[104:107], v[208:211], v[116:119]\n"
"ds_read_b64_tr_b16 a[80:81], v245 offset:33280\n"
"v_perm_b32 v204, v225, v224, s72\n"
"v_mfma_f32_16x16x32_bf16 v[120:123], a[112:115], v[208:211], v[120:123]\n"
"v_exp_f32 v226, v226\n"
"v_mfma_f32_16x16x32_bf16 v[120:123], a[116:119], v[212:215], v[120:123]\n"
"ds_read_b64_tr_b16 a[82:83], v245 offset:37408\n"
"ds_read_b64_tr_b16 a[84:85], v255 offset:33280\n"
"v_mfma_f32_16x16x32_bf16 v[124:127], a[124:127], v[212:215], v[124:127]\n"
"v_exp_f32 v227, v227\n"
"v_mfma_f32_16x16x32_bf16 v[124:127], a[120:123], v[208:211], v[124:127]\n"
"ds_read_b64_tr_b16 a[86:87], v255 offset:37408\n"
"v_perm_b32 v205, v227, v226, s72\n"
"v_mfma_f32_16x16x32_bf16 v[192:195], a[0:3], v[144:147], -4.0\n"
"v_exp_f32 v228, v228\n"
"v_mfma_f32_16x16x32_bf16 v[192:195], a[4:7], v[148:151], v[192:195]\n"
"ds_read_b64_tr_b16 a[92:93], v255 offset:33288\n"
"ds_read_b64_tr_b16 a[94:95], v255 offset:37416\n"
"v_mfma_f32_16x16x32_bf16 v[192:195], a[8:11], v[152:155], v[192:195]\n"
"v_exp_f32 v229, v229\n"
"v_mfma_f32_16x16x32_bf16 v[192:195], a[12:15], v[156:159], v[192:195]\n"
"ds_read_b64_tr_b16 a[88:89], v245 offset:33288\n"
"v_perm_b32 v206, v229, v228, s72\n"
"v_mfma_f32_16x16x32_bf16 v[196:199], a[28:31], v[156:159], -4.0\n"
"v_exp_f32 v230, v230\n"
"v_mfma_f32_16x16x32_bf16 v[196:199], a[24:27], v[152:155], v[196:199]\n"
"ds_read_b64_tr_b16 a[90:91], v245 offset:37416\n"
"ds_read_b64_tr_b16 a[96:97], v245 offset:35088\n"
"v_mfma_f32_16x16x32_bf16 v[196:199], a[20:23], v[148:151], v[196:199]\n"
"v_exp_f32 v231, v231\n"
"v_mfma_f32_16x16x32_bf16 v[196:199], a[16:19], v[144:147], v[196:199]\n"
"buffer_store_dwordx4 a[244:247], v250, s[48:51], s84 offen offset:1152 sc1\n"
"v_perm_b32 v207, v231, v230, s72\n"
"v_mfma_f32_16x16x32_bf16 v[224:227], a[32:35], v[144:147], -4.0\n"
"s_mov_b64 s[84:85], s[86:87]\n"
"v_exp_f32 v192, v192\n"
"v_mfma_f32_16x16x32_bf16 v[224:227], a[36:39], v[148:151], v[224:227]\n"
"s_mov_b64 s[86:87], s[88:89]\n"
"ds_read_b64_tr_b16 a[98:99], v245 offset:39216\n"
"ds_read_b64_tr_b16 a[100:101], v255 offset:35088\n"
"v_mfma_f32_16x16x32_bf16 v[224:227], a[40:43], v[152:155], v[224:227]\n"
"s_mov_b64 s[88:89], s[90:91]\n"
"v_exp_f32 v193, v193\n"
"v_mfma_f32_16x16x32_bf16 v[224:227], a[44:47], v[156:159], v[224:227]\n"
"s_mov_b64 s[90:91], s[92:93]\n"
"ds_read_b64_tr_b16 a[102:103], v255 offset:39216\n"
"v_perm_b32 v208, v193, v192, s72\n"
"v_mfma_f32_16x16x32_bf16 v[228:231], a[60:63], v[156:159], -4.0\n"
"s_mov_b32 s50, 65011712\n"
"v_exp_f32 v194, v194\n"
"v_mfma_f32_16x16x32_bf16 v[228:231], a[56:59], v[152:155], v[228:231]\n"
"ds_read_b64_tr_b16 a[108:109], v255 offset:35096\n"
"ds_read_b64_tr_b16 a[110:111], v255 offset:39224\n"
"v_mfma_f32_16x16x32_bf16 v[228:231], a[52:55], v[148:151], v[228:231]\n"
"v_exp_f32 v195, v195\n"
"v_mfma_f32_16x16x32_bf16 v[228:231], a[48:51], v[144:147], v[228:231]\n"
"ds_read_b64_tr_b16 a[104:105], v245 offset:35096\n"
"v_perm_b32 v209, v195, v194, s72\n"
"v_smfmac_f32_16x16x64_bf16 a[128:131], a[144:147], v[200:207], v251\n"
"s_waitcnt lgkmcnt(6)\n"
"ds_read_b64_tr_b16 a[106:107], v245 offset:39224\n"
"v_mfma_f32_16x16x32_bf16 v[0:3], a[64:67], v[200:203], v[0:3]\n"
"v_exp_f32 v196, v196\n"
"v_mfma_f32_16x16x32_bf16 v[0:3], a[68:71], v[204:207], v[0:3]\n"
"ds_read_b64_tr_b16 a[112:113], v245 offset:35344\n"
"ds_read_b64_tr_b16 a[114:115], v245 offset:39472\n"
"v_mfma_f32_16x16x32_bf16 v[4:7], a[76:79], v[204:207], v[4:7]\n"
"v_exp_f32 v197, v197\n"
"v_mfma_f32_16x16x32_bf16 v[4:7], a[72:75], v[200:203], v[4:7]\n"
"ds_read_b64_tr_b16 a[116:117], v255 offset:35344\n"
"v_perm_b32 v210, v197, v196, s72\n"
"v_mfma_f32_16x16x32_bf16 v[8:11], a[80:83], v[200:203], v[8:11]\n"
"v_exp_f32 v198, v198\n"
"v_mfma_f32_16x16x32_bf16 v[8:11], a[84:87], v[204:207], v[8:11]\n"
"ds_read_b64_tr_b16 a[118:119], v255 offset:39472\n"
"ds_read_b64_tr_b16 a[124:125], v255 offset:35352\n"
"v_mfma_f32_16x16x32_bf16 v[12:15], a[92:95], v[204:207], v[12:15]\n"
"v_exp_f32 v199, v199\n"
"v_mfma_f32_16x16x32_bf16 v[12:15], a[88:91], v[200:203], v[12:15]\n"
"s_waitcnt vmcnt(11) lgkmcnt(7)\n"
"ds_read_b64_tr_b16 a[126:127], v255 offset:39480\n"
"v_perm_b32 v211, v199, v198, s72\n"
"v_mfma_f32_16x16x32_bf16 v[16:19], a[96:99], v[200:203], v[16:19]\n"
"v_exp_f32 v224, v224\n"
"v_mfma_f32_16x16x32_bf16 v[16:19], a[100:103], v[204:207], v[16:19]\n"
"ds_read_b64_tr_b16 a[120:121], v245 offset:35352\n"
"ds_read_b64_tr_b16 a[122:123], v245 offset:39480\n"
"v_mfma_f32_16x16x32_bf16 v[20:23], a[108:111], v[204:207], v[20:23]\n"
"s_waitcnt lgkmcnt(8)\n"
"v_exp_f32 v225, v225\n"
"v_mfma_f32_16x16x32_bf16 v[20:23], a[104:107], v[200:203], v[20:23]\n"
"s_waitcnt lgkmcnt(4)\n"
"ds_read2_b64 a[216:219], v246 offset0:0 offset1:2\n"
"v_perm_b32 v212, v225, v224, s72\n"
"v_mfma_f32_16x16x32_bf16 v[24:27], a[112:115], v[200:203], v[24:27]\n"
"s_add_u32 m0, s53, 17408\n"
"v_exp_f32 v226, v226\n"
"v_mfma_f32_16x16x32_bf16 v[24:27], a[116:119], v[204:207], v[24:27]\n"
"s_waitcnt lgkmcnt(3)\n"
"buffer_load_dwordx4 v247, s[36:39], s52 offen offset:0 lds\n"
"v_mfma_f32_16x16x32_bf16 v[28:31], a[124:127], v[204:207], v[28:31]\n"
"s_waitcnt lgkmcnt(1)\n"
"v_exp_f32 v227, v227\n"
"v_mfma_f32_16x16x32_bf16 v[28:31], a[120:123], v[200:203], v[28:31]\n"
"ds_read2_b64 a[220:223], v246 offset0:1 offset1:3\n"
"v_perm_b32 v213, v227, v226, s72\n"
"v_mfma_f32_16x16x32_bf16 v[192:195], a[0:3], v[160:163], -4.0\n"
"v_exp_f32 v228, v228\n"
"v_mfma_f32_16x16x32_bf16 v[192:195], a[4:7], v[164:167], v[192:195]\n"
"ds_read2_b64 a[224:227], v246 offset0:32 offset1:34\n"
"ds_read2_b64 a[228:231], v246 offset0:33 offset1:35\n"
"v_mfma_f32_16x16x32_bf16 v[192:195], a[8:11], v[168:171], v[192:195]\n"
"v_exp_f32 v229, v229\n"
"v_mfma_f32_16x16x32_bf16 v[192:195], a[12:15], v[172:175], v[192:195]\n"
"ds_read2_b64 a[232:235], v246 offset0:64 offset1:66\n"
"v_perm_b32 v214, v229, v228, s72\n"
"v_mfma_f32_16x16x32_bf16 v[196:199], a[28:31], v[172:175], -4.0\n"
"s_add_u32 m0, s53, 17472\n"
"v_exp_f32 v230, v230\n"
"v_mfma_f32_16x16x32_bf16 v[196:199], a[24:27], v[168:171], v[196:199]\n"
"buffer_load_dwordx4 v247, s[36:39], s52 offen offset:1024 lds\n"
"v_mfma_f32_16x16x32_bf16 v[196:199], a[20:23], v[164:167], v[196:199]\n"
"v_exp_f32 v231, v231\n"
"v_mfma_f32_16x16x32_bf16 v[196:199], a[16:19], v[160:163], v[196:199]\n"
"ds_read2_b64 a[236:239], v246 offset0:65 offset1:67\n"
"v_perm_b32 v215, v231, v230, s72\n"
"v_mfma_f32_16x16x32_bf16 v[224:227], a[32:35], v[160:163], -4.0\n"
"v_exp_f32 v192, v192\n"
"v_mfma_f32_16x16x32_bf16 v[224:227], a[36:39], v[164:167], v[224:227]\n"
"ds_read2_b64 a[240:243], v246 offset0:96 offset1:98\n"
"ds_read2_b64 a[244:247], v246 offset0:97 offset1:99\n"
"v_mfma_f32_16x16x32_bf16 v[224:227], a[40:43], v[168:171], v[224:227]\n"
"v_exp_f32 v193, v193\n"
"v_mfma_f32_16x16x32_bf16 v[224:227], a[44:47], v[172:175], v[224:227]\n"
"v_perm_b32 v200, v193, v192, s72\n"
"v_mfma_f32_16x16x32_bf16 v[228:231], a[60:63], v[172:175], -4.0\n"
"s_add_u32 m0, s53, 17536\n"
"v_exp_f32 v194, v194\n"
"v_mfma_f32_16x16x32_bf16 v[228:231], a[56:59], v[168:171], v[228:231]\n"
"buffer_load_dwordx4 v247, s[36:39], s52 offen offset:2048 lds\n"
"v_mfma_f32_16x16x32_bf16 v[228:231], a[52:55], v[164:167], v[228:231]\n"
"v_exp_f32 v195, v195\n"
"v_mfma_f32_16x16x32_bf16 v[228:231], a[48:51], v[160:163], v[228:231]\n"
"s_waitcnt vmcnt(11) lgkmcnt(8)\n"
"v_perm_b32 v201, v195, v194, s72\n"
"v_smfmac_f32_16x16x64_bf16 a[132:135], a[144:147], v[208:215], v251\n"
"v_mfma_f32_16x16x32_bf16 v[32:35], a[64:67], v[208:211], v[32:35]\n"
"v_exp_f32 v196, v196\n"
"v_mfma_f32_16x16x32_bf16 v[32:35], a[68:71], v[212:215], v[32:35]\n"
"s_barrier\n"
"v_mfma_f32_16x16x32_bf16 v[36:39], a[76:79], v[212:215], v[36:39]\n"
"v_exp_f32 v197, v197\n"
"v_mfma_f32_16x16x32_bf16 v[36:39], a[72:75], v[208:211], v[36:39]\n"
"v_perm_b32 v202, v197, v196, s72\n"
"v_mfma_f32_16x16x32_bf16 v[40:43], a[80:83], v[208:211], v[40:43]\n"
"s_add_u32 m0, s53, 17600\n"
"v_exp_f32 v198, v198\n"
"v_mfma_f32_16x16x32_bf16 v[40:43], a[84:87], v[212:215], v[40:43]\n"
"buffer_load_dwordx4 v247, s[36:39], s52 offen offset:3072 lds\n"
"v_mfma_f32_16x16x32_bf16 v[44:47], a[92:95], v[212:215], v[44:47]\n"
"v_exp_f32 v199, v199\n"
"v_mfma_f32_16x16x32_bf16 v[44:47], a[88:91], v[208:211], v[44:47]\n"
"v_perm_b32 v203, v199, v198, s72\n"
"v_mfma_f32_16x16x32_bf16 v[48:51], a[96:99], v[208:211], v[48:51]\n"
"v_exp_f32 v224, v224\n"
"v_mfma_f32_16x16x32_bf16 v[48:51], a[100:103], v[212:215], v[48:51]\n"
"v_mfma_f32_16x16x32_bf16 v[52:55], a[108:111], v[212:215], v[52:55]\n"
"v_exp_f32 v225, v225\n"
"v_mfma_f32_16x16x32_bf16 v[52:55], a[104:107], v[208:211], v[52:55]\n"
"v_perm_b32 v204, v225, v224, s72\n"
"v_mfma_f32_16x16x32_bf16 v[56:59], a[112:115], v[208:211], v[56:59]\n"
"s_add_u32 m0, s55, 0\n"
"v_exp_f32 v226, v226\n"
"v_mfma_f32_16x16x32_bf16 v[56:59], a[116:119], v[212:215], v[56:59]\n"
"s_waitcnt lgkmcnt(0)\n"
"buffer_load_dwordx4 v249, s[44:47], s76 offen offset:0 nt lds\n"
"v_mfma_f32_16x16x32_bf16 v[60:63], a[124:127], v[212:215], v[60:63]\n"
"v_exp_f32 v227, v227\n"
"v_mfma_f32_16x16x32_bf16 v[60:63], a[120:123], v[208:211], v[60:63]\n"
"v_perm_b32 v205, v227, v226, s72\n"
"v_mfma_f32_16x16x32_bf16 v[192:195], a[0:3], v[176:179], -4.0\n"
"v_exp_f32 v228, v228\n"
"v_mfma_f32_16x16x32_bf16 v[192:195], a[4:7], v[180:183], v[192:195]\n"
"ds_read_b128 a[0:3], v244 offset:52224\n"
"ds_read_b128 a[4:7], v244 offset:52288\n"
"v_mfma_f32_16x16x32_bf16 v[192:195], a[8:11], v[184:187], v[192:195]\n"
"v_exp_f32 v229, v229\n"
"v_mfma_f32_16x16x32_bf16 v[192:195], a[12:15], v[188:191], v[192:195]\n"
"ds_read_b128 a[8:11], v244 offset:52352\n"
"v_perm_b32 v206, v229, v228, s72\n"
"v_mfma_f32_16x16x32_bf16 v[196:199], a[28:31], v[188:191], -4.0\n"
"s_add_u32 m0, s55, 8\n"
"v_exp_f32 v230, v230\n"
"v_mfma_f32_16x16x32_bf16 v[196:199], a[24:27], v[184:187], v[196:199]\n"
"buffer_load_dwordx4 v249, s[44:47], s76 offen offset:1024 nt lds\n"
"v_mfma_f32_16x16x32_bf16 v[196:199], a[20:23], v[180:183], v[196:199]\n"
"v_exp_f32 v231, v231\n"
"v_mfma_f32_16x16x32_bf16 v[196:199], a[16:19], v[176:179], v[196:199]\n"
"ds_read_b128 a[12:15], v244 offset:52416\n"
"v_perm_b32 v207, v231, v230, s72\n"
"v_mfma_f32_16x16x32_bf16 v[224:227], a[32:35], v[176:179], -4.0\n"
"v_exp_f32 v192, v192\n"
"v_mfma_f32_16x16x32_bf16 v[224:227], a[36:39], v[180:183], v[224:227]\n"
"ds_read_b128 a[28:31], v244 offset:56768\n"
"ds_read_b128 a[24:27], v244 offset:56704\n"
"v_mfma_f32_16x16x32_bf16 v[224:227], a[40:43], v[184:187], v[224:227]\n"
"v_exp_f32 v193, v193\n"
"v_mfma_f32_16x16x32_bf16 v[224:227], a[44:47], v[188:191], v[224:227]\n"
"ds_read_b128 a[20:23], v244 offset:56640\n"
"v_perm_b32 v208, v193, v192, s72\n"
"v_mfma_f32_16x16x32_bf16 v[228:231], a[60:63], v[188:191], -4.0\n"
"s_add_u32 m0, s55, 16\n"
"v_exp_f32 v194, v194\n"
"v_mfma_f32_16x16x32_bf16 v[228:231], a[56:59], v[184:187], v[228:231]\n"
"buffer_load_dwordx4 v249, s[44:47], s76 offen offset:2048 nt lds\n"
"v_mfma_f32_16x16x32_bf16 v[228:231], a[52:55], v[180:183], v[228:231]\n"
"v_exp_f32 v195, v195\n"
"v_mfma_f32_16x16x32_bf16 v[228:231], a[48:51], v[176:179], v[228:231]\n"
"ds_read_b128 a[16:19], v244 offset:56576\n"
"v_perm_b32 v209, v195, v194, s72\n"
"v_smfmac_f32_16x16x64_bf16 a[136:139], a[144:147], v[200:207], v251\n"
"ds_read_b128 a[32:35], v222 offset:52224\n"
"v_mfma_f32_16x16x32_bf16 v[64:67], a[64:67], v[200:203], v[64:67]\n"
"v_exp_f32 v196, v196\n"
"v_mfma_f32_16x16x32_bf16 v[64:67], a[68:71], v[204:207], v[64:67]\n"
"ds_read_b128 a[36:39], v222 offset:52288\n"
"ds_read_b128 a[40:43], v222 offset:52352\n"
"v_mfma_f32_16x16x32_bf16 v[68:71], a[76:79], v[204:207], v[68:71]\n"
"v_exp_f32 v197, v197\n"
"v_mfma_f32_16x16x32_bf16 v[68:71], a[72:75], v[200:203], v[68:71]\n"
"ds_read_b128 a[44:47], v222 offset:52416\n"
"v_perm_b32 v210, v197, v196, s72\n"
"v_mfma_f32_16x16x32_bf16 v[72:75], a[80:83], v[200:203], v[72:75]\n"
"s_add_u32 m0, s55, 24\n"
"v_exp_f32 v198, v198\n"
"v_mfma_f32_16x16x32_bf16 v[72:75], a[84:87], v[204:207], v[72:75]\n"
"buffer_load_dwordx4 v249, s[44:47], s76 offen offset:3072 nt lds\n"
"v_mfma_f32_16x16x32_bf16 v[76:79], a[92:95], v[204:207], v[76:79]\n"
"s_mov_b64 s[76:77], s[78:79]\n"
"v_exp_f32 v199, v199\n"
"v_mfma_f32_16x16x32_bf16 v[76:79], a[88:91], v[200:203], v[76:79]\n"
"s_mov_b64 s[78:79], s[80:81]\n"
"ds_read_b128 a[60:63], v222 offset:56768\n"
"v_perm_b32 v211, v199, v198, s72\n"
"v_mfma_f32_16x16x32_bf16 v[80:83], a[96:99], v[200:203], v[80:83]\n"
"s_mov_b64 s[80:81], s[82:83]\n"
"v_exp_f32 v224, v224\n"
"v_mfma_f32_16x16x32_bf16 v[80:83], a[100:103], v[204:207], v[80:83]\n"
"s_cmp_lt_u32 s56, 2\n"
"ds_read_b128 a[56:59], v222 offset:56704\n"
"ds_read_b128 a[52:55], v222 offset:56640\n"
"v_mfma_f32_16x16x32_bf16 v[84:87], a[108:111], v[204:207], v[84:87]\n"
"s_cselect_b32 s46, 65011712, 0\n"
"v_exp_f32 v225, v225\n"
"v_mfma_f32_16x16x32_bf16 v[84:87], a[104:107], v[200:203], v[84:87]\n"
"ds_read_b128 a[48:51], v222 offset:56576\n"
"v_perm_b32 v212, v225, v224, s72\n"
"v_mfma_f32_16x16x32_bf16 v[88:91], a[112:115], v[200:203], v[88:91]\n"
"s_add_u32 m0, s54, 16512\n"
"v_exp_f32 v226, v226\n"
"v_mfma_f32_16x16x32_bf16 v[88:91], a[116:119], v[204:207], v[88:91]\n"
"buffer_load_dwordx4 v248, s[40:43], s52 offen offset:0 lds\n"
"v_mfma_f32_16x16x32_bf16 v[92:95], a[124:127], v[204:207], v[92:95]\n"
"v_exp_f32 v227, v227\n"
"v_mfma_f32_16x16x32_bf16 v[92:95], a[120:123], v[200:203], v[92:95]\n"
"s_waitcnt lgkmcnt(4)\n"
"v_perm_b32 v213, v227, v226, s72\n"
"v_mfma_f32_16x16x32_bf16 v[192:195], a[0:3], v[128:131], -4.0\n"
"v_exp_f32 v228, v228\n"
"v_mfma_f32_16x16x32_bf16 v[192:195], a[4:7], v[132:135], v[192:195]\n"
"v_mfma_f32_16x16x32_bf16 v[192:195], a[8:11], v[136:139], v[192:195]\n"
"v_exp_f32 v229, v229\n"
"v_mfma_f32_16x16x32_bf16 v[192:195], a[12:15], v[140:143], v[192:195]\n"
"v_perm_b32 v214, v229, v228, s72\n"
"v_mfma_f32_16x16x32_bf16 v[196:199], a[28:31], v[140:143], -4.0\n"
"s_add_u32 m0, s54, 16520\n"
"v_exp_f32 v230, v230\n"
"v_mfma_f32_16x16x32_bf16 v[196:199], a[24:27], v[136:139], v[196:199]\n"
"buffer_load_dwordx4 v248, s[40:43], s52 offen offset:1024 lds\n"
"v_mfma_f32_16x16x32_bf16 v[196:199], a[20:23], v[132:135], v[196:199]\n"
"v_exp_f32 v231, v231\n"
"v_mfma_f32_16x16x32_bf16 v[196:199], a[16:19], v[128:131], v[196:199]\n"
"v_perm_b32 v215, v231, v230, s72\n"
"v_mfma_f32_16x16x32_bf16 v[224:227], a[32:35], v[128:131], -4.0\n"
"v_exp_f32 v192, v192\n"
"v_mfma_f32_16x16x32_bf16 v[224:227], a[36:39], v[132:135], v[224:227]\n"
"v_mfma_f32_16x16x32_bf16 v[224:227], a[40:43], v[136:139], v[224:227]\n"
"v_exp_f32 v193, v193\n"
"v_mfma_f32_16x16x32_bf16 v[224:227], a[44:47], v[140:143], v[224:227]\n"
"s_waitcnt lgkmcnt(0)\n"
"v_perm_b32 v200, v193, v192, s72\n"
"v_mfma_f32_16x16x32_bf16 v[228:231], a[60:63], v[140:143], -4.0\n"
"s_add_u32 m0, s54, 18448\n"
"v_exp_f32 v194, v194\n"
"v_mfma_f32_16x16x32_bf16 v[228:231], a[56:59], v[136:139], v[228:231]\n"
"buffer_load_dwordx4 v248, s[40:43], s52 offen offset:128 lds\n"
"v_mfma_f32_16x16x32_bf16 v[228:231], a[52:55], v[132:135], v[228:231]\n"
"v_exp_f32 v195, v195\n"
"v_mfma_f32_16x16x32_bf16 v[228:231], a[48:51], v[128:131], v[228:231]\n"
"v_perm_b32 v201, v195, v194, s72\n"
"v_smfmac_f32_16x16x64_bf16 a[140:143], a[144:147], v[208:215], v251\n"
"v_mfma_f32_16x16x32_bf16 v[96:99], a[64:67], v[208:211], v[96:99]\n"
"v_exp_f32 v196, v196\n"
"v_mfma_f32_16x16x32_bf16 v[96:99], a[68:71], v[212:215], v[96:99]\n"
"ds_read_b64_tr_b16 a[64:65], v245 offset:49536\n"
"ds_read_b64_tr_b16 a[66:67], v245 offset:53664\n"
"v_mfma_f32_16x16x32_bf16 v[100:103], a[76:79], v[212:215], v[100:103]\n"
"v_exp_f32 v197, v197\n"
"v_mfma_f32_16x16x32_bf16 v[100:103], a[72:75], v[208:211], v[100:103]\n"
"ds_read_b64_tr_b16 a[68:69], v255 offset:49536\n"
"v_perm_b32 v202, v197, v196, s72\n"
"v_mfma_f32_16x16x32_bf16 v[104:107], a[80:83], v[208:211], v[104:107]\n"
"v_exp_f32 v198, v198\n"
"v_mfma_f32_16x16x32_bf16 v[104:107], a[84:87], v[212:215], v[104:107]\n"
"ds_read_b64_tr_b16 a[70:71], v255 offset:53664\n"
"ds_read_b64_tr_b16 a[76:77], v255 offset:49544\n"
"v_mfma_f32_16x16x32_bf16 v[108:111], a[92:95], v[212:215], v[108:111]\n"
"v_exp_f32 v199, v199\n"
"v_mfma_f32_16x16x32_bf16 v[108:111], a[88:91], v[208:211], v[108:111]\n"
"ds_read_b64_tr_b16 a[78:79], v255 offset:53672\n"
"v_perm_b32 v203, v199, v198, s72\n"
"v_mfma_f32_16x16x32_bf16 v[112:115], a[96:99], v[208:211], v[112:115]\n"
"v_exp_f32 v224, v224\n"
"v_mfma_f32_16x16x32_bf16 v[112:115], a[100:103], v[212:215], v[112:115]\n"
"ds_read_b64_tr_b16 a[72:73], v245 offset:49544\n"
"ds_read_b64_tr_b16 a[74:75], v245 offset:53672\n"
"v_mfma_f32_16x16x32_bf16 v[116:119], a[108:111], v[212:215], v[116:119]\n"
"v_exp_f32 v225, v225\n"
"v_mfma_f32_16x16x32_bf16 v[116:119], a[104:107], v[208:211], v[116:119]\n"
"ds_read_b64_tr_b16 a[80:81], v245 offset:49792\n"
"v_perm_b32 v204, v225, v224, s72\n"
"v_mfma_f32_16x16x32_bf16 v[120:123], a[112:115], v[208:211], v[120:123]\n"
"v_exp_f32 v226, v226\n"
"v_mfma_f32_16x16x32_bf16 v[120:123], a[116:119], v[212:215], v[120:123]\n"
"ds_read_b64_tr_b16 a[82:83], v245 offset:53920\n"
"ds_read_b64_tr_b16 a[84:85], v255 offset:49792\n"
"v_mfma_f32_16x16x32_bf16 v[124:127], a[124:127], v[212:215], v[124:127]\n"
"v_exp_f32 v227, v227\n"
"v_mfma_f32_16x16x32_bf16 v[124:127], a[120:123], v[208:211], v[124:127]\n"
"s_cmp_eq_u32 s56, 3\n"
"ds_read_b64_tr_b16 a[86:87], v255 offset:53920\n"
"v_perm_b32 v205, v227, v226, s72\n"
"s_cbranch_scc1 LTN%=\n"
"v_mfma_f32_16x16x32_bf16 v[192:195], a[0:3], v[144:147], -4.0\n"
"v_exp_f32 v228, v228\n"
"v_mfma_f32_16x16x32_bf16 v[192:195], a[4:7], v[148:151], v[192:195]\n"
"ds_read_b64_tr_b16 a[92:93], v255 offset:49800\n"
"ds_read_b64_tr_b16 a[94:95], v255 offset:53928\n"
"v_mfma_f32_16x16x32_bf16 v[192:195], a[8:11], v[152:155], v[192:195]\n"
"v_exp_f32 v229, v229\n"
"v_mfma_f32_16x16x32_bf16 v[192:195], a[12:15], v[156:159], v[192:195]\n"
"ds_read_b64_tr_b16 a[88:89], v245 offset:49800\n"
"v_perm_b32 v206, v229, v228, s72\n"
"v_mfma_f32_16x16x32_bf16 v[196:199], a[28:31], v[156:159], -4.0\n"
"v_exp_f32 v230, v230\n"
"v_mfma_f32_16x16x32_bf16 v[196:199], a[24:27], v[152:155], v[196:199]\n"
"ds_read_b64_tr_b16 a[90:91], v245 offset:53928\n"
"ds_read_b64_tr_b16 a[96:97], v245 offset:51600\n"
"v_mfma_f32_16x16x32_bf16 v[196:199], a[20:23], v[148:151], v[196:199]\n"
"v_exp_f32 v231, v231\n"
"v_mfma_f32_16x16x32_bf16 v[196:199], a[16:19], v[144:147], v[196:199]\n"
"ds_read_b64_tr_b16 a[98:99], v245 offset:55728\n"
"v_perm_b32 v207, v231, v230, s72\n"
"v_mfma_f32_16x16x32_bf16 v[224:227], a[32:35], v[144:147], -4.0\n"
"v_exp_f32 v192, v192\n"
"v_mfma_f32_16x16x32_bf16 v[224:227], a[36:39], v[148:151], v[224:227]\n"
"ds_read_b64_tr_b16 a[100:101], v255 offset:51600\n"
"ds_read_b64_tr_b16 a[102:103], v255 offset:55728\n"
"v_mfma_f32_16x16x32_bf16 v[224:227], a[40:43], v[152:155], v[224:227]\n"
"v_exp_f32 v193, v193\n"
"v_mfma_f32_16x16x32_bf16 v[224:227], a[44:47], v[156:159], v[224:227]\n"
"ds_read_b64_tr_b16 a[108:109], v255 offset:51608\n"
"v_perm_b32 v208, v193, v192, s72\n"
"v_mfma_f32_16x16x32_bf16 v[228:231], a[60:63], v[156:159], -4.0\n"
"v_exp_f32 v194, v194\n"
"v_mfma_f32_16x16x32_bf16 v[228:231], a[56:59], v[152:155], v[228:231]\n"
"ds_read_b64_tr_b16 a[110:111], v255 offset:55736\n"
"ds_read_b64_tr_b16 a[104:105], v245 offset:51608\n"
"v_mfma_f32_16x16x32_bf16 v[228:231], a[52:55], v[148:151], v[228:231]\n"
"v_exp_f32 v195, v195\n"
"v_mfma_f32_16x16x32_bf16 v[228:231], a[48:51], v[144:147], v[228:231]\n"
"ds_read_b64_tr_b16 a[106:107], v245 offset:55736\n"
"v_perm_b32 v209, v195, v194, s72\n"
"v_smfmac_f32_16x16x64_bf16 a[128:131], a[144:147], v[200:207], v251\n"
"s_waitcnt lgkmcnt(7)\n"
"ds_read_b64_tr_b16 a[112:113], v245 offset:51856\n"
"v_mfma_f32_16x16x32_bf16 v[0:3], a[64:67], v[200:203], v[0:3]\n"
"v_exp_f32 v196, v196\n"
"v_mfma_f32_16x16x32_bf16 v[0:3], a[68:71], v[204:207], v[0:3]\n"
"ds_read_b64_tr_b16 a[114:115], v245 offset:55984\n"
"ds_read_b64_tr_b16 a[116:117], v255 offset:51856\n"
"v_mfma_f32_16x16x32_bf16 v[4:7], a[76:79], v[204:207], v[4:7]\n"
"v_exp_f32 v197, v197\n"
"v_mfma_f32_16x16x32_bf16 v[4:7], a[72:75], v[200:203], v[4:7]\n"
"ds_read_b64_tr_b16 a[118:119], v255 offset:55984\n"
"v_perm_b32 v210, v197, v196, s72\n"
"v_mfma_f32_16x16x32_bf16 v[8:11], a[80:83], v[200:203], v[8:11]\n"
"v_exp_f32 v198, v198\n"
"v_mfma_f32_16x16x32_bf16 v[8:11], a[84:87], v[204:207], v[8:11]\n"
"ds_read_b64_tr_b16 a[124:125], v255 offset:51864\n"
"ds_read_b64_tr_b16 a[126:127], v255 offset:55992\n"
"v_mfma_f32_16x16x32_bf16 v[12:15], a[92:95], v[204:207], v[12:15]\n"
"Lhop1_2_%=:\n s_cbranch_execz Lhop1_6_%=\n"
"v_exp_f32 v199, v199\n"
"v_mfma_f32_16x16x32_bf16 v[12:15], a[88:91], v[200:203], v[12:15]\n"
"s_waitcnt lgkmcnt(7)\n"
"ds_read_b64_tr_b16 a[120:121], v245 offset:51864\n"
"v_perm_b32 v211, v199, v198, s72\n"
"v_mfma_f32_16x16x32_bf16 v[16:19], a[96:99], v[200:203], v[16:19]\n"
"v_exp_f32 v224, v224\n"
"v_mfma_f32_16x16x32_bf16 v[16:19], a[100:103], v[204:207], v[16:19]\n"
"ds_read_b64_tr_b16 a[122:123], v245 offset:55992\n"
"v_accvgpr_read_b32 v254, a128\n"
"v_mfma_f32_16x16x32_bf16 v[20:23], a[108:111], v[204:207], v[20:23]\n"
"s_waitcnt lgkmcnt(7)\n"
"v_exp_f32 v225, v225\n"
"v_mfma_f32_16x16x32_bf16 v[20:23], a[104:107], v[200:203], v[20:23]\n"
"s_waitcnt lgkmcnt(2)\n"
"v_perm_b32 v212, v225, v224, s72\n"
"v_accvgpr_read_b32 v252, a129\n"
"v_add_f32 v254, v254, v252\n"
"v_rcp_f32 v252, v254\n"
"v_accvgpr_write_b32 a128, 0\n"
"v_accvgpr_write_b32 a129, 0\n"
"v_max_f32 v254, v254, v252\n"
"v_cmp_nlt_f32 s[62:63], v254, s58\n"
"v_mul_f32 v223, v218, v252\n"
"v_mul_f32 v254, 0x3b000000, v223\n"
"v_fma_f32 v0, v0, v252, -v223\n"
"v_fma_f32 v1, v1, v252, -v223\n"
"v_fma_f32 v2, v2, v252, -v223\n"
"v_fma_f32 v3, v3, v252, -v223\n"
"v_fma_f32 v4, v4, v252, -v223\n"
"v_fma_f32 v5, v5, v252, -v223\n"
"v_fma_f32 v6, v6, v252, -v223\n"
"v_fma_f32 v7, v7, v252, -v223\n"
"v_max3_f32 v253, abs(v0), abs(v1), abs(v2)\n"
"v_cmp_lt_f32 vcc, v253, v254\n"
"v_cvt_pk_bf16_f32 v0, v0, v1\n"
"v_cvt_pk_bf16_f32 v1, v2, v3\n"
"v_cvt_pk_bf16_f32 v2, v4, v5\n"
"v_cvt_pk_bf16_f32 v3, v6, v7\n"
"v_mfma_f32_16x16x32_bf16 v[24:27], a[112:115], v[200:203], v[24:27]\n"
"s_add_u32 m0, s54, 18456\n"
"v_exp_f32 v226, v226\n"
"v_mfma_f32_16x16x32_bf16 v[24:27], a[116:119], v[204:207], v[24:27]\n"
"s_and_b32 s73, vcc_lo, vcc_hi\n"
"buffer_load_dwordx4 v248, s[40:43], s52 offen offset:1152 lds\n"
"v_mfma_f32_16x16x32_bf16 v[28:31], a[124:127], v[204:207], v[28:31]\n"
"s_add_u32 s52, s52, 16384\n"
"v_exp_f32 v227, v227\n"
"s_waitcnt lgkmcnt(0)\n"
"v_mfma_f32_16x16x32_bf16 v[28:31], a[120:123], v[200:203], v[28:31]\n"
"s_lshr_b32 s74, s73, 16\n"
"ds_write_b128 v216, v[0:3] offset:0\n"
"v_perm_b32 v213, v227, v226, s72\n"
"v_fma_f32 v8, v8, v252, -v223\n"
"v_mfma_f32_16x16x32_bf16 v[192:195], a[0:3], v[160:163], -4.0\n"
"s_and_b32 s73, s73, s74\n"
"v_exp_f32 v228, v228\n"
"v_mfma_f32_16x16x32_bf16 v[192:195], a[4:7], v[164:167], v[192:195]\n"
"s_or_b32 s73, s73, s62\n"
"v_fma_f32 v9, v9, v252, -v223\n"
"v_fma_f32 v10, v10, v252, -v223\n"
"v_mfma_f32_16x16x32_bf16 v[192:195], a[8:11], v[168:171], v[192:195]\n"
"s_or_b32 s73, s73, s63\n"
"v_exp_f32 v229, v229\n"
"v_mfma_f32_16x16x32_bf16 v[192:195], a[12:15], v[172:175], v[192:195]\n"
"s_addc_u32 s57, s57, s57\n"
"v_perm_b32 v214, v229, v228, s72\n"
"v_fma_f32 v11, v11, v252, -v223\n"
"v_mfma_f32_16x16x32_bf16 v[232:235], a[148:151], a[152:155], 0\n"
"v_fma_f32 v12, v12, v252, -v223\n"
"v_fma_f32 v13, v13, v252, -v223\n"
"v_mfma_f32_16x16x32_bf16 v[236:239], a[148:151], a[156:159], 0\n"
"v_fma_f32 v14, v14, v252, -v223\n"
"v_fma_f32 v15, v15, v252, -v223\n"
"v_mfma_f32_16x16x32_bf16 v[196:199], a[28:31], v[172:175], -4.0\n"
"s_add_u32 m0, s53, 34816\n"
"v_exp_f32 v230, v230\n"
"v_mfma_f32_16x16x32_bf16 v[196:199], a[24:27], v[168:171], v[196:199]\n"
"buffer_load_dwordx4 v247, s[36:39], s52 offen offset:0 lds\n"
"v_mfma_f32_16x16x32_bf16 v[196:199], a[20:23], v[164:167], v[196:199]\n"
"v_exp_f32 v231, v231\n"
"v_mfma_f32_16x16x32_bf16 v[196:199], a[16:19], v[160:163], v[196:199]\n"
"v_perm_b32 v215, v231, v230, s72\n"
"v_cvt_pk_bf16_f32 v8, v8, v9\n"
"v_cvt_pk_bf16_f32 v9, v10, v11\n"
"v_cvt_pk_bf16_f32 v10, v12, v13\n"
"v_cvt_pk_bf16_f32 v11, v14, v15\n"
"v_mfma_f32_16x16x32_bf16 v[224:227], a[32:35], v[160:163], -4.0\n"
"v_exp_f32 v192, v192\n"
"v_mfma_f32_16x16x32_bf16 v[224:227], a[36:39], v[164:167], v[224:227]\n"
"v_fma_f32 v16, v16, v252, -v223\n"
"v_fma_f32 v17, v17, v252, -v223\n"
"v_mfma_f32_16x16x32_bf16 v[224:227], a[40:43], v[168:171], v[224:227]\n"
"v_exp_f32 v193, v193\n"
"v_mfma_f32_16x16x32_bf16 v[224:227], a[44:47], v[172:175], v[224:227]\n"
"ds_write_b128 v216, v[8:11] offset:64\n"
"v_perm_b32 v200, v193, v192, s72\n"
"v_fma_f32 v18, v18, v252, -v223\n"
"v_mfma_f32_16x16x32_bf16 v[240:243], a[148:151], a[160:163], 0\n"
"ds_read_b128 v[4:7], v217 offset:0\n"
"ds_read_b128 v[12:15], v217 offset:576\n"
"v_cvt_pk_bf16_f32 v128, v232, v233\n"
"v_cvt_pk_bf16_f32 v129, v234, v235\n"
"v_mfma_f32_16x16x32_bf16 v[232:235], a[148:151], a[164:167], 0\n"
"v_cvt_pk_bf16_f32 v130, v236, v237\n"
"v_cvt_pk_bf16_f32 v131, v238, v239\n"
"v_mfma_f32_16x16x32_bf16 v[228:231], a[60:63], v[172:175], -4.0\n"
"s_add_u32 m0, s53, 34880\n"
"v_exp_f32 v194, v194\n"
"v_mfma_f32_16x16x32_bf16 v[228:231], a[56:59], v[168:171], v[228:231]\n"
"buffer_load_dwordx4 v247, s[36:39], s52 offen offset:1024 lds\n"
"v_mfma_f32_16x16x32_bf16 v[228:231], a[52:55], v[164:167], v[228:231]\n"
"v_exp_f32 v195, v195\n"
"v_mfma_f32_16x16x32_bf16 v[228:231], a[48:51], v[160:163], v[228:231]\n"
"s_waitcnt vmcnt(14) lgkmcnt(4)\n"
"v_perm_b32 v201, v195, v194, s72\n"
"v_fma_f32 v19, v19, v252, -v223\n"
"v_fma_f32 v20, v20, v252, -v223\n"
"v_fma_f32 v21, v21, v252, -v223\n"
"v_fma_f32 v22, v22, v252, -v223\n"
"v_fma_f32 v23, v23, v252, -v223\n"
"v_cvt_pk_bf16_f32 v16, v16, v17\n"
"v_cvt_pk_bf16_f32 v17, v18, v19\n"
"v_cvt_pk_bf16_f32 v18, v20, v21\n"
"v_cvt_pk_bf16_f32 v19, v22, v23\n"
"v_smfmac_f32_16x16x64_bf16 a[132:135], a[144:147], v[208:215], v251\n"
"ds_write_b128 v216, v[16:19] offset:0\n"
"v_fma_f32 v24, v24, v252, -v223\n"
"v_mfma_f32_16x16x32_bf16 v[32:35], a[64:67], v[208:211], v[32:35]\n"
"v_exp_f32 v196, v196\n"
"v_mfma_f32_16x16x32_bf16 v[32:35], a[68:71], v[212:215], v[32:35]\n"
"s_barrier\n"
"v_fma_f32 v25, v25, v252, -v223\n"
"v_fma_f32 v26, v26, v252, -v223\n"
"v_mfma_f32_16x16x32_bf16 v[36:39], a[76:79], v[212:215], v[36:39]\n"
"v_exp_f32 v197, v197\n"
"v_mfma_f32_16x16x32_bf16 v[36:39], a[72:75], v[208:211], v[36:39]\n"
"v_perm_b32 v202, v197, v196, s72\n"
"v_fma_f32 v27, v27, v252, -v223\n"
"v_mfma_f32_16x16x32_bf16 v[236:239], a[148:151], a[168:171], 0\n"
"v_cvt_pk_bf16_f32 v132, v240, v241\n"
"v_cvt_pk_bf16_f32 v133, v242, v243\n"
"v_mfma_f32_16x16x32_bf16 v[240:243], a[148:151], a[172:175], 0\n"
"v_cvt_pk_bf16_f32 v134, v232, v233\n"
"v_cvt_pk_bf16_f32 v135, v234, v235\n"
"v_mfma_f32_16x16x32_bf16 v[40:43], a[80:83], v[208:211], v[40:43]\n"
"s_add_u32 m0, s53, 34944\n"
"v_exp_f32 v198, v198\n"
"v_mfma_f32_16x16x32_bf16 v[40:43], a[84:87], v[212:215], v[40:43]\n"
"buffer_load_dwordx4 v247, s[36:39], s52 offen offset:2048 lds\n"
"v_mfma_f32_16x16x32_bf16 v[44:47], a[92:95], v[212:215], v[44:47]\n"
"v_exp_f32 v199, v199\n"
"v_mfma_f32_16x16x32_bf16 v[44:47], a[88:91], v[208:211], v[44:47]\n"
"v_perm_b32 v203, v199, v198, s72\n"
"v_fma_f32 v28, v28, v252, -v223\n"
"v_mfma_f32_16x16x32_bf16 v[48:51], a[96:99], v[208:211], v[48:51]\n"
"v_exp_f32 v224, v224\n"
"v_mfma_f32_16x16x32_bf16 v[48:51], a[100:103], v[212:215], v[48:51]\n"
"v_fma_f32 v29, v29, v252, -v223\n"
"v_fma_f32 v30, v30, v252, -v223\n"
"v_fma_f32 v31, v31, v252, -v223\n"
"v_cvt_pk_bf16_f32 v24, v24, v25\n"
"v_cvt_pk_bf16_f32 v25, v26, v27\n"
"v_cvt_pk_bf16_f32 v26, v28, v29\n"
"v_cvt_pk_bf16_f32 v27, v30, v31\n"
"v_mfma_f32_16x16x32_bf16 v[52:55], a[108:111], v[212:215], v[52:55]\n"
"v_exp_f32 v225, v225\n"
"v_mfma_f32_16x16x32_bf16 v[52:55], a[104:107], v[208:211], v[52:55]\n"
"ds_write_b128 v216, v[24:27] offset:64\n"
"v_perm_b32 v204, v225, v224, s72\n"
"v_accvgpr_read_b32 v254, a132\n"
"v_mfma_f32_16x16x32_bf16 v[232:235], a[148:151], a[176:179], 0\n"
"v_cvt_pk_bf16_f32 v136, v236, v237\n"
"v_cvt_pk_bf16_f32 v137, v238, v239\n"
"v_mfma_f32_16x16x32_bf16 v[236:239], a[148:151], a[180:183], 0\n"
"v_cvt_pk_bf16_f32 v138, v240, v241\n"
"v_cvt_pk_bf16_f32 v139, v242, v243\n"
"v_mfma_f32_16x16x32_bf16 v[56:59], a[112:115], v[208:211], v[56:59]\n"
"s_add_u32 m0, s53, 35008\n"
"v_exp_f32 v226, v226\n"
"v_mfma_f32_16x16x32_bf16 v[56:59], a[116:119], v[212:215], v[56:59]\n"
"buffer_load_dwordx4 v247, s[36:39], s52 offen offset:3072 lds\n"
"v_mfma_f32_16x16x32_bf16 v[60:63], a[124:127], v[212:215], v[60:63]\n"
"v_exp_f32 v227, v227\n"
"v_mfma_f32_16x16x32_bf16 v[60:63], a[120:123], v[208:211], v[60:63]\n"
"ds_read_b128 v[20:23], v217 offset:0\n"
"ds_read_b128 v[28:31], v217 offset:576\n"
"v_perm_b32 v205, v227, v226, s72\n"
"v_mfma_f32_16x16x32_bf16 v[192:195], a[0:3], v[176:179], -4.0\n"
"v_exp_f32 v228, v228\n"
"v_mfma_f32_16x16x32_bf16 v[192:195], a[4:7], v[180:183], v[192:195]\n"
"s_add_u32 s56, s56, 1\n"
"ds_read_b128 a[0:3], v244 offset:0\n"
"ds_read_b128 a[4:7], v244 offset:64\n"
"v_mfma_f32_16x16x32_bf16 v[192:195], a[8:11], v[184:187], v[192:195]\n"
"v_exp_f32 v229, v229\n"
"v_mfma_f32_16x16x32_bf16 v[192:195], a[12:15], v[188:191], v[192:195]\n"
"ds_read_b128 a[8:11], v244 offset:128\n"
"v_perm_b32 v206, v229, v228, s72\n"
"v_accvgpr_read_b32 v252, a133\n"
"v_add_f32 v254, v254, v252\n"
"v_rcp_f32 v252, v254\n"
"v_accvgpr_write_b32 a132, 0\n"
"v_accvgpr_write_b32 a133, 0\n"
"v_max_f32 v254, v254, v252\n"
"v_cmp_nlt_f32 s[62:63], v254, s58\n"
"v_mul_f32 v223, v218, v252\n"
"v_mul_f32 v254, 0x3b000000, v223\n"
"v_fma_f32 v32, v32, v252, -v223\n"
"v_fma_f32 v33, v33, v252, -v223\n"
"v_fma_f32 v34, v34, v252, -v223\n"
"v_fma_f32 v35, v35, v252, -v223\n"
"v_fma_f32 v36, v36, v252, -v223\n"
"v_fma_f32 v37, v37, v252, -v223\n"
"v_fma_f32 v38, v38, v252, -v223\n"
"v_fma_f32 v39, v39, v252, -v223\n"
"v_max3_f32 v253, abs(v32), abs(v33), abs(v34)\n"
"v_cmp_lt_f32 vcc, v253, v254\n"
"v_cvt_pk_bf16_f32 v32, v32, v33\n"
"v_cvt_pk_bf16_f32 v33, v34, v35\n"
"v_cvt_pk_bf16_f32 v34, v36, v37\n"
"v_cvt_pk_bf16_f32 v35, v38, v39\n"
"v_mfma_f32_16x16x32_bf16 v[240:243], a[148:151], a[184:187], 0\n"
"s_and_b32 s73, vcc_lo, vcc_hi\n"
"ds_write_b128 v216, v[32:35] offset:0\n"
"v_cvt_pk_bf16_f32 v140, v232, v233\n"
"v_cvt_pk_bf16_f32 v141, v234, v235\n"
"v_mfma_f32_16x16x32_bf16 v[232:235], a[148:151], a[188:191], 0\n"
"s_lshr_b32 s74, s73, 16\n"
"v_cvt_pk_bf16_f32 v142, v236, v237\n"
"v_cvt_pk_bf16_f32 v143, v238, v239\n"
"v_mfma_f32_16x16x32_bf16 v[196:199], a[28:31], v[188:191], -4.0\n"
"s_add_u32 m0, s54, 33024\n"
"v_exp_f32 v230, v230\n"
"v_mfma_f32_16x16x32_bf16 v[196:199], a[24:27], v[184:187], v[196:199]\n"
"s_and_b32 s73, s73, s74\n"
"buffer_load_dwordx4 v248, s[40:43], s52 offen offset:0 lds\n"
"v_mfma_f32_16x16x32_bf16 v[196:199], a[20:23], v[180:183], v[196:199]\n"
"s_or_b32 s73, s73, s62\n"
"v_exp_f32 v231, v231\n"
"v_mfma_f32_16x16x32_bf16 v[196:199], a[16:19], v[176:179], v[196:199]\n"
"s_or_b32 s73, s73, s63\n"
"s_waitcnt lgkmcnt(9)\n"
"buffer_store_dwordx4 v[4:7], v250, s[48:51], s84 offen offset:0 sc1\n"
"v_perm_b32 v207, v231, v230, s72\n"
"v_mfma_f32_16x16x32_bf16 v[224:227], a[32:35], v[176:179], -4.0\n"
"s_addc_u32 s57, s57, s57\n"
"v_exp_f32 v192, v192\n"
"v_mfma_f32_16x16x32_bf16 v[224:227], a[36:39], v[180:183], v[224:227]\n"
"ds_read_b128 a[12:15], v244 offset:192\n"
"ds_read_b128 a[28:31], v244 offset:4544\n"
"v_mfma_f32_16x16x32_bf16 v[224:227], a[40:43], v[184:187], v[224:227]\n"
"v_exp_f32 v193, v193\n"
"v_mfma_f32_16x16x32_bf16 v[224:227], a[44:47], v[188:191], v[224:227]\n"
"ds_read_b128 a[24:27], v244 offset:4480\n"
"v_perm_b32 v208, v193, v192, s72\n"
"v_mfma_f32_16x16x32_bf16 v[236:239], a[148:151], a[192:195], 0\n"
"v_cvt_pk_bf16_f32 v144, v240, v241\n"
"v_cvt_pk_bf16_f32 v145, v242, v243\n"
"v_mfma_f32_16x16x32_bf16 v[240:243], a[148:151], a[196:199], 0\n"
"v_cvt_pk_bf16_f32 v146, v232, v233\n"
"v_cvt_pk_bf16_f32 v147, v234, v235\n"
"v_mfma_f32_16x16x32_bf16 v[228:231], a[60:63], v[188:191], -4.0\n"
"s_add_u32 m0, s54, 33032\n"
"v_exp_f32 v194, v194\n"
"v_mfma_f32_16x16x32_bf16 v[228:231], a[56:59], v[184:187], v[228:231]\n"
"buffer_load_dwordx4 v248, s[40:43], s52 offen offset:1024 lds\n"
"v_mfma_f32_16x16x32_bf16 v[228:231], a[52:55], v[180:183], v[228:231]\n"
"v_exp_f32 v195, v195\n"
"v_mfma_f32_16x16x32_bf16 v[228:231], a[48:51], v[176:179], v[228:231]\n"
"s_waitcnt vmcnt(10)\n"
"ds_read_b128 a[20:23], v244 offset:4416\n"
"v_perm_b32 v209, v195, v194, s72\n"
"v_fma_f32 v40, v40, v252, -v223\n"
"v_fma_f32 v41, v41, v252, -v223\n"
"v_fma_f32 v42, v42, v252, -v223\n"
"v_fma_f32 v43, v43, v252, -v223\n"
"v_fma_f32 v44, v44, v252, -v223\n"
"v_fma_f32 v45, v45, v252, -v223\n"
"v_fma_f32 v46, v46, v252, -v223\n"
"v_fma_f32 v47, v47, v252, -v223\n"
"v_cvt_pk_bf16_f32 v40, v40, v41\n"
"v_cvt_pk_bf16_f32 v41, v42, v43\n"
"v_cvt_pk_bf16_f32 v42, v44, v45\n"
"v_cvt_pk_bf16_f32 v43, v46, v47\n"
"v_smfmac_f32_16x16x64_bf16 a[136:139], a[144:147], v[200:207], v251\n"
"ds_read_b128 a[16:19], v244 offset:4352\n"
"v_mfma_f32_16x16x32_bf16 v[64:67], a[64:67], v[200:203], v[64:67]\n"
"v_exp_f32 v196, v196\n"
"v_mfma_f32_16x16x32_bf16 v[64:67], a[68:71], v[204:207], v[64:67]\n"
"ds_read_b128 a[32:35], v222 offset:0\n"
"ds_read_b128 a[36:39], v222 offset:64\n"
"ds_write_b128 v216, v[40:43] offset:64\n"
"v_mfma_f32_16x16x32_bf16 v[68:71], a[76:79], v[204:207], v[68:71]\n"
"v_exp_f32 v197, v197\n"
"v_mfma_f32_16x16x32_bf16 v[68:71], a[72:75], v[200:203], v[68:71]\n"
"ds_read_b128 a[40:43], v222 offset:128\n"
"v_perm_b32 v210, v197, v196, s72\n"
"v_mfma_f32_16x16x32_bf16 v[232:235], a[148:151], a[200:203], 0\n"
"v_cvt_pk_bf16_f32 v148, v236, v237\n"
"v_cvt_pk_bf16_f32 v149, v238, v239\n"
"v_mfma_f32_16x16x32_bf16 v[236:239], a[148:151], a[204:207], 0\n"
"v_cvt_pk_bf16_f32 v150, v240, v241\n"
"v_cvt_pk_bf16_f32 v151, v242, v243\n"
"v_mfma_f32_16x16x32_bf16 v[72:75], a[80:83], v[200:203], v[72:75]\n"
"s_add_u32 m0, s54, 34960\n"
"v_exp_f32 v198, v198\n"
"v_mfma_f32_16x16x32_bf16 v[72:75], a[84:87], v[204:207], v[72:75]\n"
"buffer_load_dwordx4 v248, s[40:43], s52 offen offset:128 lds\n"
"v_mfma_f32_16x16x32_bf16 v[76:79], a[92:95], v[204:207], v[76:79]\n"
"v_exp_f32 v199, v199\n"
"v_mfma_f32_16x16x32_bf16 v[76:79], a[88:91], v[200:203], v[76:79]\n"
"s_waitcnt lgkmcnt(5)\n"
"buffer_store_dwordx4 v[12:15], v250, s[48:51], s84 offen offset:1024 sc1\n"
"v_perm_b32 v211, v199, v198, s72\n"
"v_mfma_f32_16x16x32_bf16 v[80:83], a[96:99], v[200:203], v[80:83]\n"
"v_exp_f32 v224, v224\n"
"v_mfma_f32_16x16x32_bf16 v[80:83], a[100:103], v[204:207], v[80:83]\n"
"ds_read_b128 a[44:47], v222 offset:192\n"
"ds_read_b128 a[60:63], v222 offset:4544\n"
"v_fma_f32 v48, v48, v252, -v223\n"
"v_fma_f32 v49, v49, v252, -v223\n"
"v_fma_f32 v50, v50, v252, -v223\n"
"v_fma_f32 v51, v51, v252, -v223\n"
"v_fma_f32 v52, v52, v252, -v223\n"
"v_fma_f32 v53, v53, v252, -v223\n"
"v_fma_f32 v54, v54, v252, -v223\n"
"v_fma_f32 v55, v55, v252, -v223\n"
"v_cvt_pk_bf16_f32 v48, v48, v49\n"
"v_cvt_pk_bf16_f32 v49, v50, v51\n"
"v_cvt_pk_bf16_f32 v50, v52, v53\n"
"v_cvt_pk_bf16_f32 v51, v54, v55\n"
"v_mfma_f32_16x16x32_bf16 v[84:87], a[108:111], v[204:207], v[84:87]\n"
"v_exp_f32 v225, v225\n"
"v_mfma_f32_16x16x32_bf16 v[84:87], a[104:107], v[200:203], v[84:87]\n"
"ds_read_b128 a[56:59], v222 offset:4480\n"
"v_perm_b32 v212, v225, v224, s72\n"
"v_mfma_f32_16x16x32_bf16 v[240:243], a[148:151], a[208:211], 0\n"
"v_cvt_pk_bf16_f32 v152, v232, v233\n"
"v_cvt_pk_bf16_f32 v153, v234, v235\n"
"v_mfma_f32_16x16x32_bf16 v[232:235], a[148:151], a[212:215], 0\n"
"ds_read_b128 a[184:187], v217 offset:0\n"
"ds_read_b128 a[188:191], v217 offset:576\n"
"ds_write_b128 v216, v[48:51] offset:0\n"
"v_cvt_pk_bf16_f32 v154, v236, v237\n"
"v_cvt_pk_bf16_f32 v155, v238, v239\n"
"v_mfma_f32_16x16x32_bf16 v[88:91], a[112:115], v[200:203], v[88:91]\n"
"s_add_u32 m0, s54, 34968\n"
"v_exp_f32 v226, v226\n"
"v_mfma_f32_16x16x32_bf16 v[88:91], a[116:119], v[204:207], v[88:91]\n"
"buffer_load_dwordx4 v248, s[40:43], s52 offen offset:1152 lds\n"
"v_mfma_f32_16x16x32_bf16 v[92:95], a[124:127], v[204:207], v[92:95]\n"
"s_add_u32 s52, s52, 16384\n"
"v_exp_f32 v227, v227\n"
"v_mfma_f32_16x16x32_bf16 v[92:95], a[120:123], v[200:203], v[92:95]\n"
"ds_read_b128 a[52:55], v222 offset:4416\n"
"v_perm_b32 v213, v227, v226, s72\n"
"v_mfma_f32_16x16x32_bf16 v[192:195], a[0:3], v[128:131], -4.0\n"
"v_exp_f32 v228, v228\n"
"v_mfma_f32_16x16x32_bf16 v[192:195], a[4:7], v[132:135], v[192:195]\n"
"ds_read_b128 a[48:51], v222 offset:4352\n"
"ds_read2_b64 a[152:155], v246 offset0:0 offset1:2\n"
"v_mfma_f32_16x16x32_bf16 v[192:195], a[8:11], v[136:139], v[192:195]\n"
"v_exp_f32 v229, v229\n"
"v_mfma_f32_16x16x32_bf16 v[192:195], a[12:15], v[140:143], v[192:195]\n"
"ds_read2_b64 a[156:159], v246 offset0:1 offset1:3\n"
"v_perm_b32 v214, v229, v228, s72\n"
"v_fma_f32 v56, v56, v252, -v223\n"
"v_fma_f32 v57, v57, v252, -v223\n"
"v_fma_f32 v58, v58, v252, -v223\n"
"v_fma_f32 v59, v59, v252, -v223\n"
"v_fma_f32 v60, v60, v252, -v223\n"
"v_fma_f32 v61, v61, v252, -v223\n"
"v_fma_f32 v62, v62, v252, -v223\n"
"v_fma_f32 v63, v63, v252, -v223\n"
"v_cvt_pk_bf16_f32 v56, v56, v57\n"
"v_cvt_pk_bf16_f32 v57, v58, v59\n"
"v_cvt_pk_bf16_f32 v58, v60, v61\n"
"v_cvt_pk_bf16_f32 v59, v62, v63\n"
"v_mfma_f32_16x16x32_bf16 v[236:239], a[148:151], a[216:219], 0\n"
"ds_write_b128 v216, v[56:59] offset:64\n"
"v_cvt_pk_bf16_f32 v156, v240, v241\n"
"v_cvt_pk_bf16_f32 v157, v242, v243\n"
"v_mfma_f32_16x16x32_bf16 v[240:243], a[148:151], a[220:223], 0\n"
"v_cvt_pk_bf16_f32 v158, v232, v233\n"
"v_cvt_pk_bf16_f32 v159, v234, v235\n"
"v_mfma_f32_16x16x32_bf16 v[196:199], a[28:31], v[140:143], -4.0\n"
"v_exp_f32 v230, v230\n"
"v_mfma_f32_16x16x32_bf16 v[196:199], a[24:27], v[136:139], v[196:199]\n"
"ds_read2_b64 a[160:163], v246 offset0:32 offset1:34\n"
"ds_read2_b64 a[164:167], v246 offset0:33 offset1:35\n"
"v_mfma_f32_16x16x32_bf16 v[196:199], a[20:23], v[132:135], v[196:199]\n"
"s_waitcnt lgkmcnt(7)\n"
"v_exp_f32 v231, v231\n"
"v_mfma_f32_16x16x32_bf16 v[196:199], a[16:19], v[128:131], v[196:199]\n"
"buffer_store_dwordx4 v[20:23], v250, s[48:51], s84 offen offset:128 sc1\n"
"v_perm_b32 v215, v231, v230, s72\n"
"v_mfma_f32_16x16x32_bf16 v[224:227], a[32:35], v[128:131], -4.0\n"
"v_exp_f32 v192, v192\n"
"v_mfma_f32_16x16x32_bf16 v[224:227], a[36:39], v[132:135], v[224:227]\n"
"ds_read2_b64 a[168:171], v246 offset0:64 offset1:66\n"
"ds_read2_b64 a[172:175], v246 offset0:65 offset1:67\n"
"v_mfma_f32_16x16x32_bf16 v[224:227], a[40:43], v[136:139], v[224:227]\n"
"v_exp_f32 v193, v193\n"
"v_mfma_f32_16x16x32_bf16 v[224:227], a[44:47], v[140:143], v[224:227]\n"
"ds_read2_b64 a[176:179], v246 offset0:96 offset1:98\n"
"ds_read_b128 a[192:195], v217 offset:0\n"
"ds_read_b128 a[196:199], v217 offset:576\n"
"v_perm_b32 v200, v193, v192, s72\n"
"v_mfma_f32_16x16x32_bf16 v[232:235], a[148:151], a[224:227], 0\n"
"v_cvt_pk_bf16_f32 v160, v236, v237\n"
"v_cvt_pk_bf16_f32 v161, v238, v239\n"
"v_mfma_f32_16x16x32_bf16 v[236:239], a[148:151], a[228:231], 0\n"
"v_cvt_pk_bf16_f32 v162, v240, v241\n"
"v_cvt_pk_bf16_f32 v163, v242, v243\n"
"v_mfma_f32_16x16x32_bf16 v[228:231], a[60:63], v[140:143], -4.0\n"
"v_exp_f32 v194, v194\n"
"v_mfma_f32_16x16x32_bf16 v[228:231], a[56:59], v[136:139], v[228:231]\n"
"s_waitcnt lgkmcnt(7)\n"
"ds_read2_b64 a[180:183], v246 offset0:97 offset1:99\n"
"v_accvgpr_read_b32 v254, a136\n"
"v_mfma_f32_16x16x32_bf16 v[228:231], a[52:55], v[132:135], v[228:231]\n"
"v_exp_f32 v195, v195\n"
"v_mfma_f32_16x16x32_bf16 v[228:231], a[48:51], v[128:131], v[228:231]\n"
"buffer_store_dwordx4 v[28:31], v250, s[48:51], s84 offen offset:1152 sc1\n"
"v_perm_b32 v201, v195, v194, s72\n"
"v_smfmac_f32_16x16x64_bf16 a[140:143], a[144:147], v[208:215], v251\n"
"s_add_u32 s84, s84, 4096\n"
"v_accvgpr_read_b32 v252, a137\n"
"v_mfma_f32_16x16x32_bf16 v[96:99], a[64:67], v[208:211], v[96:99]\n"
"v_exp_f32 v196, v196\n"
"v_mfma_f32_16x16x32_bf16 v[96:99], a[68:71], v[212:215], v[96:99]\n"
"ds_read_b64_tr_b16 a[64:65], v245 offset:0\n"
"ds_read_b64_tr_b16 a[66:67], v245 offset:4128\n"
"v_add_f32 v254, v254, v252\n"
"v_rcp_f32 v252, v254\n"
"v_accvgpr_write_b32 a136, 0\n"
"v_accvgpr_write_b32 a137, 0\n"
"v_max_f32 v254, v254, v252\n"
"v_cmp_nlt_f32 s[62:63], v254, s58\n"
"v_mul_f32 v223, v218, v252\n"
"v_mul_f32 v254, 0x3b000000, v223\n"
"v_fma_f32 v64, v64, v252, -v223\n"
"v_fma_f32 v65, v65, v252, -v223\n"
"v_fma_f32 v66, v66, v252, -v223\n"
"v_fma_f32 v67, v67, v252, -v223\n"
"v_fma_f32 v68, v68, v252, -v223\n"
"v_fma_f32 v69, v69, v252, -v223\n"
"v_fma_f32 v70, v70, v252, -v223\n"
"v_fma_f32 v71, v71, v252, -v223\n"
"v_max3_f32 v253, abs(v64), abs(v65), abs(v66)\n"
"v_cmp_lt_f32 vcc, v253, v254\n"
"v_cvt_pk_bf16_f32 v64, v64, v65\n"
"v_cvt_pk_bf16_f32 v65, v66, v67\n"
"v_cvt_pk_bf16_f32 v66, v68, v69\n"
"v_cvt_pk_bf16_f32 v67, v70, v71\n"
"v_mfma_f32_16x16x32_bf16 v[100:103], a[76:79], v[212:215], v[100:103]\n"
"s_and_b32 s73, vcc_lo, vcc_hi\n"
"v_exp_f32 v197, v197\n"
"v_mfma_f32_16x16x32_bf16 v[100:103], a[72:75], v[208:211], v[100:103]\n"
"s_lshr_b32 s74, s73, 16\n"
"ds_read_b64_tr_b16 a[68:69], v255 offset:0\n"
"ds_write_b128 v216, v[64:67] offset:0\n"
"v_perm_b32 v202, v197, v196, s72\n"
"v_mfma_f32_16x16x32_bf16 v[240:243], a[148:151], a[232:235], 0\n"
"s_and_b32 s73, s73, s74\n"
"v_cvt_pk_bf16_f32 v164, v232, v233\n"
"v_cvt_pk_bf16_f32 v165, v234, v235\n"
"v_mfma_f32_16x16x32_bf16 v[232:235], a[148:151], a[236:239], 0\n"
"s_or_b32 s73, s73, s62\n"
"v_cvt_pk_bf16_f32 v166, v236, v237\n"
"v_cvt_pk_bf16_f32 v167, v238, v239\n"
"v_mfma_f32_16x16x32_bf16 v[104:107], a[80:83], v[208:211], v[104:107]\n"
"s_or_b32 s73, s73, s63\n"
"v_exp_f32 v198, v198\n"
"v_mfma_f32_16x16x32_bf16 v[104:107], a[84:87], v[212:215], v[104:107]\n"
"s_addc_u32 s57, s57, s57\n"
"ds_read_b64_tr_b16 a[70:71], v255 offset:4128\n"
"ds_read_b64_tr_b16 a[76:77], v255 offset:8\n"
"v_mfma_f32_16x16x32_bf16 v[108:111], a[92:95], v[212:215], v[108:111]\n"
"v_exp_f32 v199, v199\n"
"v_mfma_f32_16x16x32_bf16 v[108:111], a[88:91], v[208:211], v[108:111]\n"
"ds_read_b64_tr_b16 a[78:79], v255 offset:4136\n"
"v_perm_b32 v203, v199, v198, s72\n"
"v_fma_f32 v72, v72, v252, -v223\n"
"v_fma_f32 v73, v73, v252, -v223\n"
"v_fma_f32 v74, v74, v252, -v223\n"
"v_fma_f32 v75, v75, v252, -v223\n"
"v_fma_f32 v76, v76, v252, -v223\n"
"v_fma_f32 v77, v77, v252, -v223\n"
"v_fma_f32 v78, v78, v252, -v223\n"
"v_fma_f32 v79, v79, v252, -v223\n"
"v_cvt_pk_bf16_f32 v72, v72, v73\n"
"v_cvt_pk_bf16_f32 v73, v74, v75\n"
"v_cvt_pk_bf16_f32 v74, v76, v77\n"
"v_cvt_pk_bf16_f32 v75, v78, v79\n"
"v_mfma_f32_16x16x32_bf16 v[112:115], a[96:99], v[208:211], v[112:115]\n"
"v_exp_f32 v224, v224\n"
"v_mfma_f32_16x16x32_bf16 v[112:115], a[100:103], v[212:215], v[112:115]\n"
"ds_read_b64_tr_b16 a[72:73], v245 offset:8\n"
"ds_read_b64_tr_b16 a[74:75], v245 offset:4136\n"
"ds_write_b128 v216, v[72:75] offset:64\n"
"v_mfma_f32_16x16x32_bf16 v[116:119], a[108:111], v[212:215], v[116:119]\n"
"v_exp_f32 v225, v225\n"
"v_mfma_f32_16x16x32_bf16 v[116:119], a[104:107], v[208:211], v[116:119]\n"
"ds_read_b64_tr_b16 a[80:81], v245 offset:256\n"
"v_perm_b32 v204, v225, v224, s72\n"
"v_mfma_f32_16x16x32_bf16 v[236:239], a[148:151], a[240:243], 0\n"
"v_cvt_pk_bf16_f32 v168, v240, v241\n"
"v_cvt_pk_bf16_f32 v169, v242, v243\n"
"v_mfma_f32_16x16x32_bf16 v[240:243], a[148:151], a[244:247], 0\n"
"ds_read_b128 a[216:219], v217 offset:0\n"
"ds_read_b128 a[220:223], v217 offset:576\n"
"v_cvt_pk_bf16_f32 v170, v232, v233\n"
"v_cvt_pk_bf16_f32 v171, v234, v235\n"
"v_mfma_f32_16x16x32_bf16 v[120:123], a[112:115], v[208:211], v[120:123]\n"
"v_exp_f32 v226, v226\n"
"v_mfma_f32_16x16x32_bf16 v[120:123], a[116:119], v[212:215], v[120:123]\n"
"ds_read_b64_tr_b16 a[82:83], v245 offset:4384\n"
"ds_read_b64_tr_b16 a[84:85], v255 offset:256\n"
"v_mfma_f32_16x16x32_bf16 v[124:127], a[124:127], v[212:215], v[124:127]\n"
"v_exp_f32 v227, v227\n"
"v_mfma_f32_16x16x32_bf16 v[124:127], a[120:123], v[208:211], v[124:127]\n"
"ds_read_b64_tr_b16 a[86:87], v255 offset:4384\n"
"v_perm_b32 v205, v227, v226, s72\n"
"v_mfma_f32_16x16x32_bf16 v[192:195], a[0:3], v[144:147], -4.0\n"
"v_exp_f32 v228, v228\n"
"v_mfma_f32_16x16x32_bf16 v[192:195], a[4:7], v[148:151], v[192:195]\n"
"ds_read_b64_tr_b16 a[92:93], v255 offset:264\n"
"ds_read_b64_tr_b16 a[94:95], v255 offset:4392\n"
"v_mfma_f32_16x16x32_bf16 v[192:195], a[8:11], v[152:155], v[192:195]\n"
"v_exp_f32 v229, v229\n"
"v_mfma_f32_16x16x32_bf16 v[192:195], a[12:15], v[156:159], v[192:195]\n"
"ds_read_b64_tr_b16 a[88:89], v245 offset:264\n"
"v_perm_b32 v206, v229, v228, s72\n"
"v_fma_f32 v80, v80, v252, -v223\n"
"v_fma_f32 v81, v81, v252, -v223\n"
"v_fma_f32 v82, v82, v252, -v223\n"
"v_fma_f32 v83, v83, v252, -v223\n"
"v_fma_f32 v84, v84, v252, -v223\n"
"v_fma_f32 v85, v85, v252, -v223\n"
"v_fma_f32 v86, v86, v252, -v223\n"
"v_fma_f32 v87, v87, v252, -v223\n"
"v_cvt_pk_bf16_f32 v80, v80, v81\n"
"v_cvt_pk_bf16_f32 v81, v82, v83\n"
"v_cvt_pk_bf16_f32 v82, v84, v85\n"
"v_cvt_pk_bf16_f32 v83, v86, v87\n"
"v_mfma_f32_16x16x32_bf16 v[232:235], a[148:151], a[152:155], 0\n"
"ds_write_b128 v216, v[80:83] offset:0\n"
"v_cvt_pk_bf16_f32 v172, v236, v237\n"
"v_cvt_pk_bf16_f32 v173, v238, v239\n"
"v_mfma_f32_16x16x32_bf16 v[236:239], a[148:151], a[156:159], 0\n"
"v_cvt_pk_bf16_f32 v174, v240, v241\n"
"v_cvt_pk_bf16_f32 v175, v242, v243\n"
"v_mfma_f32_16x16x32_bf16 v[196:199], a[28:31], v[156:159], -4.0\n"
"v_exp_f32 v230, v230\n"
"v_mfma_f32_16x16x32_bf16 v[196:199], a[24:27], v[152:155], v[196:199]\n"
"ds_read_b64_tr_b16 a[90:91], v245 offset:4392\n"
"ds_read_b64_tr_b16 a[96:97], v245 offset:2064\n"
"v_mfma_f32_16x16x32_bf16 v[196:199], a[20:23], v[148:151], v[196:199]\n"
"Lhop1_3_%=:\n s_cbranch_execz Lhop1_7_%=\n"
"v_exp_f32 v231, v231\n"
"v_mfma_f32_16x16x32_bf16 v[196:199], a[16:19], v[144:147], v[196:199]\n"
"ds_read_b64_tr_b16 a[98:99], v245 offset:6192\n"
"v_perm_b32 v207, v231, v230, s72\n"
"v_mfma_f32_16x16x32_bf16 v[224:227], a[32:35], v[144:147], -4.0\n"
"v_exp_f32 v192, v192\n"
"v_mfma_f32_16x16x32_bf16 v[224:227], a[36:39], v[148:151], v[224:227]\n"
"ds_read_b64_tr_b16 a[100:101], v255 offset:2064\n"
"ds_read_b64_tr_b16 a[102:103], v255 offset:6192\n"
"v_mfma_f32_16x16x32_bf16 v[224:227], a[40:43], v[152:155], v[224:227]\n"
"v_exp_f32 v193, v193\n"
"v_mfma_f32_16x16x32_bf16 v[224:227], a[44:47], v[156:159], v[224:227]\n"
"s_waitcnt lgkmcnt(7)\n"
"ds_read_b64_tr_b16 a[108:109], v255 offset:2072\n"
"v_perm_b32 v208, v193, v192, s72\n"
"v_fma_f32 v88, v88, v252, -v223\n"
"v_fma_f32 v89, v89, v252, -v223\n"
"v_fma_f32 v90, v90, v252, -v223\n"
"v_fma_f32 v91, v91, v252, -v223\n"
"v_fma_f32 v92, v92, v252, -v223\n"
"v_fma_f32 v93, v93, v252, -v223\n"
"v_fma_f32 v94, v94, v252, -v223\n"
"v_fma_f32 v95, v95, v252, -v223\n"
"v_cvt_pk_bf16_f32 v88, v88, v89\n"
"v_cvt_pk_bf16_f32 v89, v90, v91\n"
"v_cvt_pk_bf16_f32 v90, v92, v93\n"
"v_cvt_pk_bf16_f32 v91, v94, v95\n"
"v_mfma_f32_16x16x32_bf16 v[240:243], a[148:151], a[160:163], 0\n"
"ds_write_b128 v216, v[88:91] offset:64\n"
"v_cvt_pk_bf16_f32 v176, v232, v233\n"
"v_cvt_pk_bf16_f32 v177, v234, v235\n"
"v_mfma_f32_16x16x32_bf16 v[232:235], a[148:151], a[164:167], 0\n"
"v_cvt_pk_bf16_f32 v178, v236, v237\n"
"v_cvt_pk_bf16_f32 v179, v238, v239\n"
"v_mfma_f32_16x16x32_bf16 v[228:231], a[60:63], v[156:159], -4.0\n"
"v_exp_f32 v194, v194\n"
"v_mfma_f32_16x16x32_bf16 v[228:231], a[56:59], v[152:155], v[228:231]\n"
"ds_read_b64_tr_b16 a[110:111], v255 offset:6200\n"
"ds_read_b64_tr_b16 a[104:105], v245 offset:2072\n"
"v_mfma_f32_16x16x32_bf16 v[228:231], a[52:55], v[148:151], v[228:231]\n"
"v_exp_f32 v195, v195\n"
"v_mfma_f32_16x16x32_bf16 v[228:231], a[48:51], v[144:147], v[228:231]\n"
"ds_read_b64_tr_b16 a[106:107], v245 offset:6200\n"
"v_perm_b32 v209, v195, v194, s72\n"
"v_smfmac_f32_16x16x64_bf16 a[128:131], a[144:147], v[200:207], v251\n"
"ds_read_b64_tr_b16 a[112:113], v245 offset:2320\n"
"v_mfma_f32_16x16x32_bf16 v[0:3], a[64:67], v[200:203], v[218:221]\n"
"v_exp_f32 v196, v196\n"
"v_mfma_f32_16x16x32_bf16 v[0:3], a[68:71], v[204:207], v[0:3]\n"
"ds_read_b64_tr_b16 a[114:115], v245 offset:6448\n"
"ds_read_b64_tr_b16 a[116:117], v255 offset:2320\n"
"ds_read_b128 a[224:227], v217 offset:0\n"
"ds_read_b128 a[228:231], v217 offset:576\n"
"v_mfma_f32_16x16x32_bf16 v[4:7], a[76:79], v[204:207], v[218:221]\n"
"v_exp_f32 v197, v197\n"
"v_mfma_f32_16x16x32_bf16 v[4:7], a[72:75], v[200:203], v[4:7]\n"
"ds_read_b64_tr_b16 a[118:119], v255 offset:6448\n"
"v_perm_b32 v210, v197, v196, s72\n"
"v_mfma_f32_16x16x32_bf16 v[236:239], a[148:151], a[168:171], 0\n"
"v_cvt_pk_bf16_f32 v180, v240, v241\n"
"v_cvt_pk_bf16_f32 v181, v242, v243\n"
"v_mfma_f32_16x16x32_bf16 v[240:243], a[148:151], a[172:175], 0\n"
"v_cvt_pk_bf16_f32 v182, v232, v233\n"
"v_cvt_pk_bf16_f32 v183, v234, v235\n"
"v_mfma_f32_16x16x32_bf16 v[8:11], a[80:83], v[200:203], v[218:221]\n"
"v_exp_f32 v198, v198\n"
"v_mfma_f32_16x16x32_bf16 v[8:11], a[84:87], v[204:207], v[8:11]\n"
"s_waitcnt lgkmcnt(7)\n"
"ds_read_b64_tr_b16 a[124:125], v255 offset:2328\n"
"ds_read_b64_tr_b16 a[126:127], v255 offset:6456\n"
"v_accvgpr_read_b32 v254, a140\n"
"v_accvgpr_read_b32 v252, a141\n"
"v_add_f32 v254, v254, v252\n"
"v_rcp_f32 v252, v254\n"
"v_accvgpr_write_b32 a140, 0\n"
"v_accvgpr_write_b32 a141, 0\n"
"v_max_f32 v254, v254, v252\n"
"v_cmp_nlt_f32 s[62:63], v254, s58\n"
"v_mul_f32 v223, v218, v252\n"
"v_mul_f32 v254, 0x3b000000, v223\n"
"v_fma_f32 v96, v96, v252, -v223\n"
"v_fma_f32 v97, v97, v252, -v223\n"
"v_fma_f32 v98, v98, v252, -v223\n"
"v_fma_f32 v99, v99, v252, -v223\n"
"v_fma_f32 v100, v100, v252, -v223\n"
"v_fma_f32 v101, v101, v252, -v223\n"
"v_fma_f32 v102, v102, v252, -v223\n"
"v_fma_f32 v103, v103, v252, -v223\n"
"v_max3_f32 v253, abs(v96), abs(v97), abs(v98)\n"
"v_cmp_lt_f32 vcc, v253, v254\n"
"v_cvt_pk_bf16_f32 v96, v96, v97\n"
"v_cvt_pk_bf16_f32 v97, v98, v99\n"
"v_cvt_pk_bf16_f32 v98, v100, v101\n"
"v_cvt_pk_bf16_f32 v99, v102, v103\n"
"v_mfma_f32_16x16x32_bf16 v[12:15], a[92:95], v[204:207], v[218:221]\n"
"s_and_b32 s73, vcc_lo, vcc_hi\n"
"v_exp_f32 v199, v199\n"
"v_mfma_f32_16x16x32_bf16 v[12:15], a[88:91], v[200:203], v[12:15]\n"
"s_lshr_b32 s74, s73, 16\n"
"ds_read_b64_tr_b16 a[120:121], v245 offset:2328\n"
"ds_write_b128 v216, v[96:99] offset:0\n"
"v_perm_b32 v211, v199, v198, s72\n"
"v_mfma_f32_16x16x32_bf16 v[16:19], a[96:99], v[200:203], v[218:221]\n"
"s_and_b32 s73, s73, s74\n"
"v_exp_f32 v224, v224\n"
"v_mfma_f32_16x16x32_bf16 v[16:19], a[100:103], v[204:207], v[16:19]\n"
"s_or_b32 s73, s73, s62\n"
"ds_read_b64_tr_b16 a[122:123], v245 offset:6456\n"
"v_fma_f32 v104, v104, v252, -v223\n"
"v_mfma_f32_16x16x32_bf16 v[20:23], a[108:111], v[204:207], v[218:221]\n"
"s_or_b32 s73, s73, s63\n"
"v_exp_f32 v225, v225\n"
"s_waitcnt lgkmcnt(11)\n"
"v_mfma_f32_16x16x32_bf16 v[20:23], a[104:107], v[200:203], v[20:23]\n"
"s_addc_u32 s57, s57, s57\n"
"v_perm_b32 v212, v225, v224, s72\n"
"v_fma_f32 v105, v105, v252, -v223\n"
"v_mfma_f32_16x16x32_bf16 v[232:235], a[148:151], a[176:179], 0\n"
"v_cvt_pk_bf16_f32 v184, v236, v237\n"
"v_cvt_pk_bf16_f32 v185, v238, v239\n"
"v_mfma_f32_16x16x32_bf16 v[236:239], a[148:151], a[180:183], 0\n"
"s_waitcnt lgkmcnt(5)\n"
"v_cvt_pk_bf16_f32 v186, v240, v241\n"
"v_cvt_pk_bf16_f32 v187, v242, v243\n"
"v_mfma_f32_16x16x32_bf16 v[24:27], a[112:115], v[200:203], v[218:221]\n"
"s_add_u32 m0, s53, 52224\n"
"v_exp_f32 v226, v226\n"
"v_mfma_f32_16x16x32_bf16 v[24:27], a[116:119], v[204:207], v[24:27]\n"
"s_waitcnt lgkmcnt(3)\n"
"buffer_load_dwordx4 v247, s[36:39], s52 offen offset:0 lds\n"
"v_mfma_f32_16x16x32_bf16 v[28:31], a[124:127], v[204:207], v[218:221]\n"
"s_waitcnt lgkmcnt(0)\n"
"v_exp_f32 v227, v227\n"
"v_mfma_f32_16x16x32_bf16 v[28:31], a[120:123], v[200:203], v[28:31]\n"
"v_perm_b32 v213, v227, v226, s72\n"
"v_cvt_pk_bf16_f32 v188, v232, v233\n"
"v_cvt_pk_bf16_f32 v189, v234, v235\n"
"v_cvt_pk_bf16_f32 v190, v236, v237\n"
"v_cvt_pk_bf16_f32 v191, v238, v239\n"
"v_fma_f32 v106, v106, v252, -v223\n"
"v_fma_f32 v107, v107, v252, -v223\n"
"v_fma_f32 v108, v108, v252, -v223\n"
"v_fma_f32 v109, v109, v252, -v223\n"
"v_fma_f32 v110, v110, v252, -v223\n"
"v_fma_f32 v111, v111, v252, -v223\n"
"v_cvt_pk_bf16_f32 v104, v104, v105\n"
"v_cvt_pk_bf16_f32 v105, v106, v107\n"
"v_cvt_pk_bf16_f32 v106, v108, v109\n"
"v_cvt_pk_bf16_f32 v107, v110, v111\n"
"v_mfma_f32_16x16x32_bf16 v[192:195], a[0:3], v[160:163], -4.0\n"
"v_exp_f32 v228, v228\n"
"v_mfma_f32_16x16x32_bf16 v[192:195], a[4:7], v[164:167], v[192:195]\n"
"v_fma_f32 v112, v112, v252, -v223\n"
"v_fma_f32 v113, v113, v252, -v223\n"
"v_mfma_f32_16x16x32_bf16 v[192:195], a[8:11], v[168:171], v[192:195]\n"
"v_exp_f32 v229, v229\n"
"v_mfma_f32_16x16x32_bf16 v[192:195], a[12:15], v[172:175], v[192:195]\n"
"ds_write_b128 v216, v[104:107] offset:64\n"
"v_perm_b32 v214, v229, v228, s72\n"
"v_fma_f32 v114, v114, v252, -v223\n"
"v_mfma_f32_16x16x32_bf16 v[196:199], a[28:31], v[172:175], -4.0\n"
"s_add_u32 m0, s53, 52288\n"
"v_exp_f32 v230, v230\n"
"v_mfma_f32_16x16x32_bf16 v[196:199], a[24:27], v[168:171], v[196:199]\n"
"buffer_load_dwordx4 v247, s[36:39], s52 offen offset:1024 lds\n"
"v_mfma_f32_16x16x32_bf16 v[196:199], a[20:23], v[164:167], v[196:199]\n"
"v_exp_f32 v231, v231\n"
"v_mfma_f32_16x16x32_bf16 v[196:199], a[16:19], v[160:163], v[196:199]\n"
"ds_read_b128 a[232:235], v217 offset:0\n"
"ds_read_b128 a[236:239], v217 offset:576\n"
"v_perm_b32 v215, v231, v230, s72\n"
"v_fma_f32 v115, v115, v252, -v223\n"
"v_fma_f32 v116, v116, v252, -v223\n"
"v_fma_f32 v117, v117, v252, -v223\n"
"v_fma_f32 v118, v118, v252, -v223\n"
"v_fma_f32 v119, v119, v252, -v223\n"
"v_cvt_pk_bf16_f32 v112, v112, v113\n"
"v_cvt_pk_bf16_f32 v113, v114, v115\n"
"v_cvt_pk_bf16_f32 v114, v116, v117\n"
"v_cvt_pk_bf16_f32 v115, v118, v119\n"
"v_mfma_f32_16x16x32_bf16 v[224:227], a[32:35], v[160:163], -4.0\n"
"v_exp_f32 v192, v192\n"
"v_mfma_f32_16x16x32_bf16 v[224:227], a[36:39], v[164:167], v[224:227]\n"
"v_fma_f32 v120, v120, v252, -v223\n"
"v_fma_f32 v121, v121, v252, -v223\n"
"v_mfma_f32_16x16x32_bf16 v[224:227], a[40:43], v[168:171], v[224:227]\n"
"v_exp_f32 v193, v193\n"
"v_mfma_f32_16x16x32_bf16 v[224:227], a[44:47], v[172:175], v[224:227]\n"
"ds_write_b128 v216, v[112:115] offset:0\n"
"v_perm_b32 v200, v193, v192, s72\n"
"v_fma_f32 v122, v122, v252, -v223\n"
"v_fma_f32 v123, v123, v252, -v223\n"
"v_fma_f32 v124, v124, v252, -v223\n"
"v_fma_f32 v125, v125, v252, -v223\n"
"v_fma_f32 v126, v126, v252, -v223\n"
"v_fma_f32 v127, v127, v252, -v223\n"
"v_cvt_pk_bf16_f32 v120, v120, v121\n"
"v_cvt_pk_bf16_f32 v121, v122, v123\n"
"v_cvt_pk_bf16_f32 v122, v124, v125\n"
"v_cvt_pk_bf16_f32 v123, v126, v127\n"
"v_mfma_f32_16x16x32_bf16 v[228:231], a[60:63], v[172:175], -4.0\n"
"s_add_u32 m0, s53, 52352\n"
"v_exp_f32 v194, v194\n"
"v_mfma_f32_16x16x32_bf16 v[228:231], a[56:59], v[168:171], v[228:231]\n"
"buffer_load_dwordx4 v247, s[36:39], s52 offen offset:2048 lds\n"
"v_mfma_f32_16x16x32_bf16 v[228:231], a[52:55], v[164:167], v[228:231]\n"
"v_exp_f32 v195, v195\n"
"v_mfma_f32_16x16x32_bf16 v[228:231], a[48:51], v[160:163], v[228:231]\n"
"s_waitcnt vmcnt(11) lgkmcnt(4)\n"
"ds_write_b128 v216, v[120:123] offset:64\n"
"v_perm_b32 v201, v195, v194, s72\n"
"v_smfmac_f32_16x16x64_bf16 a[132:135], a[144:147], v[208:215], v251\n"
"ds_read_b128 a[240:243], v217 offset:0\n"
"v_mfma_f32_16x16x32_bf16 v[32:35], a[64:67], v[208:211], v[218:221]\n"
"v_exp_f32 v196, v196\n"
"v_mfma_f32_16x16x32_bf16 v[32:35], a[68:71], v[212:215], v[32:35]\n"
"s_barrier\n"
"ds_read_b128 a[244:247], v217 offset:576\n"
"v_mfma_f32_16x16x32_bf16 v[36:39], a[76:79], v[212:215], v[218:221]\n"
"v_exp_f32 v197, v197\n"
"v_mfma_f32_16x16x32_bf16 v[36:39], a[72:75], v[208:211], v[36:39]\n"
"v_perm_b32 v202, v197, v196, s72\n"
"v_mfma_f32_16x16x32_bf16 v[40:43], a[80:83], v[208:211], v[218:221]\n"
"s_add_u32 m0, s53, 52416\n"
"v_exp_f32 v198, v198\n"
"v_mfma_f32_16x16x32_bf16 v[40:43], a[84:87], v[212:215], v[40:43]\n"
"buffer_load_dwordx4 v247, s[36:39], s52 offen offset:3072 lds\n"
"v_mfma_f32_16x16x32_bf16 v[44:47], a[92:95], v[212:215], v[218:221]\n"
"v_exp_f32 v199, v199\n"
"v_mfma_f32_16x16x32_bf16 v[44:47], a[88:91], v[208:211], v[44:47]\n"
"v_perm_b32 v203, v199, v198, s72\n"
"v_mfma_f32_16x16x32_bf16 v[48:51], a[96:99], v[208:211], v[218:221]\n"
"v_exp_f32 v224, v224\n"
"v_mfma_f32_16x16x32_bf16 v[48:51], a[100:103], v[212:215], v[48:51]\n"
"v_mfma_f32_16x16x32_bf16 v[52:55], a[108:111], v[212:215], v[218:221]\n"
"v_exp_f32 v225, v225\n"
"v_mfma_f32_16x16x32_bf16 v[52:55], a[104:107], v[208:211], v[52:55]\n"
"v_perm_b32 v204, v225, v224, s72\n"
"v_mfma_f32_16x16x32_bf16 v[56:59], a[112:115], v[208:211], v[218:221]\n"
"s_add_u32 m0, s54, 49536\n"
"v_exp_f32 v226, v226\n"
"v_mfma_f32_16x16x32_bf16 v[56:59], a[116:119], v[212:215], v[56:59]\n"
"buffer_load_dwordx4 v248, s[40:43], s52 offen offset:0 lds\n"
"v_mfma_f32_16x16x32_bf16 v[60:63], a[124:127], v[212:215], v[218:221]\n"
"v_exp_f32 v227, v227\n"
"v_mfma_f32_16x16x32_bf16 v[60:63], a[120:123], v[208:211], v[60:63]\n"
"s_cmp_eq_u32 s69, s56\n"
"v_perm_b32 v205, v227, v226, s72\n"
"s_waitcnt lgkmcnt(0)\n"
"s_cbranch_scc1 LAS%=\n"
"s_branch LAN%=\n"
"LTN%=:\n"
"v_mfma_f32_16x16x32_bf16 v[192:195], a[0:3], v[144:147], -4.0\n"
"Lhop2_2_%=:\n s_cbranch_execz Lback2_%=\n"
"v_exp_f32 v228, v228\n"
"v_mfma_f32_16x16x32_bf16 v[192:195], a[4:7], v[148:151], v[192:195]\n"
"ds_read_b64_tr_b16 a[92:93], v255 offset:49800\n"
"ds_read_b64_tr_b16 a[94:95], v255 offset:53928\n"
"v_mfma_f32_16x16x32_bf16 v[192:195], a[8:11], v[152:155], v[192:195]\n"
"v_exp_f32 v229, v229\n"
"v_mfma_f32_16x16x32_bf16 v[192:195], a[12:15], v[156:159], v[192:195]\n"
"ds_read_b64_tr_b16 a[88:89], v245 offset:49800\n"
"v_perm_b32 v206, v229, v228, s72\n"
"v_mfma_f32_16x16x32_bf16 v[196:199], a[28:31], v[156:159], -4.0\n"
"v_exp_f32 v230, v230\n"
"v_mfma_f32_16x16x32_bf16 v[196:199], a[24:27], v[152:155], v[196:199]\n"
"ds_read_b64_tr_b16 a[90:91], v245 offset:53928\n"
"ds_read_b64_tr_b16 a[96:97], v245 offset:51600\n"
"v_mfma_f32_16x16x32_bf16 v[196:199], a[20:23], v[148:151], v[196:199]\n"
"v_exp_f32 v231, v231\n"
"v_mfma_f32_16x16x32_bf16 v[196:199], a[16:19], v[144:147], v[196:199]\n"
"ds_read_b64_tr_b16 a[98:99], v245 offset:55728\n"
"v_perm_b32 v207, v231, v230, s72\n"
"v_mfma_f32_16x16x32_bf16 v[224:227], a[32:35], v[144:147], -4.0\n"
"v_exp_f32 v192, v192\n"
"v_mfma_f32_16x16x32_bf16 v[224:227], a[36:39], v[148:151], v[224:227]\n"
"ds_read_b64_tr_b16 a[100:101], v255 offset:51600\n"
"ds_read_b64_tr_b16 a[102:103], v255 offset:55728\n"
"v_mfma_f32_16x16x32_bf16 v[224:227], a[40:43], v[152:155], v[224:227]\n"
"v_exp_f32 v193, v193\n"
"v_mfma_f32_16x16x32_bf16 v[224:227], a[44:47], v[156:159], v[224:227]\n"
"ds_read_b64_tr_b16 a[108:109], v255 offset:51608\n"
"v_perm_b32 v208, v193, v192, s72\n"
"v_mfma_f32_16x16x32_bf16 v[228:231], a[60:63], v[156:159], -4.0\n"
"v_exp_f32 v194, v194\n"
"v_mfma_f32_16x16x32_bf16 v[228:231], a[56:59], v[152:155], v[228:231]\n"
"ds_read_b64_tr_b16 a[110:111], v255 offset:55736\n"
"ds_read_b64_tr_b16 a[104:105], v245 offset:51608\n"
"v_mfma_f32_16x16x32_bf16 v[228:231], a[52:55], v[148:151], v[228:231]\n"
"v_exp_f32 v195, v195\n"
"v_mfma_f32_16x16x32_bf16 v[228:231], a[48:51], v[144:147], v[228:231]\n"
"ds_read_b64_tr_b16 a[106:107], v245 offset:55736\n"
"v_perm_b32 v209, v195, v194, s72\n"
"v_smfmac_f32_16x16x64_bf16 a[128:131], a[144:147], v[200:207], v251\n"
"s_waitcnt lgkmcnt(7)\n"
"ds_read_b64_tr_b16 a[112:113], v245 offset:51856\n"
"v_mfma_f32_16x16x32_bf16 v[0:3], a[64:67], v[200:203], v[0:3]\n"
"v_exp_f32 v196, v196\n"
"v_mfma_f32_16x16x32_bf16 v[0:3], a[68:71], v[204:207], v[0:3]\n"
"ds_read_b64_tr_b16 a[114:115], v245 offset:55984\n"
"ds_read_b64_tr_b16 a[116:117], v255 offset:51856\n"
"v_mfma_f32_16x16x32_bf16 v[4:7], a[76:79], v[204:207], v[4:7]\n"
"v_exp_f32 v197, v197\n"
"v_mfma_f32_16x16x32_bf16 v[4:7], a[72:75], v[200:203], v[4:7]\n"
"ds_read_b64_tr_b16 a[118:119], v255 offset:55984\n"
"v_perm_b32 v210, v197, v196, s72\n"
"v_mfma_f32_16x16x32_bf16 v[8:11], a[80:83], v[200:203], v[8:11]\n"
"v_exp_f32 v198, v198\n"
"v_mfma_f32_16x16x32_bf16 v[8:11], a[84:87], v[204:207], v[8:11]\n"
"ds_read_b64_tr_b16 a[124:125], v255 offset:51864\n"
"ds_read_b64_tr_b16 a[126:127], v255 offset:55992\n"
"v_mfma_f32_16x16x32_bf16 v[12:15], a[92:95], v[204:207], v[12:15]\n"
"v_exp_f32 v199, v199\n"
"v_mfma_f32_16x16x32_bf16 v[12:15], a[88:91], v[200:203], v[12:15]\n"
"s_waitcnt lgkmcnt(7)\n"
"ds_read_b64_tr_b16 a[120:121], v245 offset:51864\n"
"v_perm_b32 v211, v199, v198, s72\n"
"v_mfma_f32_16x16x32_bf16 v[16:19], a[96:99], v[200:203], v[16:19]\n"
"v_exp_f32 v224, v224\n"
"v_mfma_f32_16x16x32_bf16 v[16:19], a[100:103], v[204:207], v[16:19]\n"
"s_waitcnt lgkmcnt(5)\n"
"ds_read_b64_tr_b16 a[122:123], v245 offset:55992\n"
"v_accvgpr_read_b32 v254, a128\n"
"v_accvgpr_read_b32 v252, a129\n"
"v_add_f32 v254, v254, v252\n"
"v_rcp_f32 v252, v254\n"
"s_nop 0\n"
"v_max_f32 v254, v254, v252\n"
"v_cmp_nlt_f32 s[62:63], v254, s58\n"
"v_mul_f32 v223, v218, v252\n"
"v_mul_f32 v254, 0x3b000000, v223\n"
"v_fma_f32 v0, v0, v252, -v223\n"
"v_fma_f32 v1, v1, v252, -v223\n"
"v_fma_f32 v2, v2, v252, -v223\n"
"v_fma_f32 v3, v3, v252, -v223\n"
"v_fma_f32 v4, v4, v252, -v223\n"
"v_fma_f32 v5, v5, v252, -v223\n"
"v_fma_f32 v6, v6, v252, -v223\n"
"v_fma_f32 v7, v7, v252, -v223\n"
"v_max3_f32 v253, abs(v0), abs(v1), abs(v2)\n"
"v_cmp_lt_f32 vcc, v253, v254\n"
"v_cvt_pk_bf16_f32 v0, v0, v1\n"
"v_cvt_pk_bf16_f32 v1, v2, v3\n"
"v_cvt_pk_bf16_f32 v2, v4, v5\n"
"v_cvt_pk_bf16_f32 v3, v6, v7\n"
"v_mfma_f32_16x16x32_bf16 v[20:23], a[108:111], v[204:207], v[20:23]\n"
"s_and_b32 s73, vcc_lo, vcc_hi\n"
"v_exp_f32 v225, v225\n"
"v_mfma_f32_16x16x32_bf16 v[20:23], a[104:107], v[200:203], v[20:23]\n"
"s_lshr_b32 s74, s73, 16\n"
"ds_write_b128 v216, v[0:3] offset:0\n"
"v_perm_b32 v212, v225, v224, s72\n"
"v_fma_f32 v8, v8, v252, -v223\n"
"v_mfma_f32_16x16x32_bf16 v[24:27], a[112:115], v[200:203], v[24:27]\n"
"s_and_b32 s73, s73, s74\n"
"v_exp_f32 v226, v226\n"
"s_waitcnt lgkmcnt(5)\n"
"v_mfma_f32_16x16x32_bf16 v[24:27], a[116:119], v[204:207], v[24:27]\n"
"s_or_b32 s73, s73, s62\n"
"v_fma_f32 v9, v9, v252, -v223\n"
"v_fma_f32 v10, v10, v252, -v223\n"
"s_waitcnt lgkmcnt(3)\n"
"v_mfma_f32_16x16x32_bf16 v[28:31], a[124:127], v[204:207], v[28:31]\n"
"s_or_b32 s73, s73, s63\n"
"v_exp_f32 v227, v227\n"
"s_waitcnt lgkmcnt(1)\n"
"v_mfma_f32_16x16x32_bf16 v[28:31], a[120:123], v[200:203], v[28:31]\n"
"s_addc_u32 s57, s57, s57\n"
"v_perm_b32 v213, v227, v226, s72\n"
"v_fma_f32 v11, v11, v252, -v223\n"
"v_mfma_f32_16x16x32_bf16 v[192:195], a[0:3], v[160:163], -4.0\n"
"v_exp_f32 v228, v228\n"
"v_mfma_f32_16x16x32_bf16 v[192:195], a[4:7], v[164:167], v[192:195]\n"
"v_fma_f32 v12, v12, v252, -v223\n"
"v_fma_f32 v13, v13, v252, -v223\n"
"v_mfma_f32_16x16x32_bf16 v[192:195], a[8:11], v[168:171], v[192:195]\n"
"v_exp_f32 v229, v229\n"
"v_mfma_f32_16x16x32_bf16 v[192:195], a[12:15], v[172:175], v[192:195]\n"
"v_perm_b32 v214, v229, v228, s72\n"
"v_fma_f32 v14, v14, v252, -v223\n"
"v_fma_f32 v15, v15, v252, -v223\n"
"v_cvt_pk_bf16_f32 v8, v8, v9\n"
"v_cvt_pk_bf16_f32 v9, v10, v11\n"
"v_cvt_pk_bf16_f32 v10, v12, v13\n"
"v_cvt_pk_bf16_f32 v11, v14, v15\n"
"v_mfma_f32_16x16x32_bf16 v[196:199], a[28:31], v[172:175], -4.0\n"
"v_exp_f32 v230, v230\n"
"v_mfma_f32_16x16x32_bf16 v[196:199], a[24:27], v[168:171], v[196:199]\n"
"v_fma_f32 v16, v16, v252, -v223\n"
"v_fma_f32 v17, v17, v252, -v223\n"
"v_mfma_f32_16x16x32_bf16 v[196:199], a[20:23], v[164:167], v[196:199]\n"
"v_exp_f32 v231, v231\n"
"v_mfma_f32_16x16x32_bf16 v[196:199], a[16:19], v[160:163], v[196:199]\n"
"ds_write_b128 v216, v[8:11] offset:64\n"
"v_perm_b32 v215, v231, v230, s72\n"
"v_fma_f32 v18, v18, v252, -v223\n"
"v_mfma_f32_16x16x32_bf16 v[224:227], a[32:35], v[160:163], -4.0\n"
"v_exp_f32 v192, v192\n"
"v_mfma_f32_16x16x32_bf16 v[224:227], a[36:39], v[164:167], v[224:227]\n"
"ds_read_b128 v[4:7], v217 offset:0\n"
"ds_read_b128 v[12:15], v217 offset:576\n"
"v_mfma_f32_16x16x32_bf16 v[224:227], a[40:43], v[168:171], v[224:227]\n"
"v_exp_f32 v193, v193\n"
"v_mfma_f32_16x16x32_bf16 v[224:227], a[44:47], v[172:175], v[224:227]\n"
"v_perm_b32 v200, v193, v192, s72\n"
"v_fma_f32 v19, v19, v252, -v223\n"
"v_fma_f32 v20, v20, v252, -v223\n"
"v_fma_f32 v21, v21, v252, -v223\n"
"v_fma_f32 v22, v22, v252, -v223\n"
"v_fma_f32 v23, v23, v252, -v223\n"
"v_cvt_pk_bf16_f32 v16, v16, v17\n"
"v_cvt_pk_bf16_f32 v17, v18, v19\n"
"v_cvt_pk_bf16_f32 v18, v20, v21\n"
"v_cvt_pk_bf16_f32 v19, v22, v23\n"
"v_mfma_f32_16x16x32_bf16 v[228:231], a[60:63], v[172:175], -4.0\n"
"v_exp_f32 v194, v194\n"
"v_mfma_f32_16x16x32_bf16 v[228:231], a[56:59], v[168:171], v[228:231]\n"
"v_fma_f32 v24, v24, v252, -v223\n"
"v_fma_f32 v25, v25, v252, -v223\n"
"v_mfma_f32_16x16x32_bf16 v[228:231], a[52:55], v[164:167], v[228:231]\n"
"v_exp_f32 v195, v195\n"
"v_mfma_f32_16x16x32_bf16 v[228:231], a[48:51], v[160:163], v[228:231]\n"
"s_waitcnt lgkmcnt(1)\n"
"buffer_store_dwordx4 v[4:7], v250, s[48:51], s84 offen offset:0 sc1\n"
"v_perm_b32 v201, v195, v194, s72\n"
"v_fma_f32 v26, v26, v252, -v223\n"
"v_fma_f32 v27, v27, v252, -v223\n"
"v_smfmac_f32_16x16x64_bf16 a[132:135], a[144:147], v[208:215], v251\n"
"s_waitcnt lgkmcnt(0)\n"
"buffer_store_dwordx4 v[12:15], v250, s[48:51], s84 offen offset:1024 sc1\n"
"v_fma_f32 v28, v28, v252, -v223\n"
"v_fma_f32 v29, v29, v252, -v223\n"
"v_mfma_f32_16x16x32_bf16 v[32:35], a[64:67], v[208:211], v[32:35]\n"
"v_exp_f32 v196, v196\n"
"v_mfma_f32_16x16x32_bf16 v[32:35], a[68:71], v[212:215], v[32:35]\n"
"ds_write_b128 v216, v[16:19] offset:0\n"
"v_fma_f32 v30, v30, v252, -v223\n"
"v_fma_f32 v31, v31, v252, -v223\n"
"v_mfma_f32_16x16x32_bf16 v[36:39], a[76:79], v[212:215], v[36:39]\n"
"v_exp_f32 v197, v197\n"
"v_mfma_f32_16x16x32_bf16 v[36:39], a[72:75], v[208:211], v[36:39]\n"
"v_perm_b32 v202, v197, v196, s72\n"
"v_cvt_pk_bf16_f32 v24, v24, v25\n"
"v_cvt_pk_bf16_f32 v25, v26, v27\n"
"v_cvt_pk_bf16_f32 v26, v28, v29\n"
"v_cvt_pk_bf16_f32 v27, v30, v31\n"
"v_mfma_f32_16x16x32_bf16 v[40:43], a[80:83], v[208:211], v[40:43]\n"
"v_exp_f32 v198, v198\n"
"v_mfma_f32_16x16x32_bf16 v[40:43], a[84:87], v[212:215], v[40:43]\n"
"ds_write_b128 v216, v[24:27] offset:64\n"
"v_accvgpr_read_b32 v254, a132\n"
"v_accvgpr_read_b32 v252, a133\n"
"v_mfma_f32_16x16x32_bf16 v[44:47], a[92:95], v[212:215], v[44:47]\n"
"v_exp_f32 v199, v199\n"
"v_mfma_f32_16x16x32_bf16 v[44:47], a[88:91], v[208:211], v[44:47]\n"
"ds_read_b128 v[20:23], v217 offset:0\n"
"v_perm_b32 v203, v199, v198, s72\n"
"v_mfma_f32_16x16x32_bf16 v[48:51], a[96:99], v[208:211], v[48:51]\n"
"v_exp_f32 v224, v224\n"
"v_mfma_f32_16x16x32_bf16 v[48:51], a[100:103], v[212:215], v[48:51]\n"
"ds_read_b128 v[28:31], v217 offset:576\n"
"v_add_f32 v254, v254, v252\n"
"v_mfma_f32_16x16x32_bf16 v[52:55], a[108:111], v[212:215], v[52:55]\n"
"v_exp_f32 v225, v225\n"
"v_mfma_f32_16x16x32_bf16 v[52:55], a[104:107], v[208:211], v[52:55]\n"
"v_perm_b32 v204, v225, v224, s72\n"
"v_mfma_f32_16x16x32_bf16 v[56:59], a[112:115], v[208:211], v[56:59]\n"
"v_exp_f32 v226, v226\n"
"v_mfma_f32_16x16x32_bf16 v[56:59], a[116:119], v[212:215], v[56:59]\n"
"s_waitcnt lgkmcnt(0)\n"
"buffer_store_dwordx4 v[20:23], v250, s[48:51], s84 offen offset:128 sc1\n"
"v_rcp_f32 v252, v254\n"
"s_nop 0\n"
"v_max_f32 v254, v254, v252\n"
"v_cmp_nlt_f32 s[62:63], v254, s58\n"
"v_mul_f32 v223, v218, v252\n"
"v_mul_f32 v254, 0x3b000000, v223\n"
"v_fma_f32 v32, v32, v252, -v223\n"
"v_fma_f32 v33, v33, v252, -v223\n"
"v_fma_f32 v34, v34, v252, -v223\n"
"v_fma_f32 v35, v35, v252, -v223\n"
"v_fma_f32 v36, v36, v252, -v223\n"
"v_fma_f32 v37, v37, v252, -v223\n"
"v_fma_f32 v38, v38, v252, -v223\n"
"v_fma_f32 v39, v39, v252, -v223\n"
"v_max3_f32 v253, abs(v32), abs(v33), abs(v34)\n"
"v_cmp_lt_f32 vcc, v253, v254\n"
"v_cvt_pk_bf16_f32 v32, v32, v33\n"
"v_cvt_pk_bf16_f32 v33, v34, v35\n"
"v_cvt_pk_bf16_f32 v34, v36, v37\n"
"v_cvt_pk_bf16_f32 v35, v38, v39\n"
"v_mfma_f32_16x16x32_bf16 v[60:63], a[124:127], v[212:215], v[60:63]\n"
"s_and_b32 s73, vcc_lo, vcc_hi\n"
"v_exp_f32 v227, v227\n"
"v_mfma_f32_16x16x32_bf16 v[60:63], a[120:123], v[208:211], v[60:63]\n"
"s_lshr_b32 s74, s73, 16\n"
"buffer_store_dwordx4 v[28:31], v250, s[48:51], s84 offen offset:1152 sc1\n"
"v_perm_b32 v205, v227, v226, s72\n"
"v_fma_f32 v40, v40, v252, -v223\n"
"v_fma_f32 v41, v41, v252, -v223\n"
"v_mfma_f32_16x16x32_bf16 v[192:195], a[0:3], v[176:179], -4.0\n"
"s_and_b32 s73, s73, s74\n"
"v_exp_f32 v228, v228\n"
"v_mfma_f32_16x16x32_bf16 v[192:195], a[4:7], v[180:183], v[192:195]\n"
"s_or_b32 s73, s73, s62\n"
"v_fma_f32 v42, v42, v252, -v223\n"
"v_fma_f32 v43, v43, v252, -v223\n"
"v_mfma_f32_16x16x32_bf16 v[192:195], a[8:11], v[184:187], v[192:195]\n"
"s_or_b32 s73, s73, s63\n"
"v_exp_f32 v229, v229\n"
"v_mfma_f32_16x16x32_bf16 v[192:195], a[12:15], v[188:191], v[192:195]\n"
"s_addc_u32 s57, s57, s57\n"
"ds_write_b128 v216, v[32:35] offset:0\n"
"v_perm_b32 v206, v229, v228, s72\n"
"v_fma_f32 v44, v44, v252, -v223\n"
"v_mfma_f32_16x16x32_bf16 v[196:199], a[28:31], v[188:191], -4.0\n"
"s_add_u32 s84, s84, 4096\n"
"v_exp_f32 v230, v230\n"
"v_mfma_f32_16x16x32_bf16 v[196:199], a[24:27], v[184:187], v[196:199]\n"
"v_fma_f32 v45, v45, v252, -v223\n"
"v_fma_f32 v46, v46, v252, -v223\n"
"v_mfma_f32_16x16x32_bf16 v[196:199], a[20:23], v[180:183], v[196:199]\n"
"v_exp_f32 v231, v231\n"
"v_mfma_f32_16x16x32_bf16 v[196:199], a[16:19], v[176:179], v[196:199]\n"
"v_perm_b32 v207, v231, v230, s72\n"
"v_fma_f32 v47, v47, v252, -v223\n"
"v_cvt_pk_bf16_f32 v40, v40, v41\n"
"v_cvt_pk_bf16_f32 v41, v42, v43\n"
"v_cvt_pk_bf16_f32 v42, v44, v45\n"
"v_cvt_pk_bf16_f32 v43, v46, v47\n"
"v_mfma_f32_16x16x32_bf16 v[224:227], a[32:35], v[176:179], -4.0\n"
"v_exp_f32 v192, v192\n"
"v_mfma_f32_16x16x32_bf16 v[224:227], a[36:39], v[180:183], v[224:227]\n"
"v_fma_f32 v48, v48, v252, -v223\n"
"v_fma_f32 v49, v49, v252, -v223\n"
"v_mfma_f32_16x16x32_bf16 v[224:227], a[40:43], v[184:187], v[224:227]\n"
"v_exp_f32 v193, v193\n"
"v_mfma_f32_16x16x32_bf16 v[224:227], a[44:47], v[188:191], v[224:227]\n"
"ds_write_b128 v216, v[40:43] offset:64\n"
"v_perm_b32 v208, v193, v192, s72\n"
"v_fma_f32 v50, v50, v252, -v223\n"
"v_mfma_f32_16x16x32_bf16 v[228:231], a[60:63], v[188:191], -4.0\n"
"v_exp_f32 v194, v194\n"
"v_mfma_f32_16x16x32_bf16 v[228:231], a[56:59], v[184:187], v[228:231]\n"
"ds_read_b128 v[36:39], v217 offset:0\n"
"ds_read_b128 v[44:47], v217 offset:576\n"
"v_mfma_f32_16x16x32_bf16 v[228:231], a[52:55], v[180:183], v[228:231]\n"
"v_exp_f32 v195, v195\n"
"v_mfma_f32_16x16x32_bf16 v[228:231], a[48:51], v[176:179], v[228:231]\n"
"v_perm_b32 v209, v195, v194, s72\n"
"v_fma_f32 v51, v51, v252, -v223\n"
"v_fma_f32 v52, v52, v252, -v223\n"
"v_fma_f32 v53, v53, v252, -v223\n"
"v_fma_f32 v54, v54, v252, -v223\n"
"v_fma_f32 v55, v55, v252, -v223\n"
"v_cvt_pk_bf16_f32 v48, v48, v49\n"
"v_cvt_pk_bf16_f32 v49, v50, v51\n"
"v_cvt_pk_bf16_f32 v50, v52, v53\n"
"v_cvt_pk_bf16_f32 v51, v54, v55\n"
"v_smfmac_f32_16x16x64_bf16 a[136:139], a[144:147], v[200:207], v251\n"
"ds_write_b128 v216, v[48:51] offset:0\n"
"v_fma_f32 v56, v56, v252, -v223\n"
"v_mfma_f32_16x16x32_bf16 v[64:67], a[64:67], v[200:203], v[64:67]\n"
"v_exp_f32 v196, v196\n"
"v_mfma_f32_16x16x32_bf16 v[64:67], a[68:71], v[204:207], v[64:67]\n"
"v_fma_f32 v57, v57, v252, -v223\n"
"v_fma_f32 v58, v58, v252, -v223\n"
"v_mfma_f32_16x16x32_bf16 v[68:71], a[76:79], v[204:207], v[68:71]\n"
"v_exp_f32 v197, v197\n"
"v_mfma_f32_16x16x32_bf16 v[68:71], a[72:75], v[200:203], v[68:71]\n"
"s_waitcnt lgkmcnt(2)\n"
"buffer_store_dwordx4 v[36:39], v250, s[48:51], s84 offen offset:0 sc1\n"
"v_perm_b32 v210, v197, v196, s72\n"
"v_fma_f32 v59, v59, v252, -v223\n"
"v_fma_f32 v60, v60, v252, -v223\n"
"v_mfma_f32_16x16x32_bf16 v[72:75], a[80:83], v[200:203], v[72:75]\n"
"v_exp_f32 v198, v198\n"
"v_mfma_f32_16x16x32_bf16 v[72:75], a[84:87], v[204:207], v[72:75]\n"
"s_waitcnt lgkmcnt(1)\n"
"buffer_store_dwordx4 v[44:47], v250, s[48:51], s84 offen offset:1024 sc1\n"
"v_fma_f32 v61, v61, v252, -v223\n"
"v_fma_f32 v62, v62, v252, -v223\n"
"v_fma_f32 v63, v63, v252, -v223\n"
"Lhop2_3_%=:\n s_cbranch_execz Lback2_%=\n"
"v_mfma_f32_16x16x32_bf16 v[76:79], a[92:95], v[204:207], v[76:79]\n"
"v_exp_f32 v199, v199\n"
"v_mfma_f32_16x16x32_bf16 v[76:79], a[88:91], v[200:203], v[76:79]\n"
"v_perm_b32 v211, v199, v198, s72\n"
"v_cvt_pk_bf16_f32 v56, v56, v57\n"
"v_cvt_pk_bf16_f32 v57, v58, v59\n"
"v_cvt_pk_bf16_f32 v58, v60, v61\n"
"v_cvt_pk_bf16_f32 v59, v62, v63\n"
"v_mfma_f32_16x16x32_bf16 v[80:83], a[96:99], v[200:203], v[80:83]\n"
"v_exp_f32 v224, v224\n"
"v_mfma_f32_16x16x32_bf16 v[80:83], a[100:103], v[204:207], v[80:83]\n"
"ds_write_b128 v216, v[56:59] offset:64\n"
"v_accvgpr_read_b32 v254, a136\n"
"v_accvgpr_read_b32 v252, a137\n"
"v_mfma_f32_16x16x32_bf16 v[84:87], a[108:111], v[204:207], v[84:87]\n"
"v_exp_f32 v225, v225\n"
"v_mfma_f32_16x16x32_bf16 v[84:87], a[104:107], v[200:203], v[84:87]\n"
"ds_read_b128 v[52:55], v217 offset:0\n"
"v_perm_b32 v212, v225, v224, s72\n"
"v_mfma_f32_16x16x32_bf16 v[88:91], a[112:115], v[200:203], v[88:91]\n"
"v_exp_f32 v226, v226\n"
"v_mfma_f32_16x16x32_bf16 v[88:91], a[116:119], v[204:207], v[88:91]\n"
"ds_read_b128 v[60:63], v217 offset:576\n"
"v_add_f32 v254, v254, v252\n"
"v_mfma_f32_16x16x32_bf16 v[92:95], a[124:127], v[204:207], v[92:95]\n"
"v_exp_f32 v227, v227\n"
"v_mfma_f32_16x16x32_bf16 v[92:95], a[120:123], v[200:203], v[92:95]\n"
"v_perm_b32 v213, v227, v226, s72\n"
"v_exp_f32 v228, v228\n"
"v_rcp_f32 v252, v254\n"
"s_nop 0\n"
"v_max_f32 v254, v254, v252\n"
"v_cmp_nlt_f32 s[62:63], v254, s58\n"
"v_mul_f32 v223, v218, v252\n"
"v_mul_f32 v254, 0x3b000000, v223\n"
"v_fma_f32 v64, v64, v252, -v223\n"
"v_fma_f32 v65, v65, v252, -v223\n"
"v_fma_f32 v66, v66, v252, -v223\n"
"v_fma_f32 v67, v67, v252, -v223\n"
"v_fma_f32 v68, v68, v252, -v223\n"
"v_fma_f32 v69, v69, v252, -v223\n"
"v_fma_f32 v70, v70, v252, -v223\n"
"v_fma_f32 v71, v71, v252, -v223\n"
"v_max3_f32 v253, abs(v64), abs(v65), abs(v66)\n"
"v_cmp_lt_f32 vcc, v253, v254\n"
"v_cvt_pk_bf16_f32 v64, v64, v65\n"
"v_cvt_pk_bf16_f32 v65, v66, v67\n"
"v_cvt_pk_bf16_f32 v66, v68, v69\n"
"v_cvt_pk_bf16_f32 v67, v70, v71\n"
"s_and_b32 s73, vcc_lo, vcc_hi\n"
"ds_write_b128 v216, v[64:67] offset:0\n"
"v_fma_f32 v72, v72, v252, -v223\n"
"v_fma_f32 v73, v73, v252, -v223\n"
"s_lshr_b32 s74, s73, 16\n"
"s_waitcnt lgkmcnt(2)\n"
"buffer_store_dwordx4 v[52:55], v250, s[48:51], s84 offen offset:128 sc1\n"
"v_exp_f32 v229, v229\n"
"v_fma_f32 v74, v74, v252, -v223\n"
"s_and_b32 s73, s73, s74\n"
"s_waitcnt lgkmcnt(1)\n"
"buffer_store_dwordx4 v[60:63], v250, s[48:51], s84 offen offset:1152 sc1\n"
"v_perm_b32 v214, v229, v228, s72\n"
"v_fma_f32 v75, v75, v252, -v223\n"
"v_fma_f32 v76, v76, v252, -v223\n"
"s_or_b32 s73, s73, s62\n"
"v_exp_f32 v230, v230\n"
"s_or_b32 s73, s73, s63\n"
"v_fma_f32 v77, v77, v252, -v223\n"
"v_fma_f32 v78, v78, v252, -v223\n"
"s_addc_u32 s57, s57, s57\n"
"v_exp_f32 v231, v231\n"
"s_add_u32 s84, s84, 4096\n"
"v_perm_b32 v215, v231, v230, s72\n"
"v_fma_f32 v79, v79, v252, -v223\n"
"v_cvt_pk_bf16_f32 v72, v72, v73\n"
"v_cvt_pk_bf16_f32 v73, v74, v75\n"
"v_cvt_pk_bf16_f32 v74, v76, v77\n"
"v_cvt_pk_bf16_f32 v75, v78, v79\n"
"ds_write_b128 v216, v[72:75] offset:64\n"
"v_fma_f32 v80, v80, v252, -v223\n"
"v_fma_f32 v81, v81, v252, -v223\n"
"ds_read_b128 v[68:71], v217 offset:0\n"
"ds_read_b128 v[76:79], v217 offset:576\n"
"v_fma_f32 v82, v82, v252, -v223\n"
"v_fma_f32 v83, v83, v252, -v223\n"
"v_fma_f32 v84, v84, v252, -v223\n"
"v_fma_f32 v85, v85, v252, -v223\n"
"v_fma_f32 v86, v86, v252, -v223\n"
"v_fma_f32 v87, v87, v252, -v223\n"
"v_cvt_pk_bf16_f32 v80, v80, v81\n"
"v_cvt_pk_bf16_f32 v81, v82, v83\n"
"v_smfmac_f32_16x16x64_bf16 a[140:143], a[144:147], v[208:215], v251\n"
"s_waitcnt lgkmcnt(1)\n"
"buffer_store_dwordx4 v[68:71], v250, s[48:51], s84 offen offset:0 sc1\n"
"v_cvt_pk_bf16_f32 v82, v84, v85\n"
"v_cvt_pk_bf16_f32 v83, v86, v87\n"
"v_mfma_f32_16x16x32_bf16 v[96:99], a[64:67], v[208:211], v[96:99]\n"
"v_fma_f32 v88, v88, v252, -v223\n"
"v_fma_f32 v89, v89, v252, -v223\n"
"v_mfma_f32_16x16x32_bf16 v[96:99], a[68:71], v[212:215], v[96:99]\n"
"s_waitcnt lgkmcnt(0)\n"
"buffer_store_dwordx4 v[76:79], v250, s[48:51], s84 offen offset:1024 sc1\n"
"v_fma_f32 v90, v90, v252, -v223\n"
"v_fma_f32 v91, v91, v252, -v223\n"
"v_fma_f32 v92, v92, v252, -v223\n"
"v_mfma_f32_16x16x32_bf16 v[100:103], a[76:79], v[212:215], v[100:103]\n"
"v_fma_f32 v93, v93, v252, -v223\n"
"v_fma_f32 v94, v94, v252, -v223\n"
"v_mfma_f32_16x16x32_bf16 v[100:103], a[72:75], v[208:211], v[100:103]\n"
"ds_write_b128 v216, v[80:83] offset:0\n"
"v_fma_f32 v95, v95, v252, -v223\n"
"v_cvt_pk_bf16_f32 v88, v88, v89\n"
"v_mfma_f32_16x16x32_bf16 v[104:107], a[80:83], v[208:211], v[104:107]\n"
"v_cvt_pk_bf16_f32 v89, v90, v91\n"
"v_cvt_pk_bf16_f32 v90, v92, v93\n"
"v_mfma_f32_16x16x32_bf16 v[104:107], a[84:87], v[212:215], v[104:107]\n"
"v_cvt_pk_bf16_f32 v91, v94, v95\n"
"v_accvgpr_read_b32 v254, a140\n"
"v_mfma_f32_16x16x32_bf16 v[108:111], a[92:95], v[212:215], v[108:111]\n"
"v_accvgpr_read_b32 v252, a141\n"
"v_add_f32 v254, v254, v252\n"
"v_mfma_f32_16x16x32_bf16 v[108:111], a[88:91], v[208:211], v[108:111]\n"
"ds_write_b128 v216, v[88:91] offset:64\n"
"v_rcp_f32 v252, v254\n"
"v_mfma_f32_16x16x32_bf16 v[112:115], a[96:99], v[208:211], v[112:115]\n"
"ds_read_b128 v[84:87], v217 offset:0\n"
"ds_read_b128 v[92:95], v217 offset:576\n"
"v_mfma_f32_16x16x32_bf16 v[112:115], a[100:103], v[212:215], v[112:115]\n"
"v_max_f32 v254, v254, v252\n"
"v_cmp_nlt_f32 s[62:63], v254, s58\n"
"v_mfma_f32_16x16x32_bf16 v[116:119], a[108:111], v[212:215], v[116:119]\n"
"v_mul_f32 v223, v218, v252\n"
"v_mul_f32 v254, 0x3b000000, v223\n"
"v_mfma_f32_16x16x32_bf16 v[116:119], a[104:107], v[208:211], v[116:119]\n"
"v_fma_f32 v96, v96, v252, -v223\n"
"v_fma_f32 v97, v97, v252, -v223\n"
"v_mfma_f32_16x16x32_bf16 v[120:123], a[112:115], v[208:211], v[120:123]\n"
"v_fma_f32 v98, v98, v252, -v223\n"
"v_fma_f32 v99, v99, v252, -v223\n"
"v_mfma_f32_16x16x32_bf16 v[120:123], a[116:119], v[212:215], v[120:123]\n"
"v_fma_f32 v100, v100, v252, -v223\n"
"v_fma_f32 v101, v101, v252, -v223\n"
"v_mfma_f32_16x16x32_bf16 v[124:127], a[124:127], v[212:215], v[124:127]\n"
"v_fma_f32 v102, v102, v252, -v223\n"
"v_fma_f32 v103, v103, v252, -v223\n"
"v_mfma_f32_16x16x32_bf16 v[124:127], a[120:123], v[208:211], v[124:127]\n"
"s_waitcnt lgkmcnt(1)\n"
"buffer_store_dwordx4 v[84:87], v250, s[48:51], s84 offen offset:128 sc1\n"
"v_max3_f32 v253, abs(v96), abs(v97), abs(v98)\n"
"v_cmp_lt_f32 vcc, v253, v254\n"
"v_cvt_pk_bf16_f32 v96, v96, v97\n"
"s_and_b32 s73, vcc_lo, vcc_hi\n"
"v_cvt_pk_bf16_f32 v97, v98, v99\n"
"v_cvt_pk_bf16_f32 v98, v100, v101\n"
"v_cvt_pk_bf16_f32 v99, v102, v103\n"
"s_lshr_b32 s74, s73, 16\n"
"ds_write_b128 v216, v[96:99] offset:0\n"
"v_fma_f32 v104, v104, v252, -v223\n"
"v_fma_f32 v105, v105, v252, -v223\n"
"v_fma_f32 v106, v106, v252, -v223\n"
"v_fma_f32 v107, v107, v252, -v223\n"
"v_fma_f32 v108, v108, v252, -v223\n"
"v_fma_f32 v109, v109, v252, -v223\n"
"v_fma_f32 v110, v110, v252, -v223\n"
"v_fma_f32 v111, v111, v252, -v223\n"
"v_cvt_pk_bf16_f32 v104, v104, v105\n"
"v_cvt_pk_bf16_f32 v105, v106, v107\n"
"v_cvt_pk_bf16_f32 v106, v108, v109\n"
"v_cvt_pk_bf16_f32 v107, v110, v111\n"
"s_and_b32 s73, s73, s74\n"
"s_waitcnt lgkmcnt(1)\n"
"buffer_store_dwordx4 v[92:95], v250, s[48:51], s84 offen offset:1152 sc1\n"
"v_fma_f32 v112, v112, v252, -v223\n"
"v_fma_f32 v113, v113, v252, -v223\n"
"v_fma_f32 v114, v114, v252, -v223\n"
"v_fma_f32 v115, v115, v252, -v223\n"
"v_fma_f32 v116, v116, v252, -v223\n"
"v_fma_f32 v117, v117, v252, -v223\n"
"v_fma_f32 v118, v118, v252, -v223\n"
"v_fma_f32 v119, v119, v252, -v223\n"
"v_cvt_pk_bf16_f32 v112, v112, v113\n"
"v_cvt_pk_bf16_f32 v113, v114, v115\n"
"v_cvt_pk_bf16_f32 v114, v116, v117\n"
"v_cvt_pk_bf16_f32 v115, v118, v119\n"
"s_or_b32 s73, s73, s62\n"
"ds_write_b128 v216, v[104:107] offset:64\n"
"v_fma_f32 v120, v120, v252, -v223\n"
"v_fma_f32 v121, v121, v252, -v223\n"
"v_fma_f32 v122, v122, v252, -v223\n"
"v_fma_f32 v123, v123, v252, -v223\n"
"v_fma_f32 v124, v124, v252, -v223\n"
"v_fma_f32 v125, v125, v252, -v223\n"
"v_fma_f32 v126, v126, v252, -v223\n"
"v_fma_f32 v127, v127, v252, -v223\n"
"v_cvt_pk_bf16_f32 v120, v120, v121\n"
"v_cvt_pk_bf16_f32 v121, v122, v123\n"
"v_cvt_pk_bf16_f32 v122, v124, v125\n"
"v_cvt_pk_bf16_f32 v123, v126, v127\n"
"s_or_b32 s73, s73, s63\n"
"ds_read_b128 v[100:103], v217 offset:0\n"
"ds_read_b128 v[108:111], v217 offset:576\n"
"ds_write_b128 v216, v[112:115] offset:0\n"
"s_addc_u32 s57, s57, s57\n"
"ds_write_b128 v216, v[120:123] offset:64\n"
"s_mov_b32 s84, s85\n"
"ds_read_b128 v[116:119], v217 offset:0\n"
"ds_read_b128 v[124:127], v217 offset:576\n"
"s_waitcnt lgkmcnt(5)\n"
"buffer_store_dwordx4 v[100:103], v250, s[48:51], s84 offen offset:0 sc1\n"
"s_waitcnt lgkmcnt(4)\n"
"buffer_store_dwordx4 v[108:111], v250, s[48:51], s84 offen offset:1024 sc1\n"
"s_waitcnt lgkmcnt(1)\n"
"buffer_store_dwordx4 v[116:119], v250, s[48:51], s84 offen offset:128 sc1\n"
"s_waitcnt lgkmcnt(0)\n"
"buffer_store_dwordx4 v[124:127], v250, s[48:51], s84 offen offset:1152 sc1\n"
"s_cmp_eq_u32 s57, 0\n"
"s_cbranch_scc0 Lend%=\n"
"s_endpgm\n"
"LHS%=:\n"
"v_mfma_f32_16x16x32_bf16 v[192:195], a[32:35], v[176:179], -4.0\n"
"Lhop0_2_%=:\n s_cbranch_execz Lhop0_6_%=\n"
"v_exp_f32 v228, v228\n"
"v_mfma_f32_16x16x32_bf16 v[192:195], a[36:39], v[180:183], v[192:195]\n"
"ds_read_b128 a[0:3], v244 offset:17408\n"
"ds_read_b128 a[4:7], v244 offset:17472\n"
"v_mfma_f32_16x16x32_bf16 v[192:195], a[40:43], v[184:187], v[192:195]\n"
"v_exp_f32 v229, v229\n"
"v_mfma_f32_16x16x32_bf16 v[192:195], a[44:47], v[188:191], v[192:195]\n"
"ds_read_b128 a[8:11], v244 offset:17536\n"
"v_perm_b32 v206, v229, v228, s72\n"
"v_mfma_f32_16x16x32_bf16 v[224:227], a[60:63], v[188:191], -4.0\n"
"s_add_u32 m0, s54, 33024\n"
"v_exp_f32 v230, v230\n"
"v_mfma_f32_16x16x32_bf16 v[224:227], a[56:59], v[184:187], v[224:227]\n"
"buffer_load_dwordx4 v248, s[40:43], s52 offen offset:0 lds\n"
"v_mfma_f32_16x16x32_bf16 v[224:227], a[52:55], v[180:183], v[224:227]\n"
"v_exp_f32 v231, v231\n"
"v_mfma_f32_16x16x32_bf16 v[224:227], a[48:51], v[176:179], v[224:227]\n"
"ds_read_b128 a[12:15], v244 offset:17600\n"
"v_perm_b32 v207, v231, v230, s72\n"
"v_mfma_f32_16x16x32_bf16 v[64:67], a[64:67], v[200:203], v[218:221]\n"
"v_exp_f32 v192, v192\n"
"v_mfma_f32_16x16x32_bf16 v[64:67], a[68:71], v[204:207], v[64:67]\n"
"ds_read_b128 a[28:31], v244 offset:21952\n"
"ds_read_b128 a[24:27], v244 offset:21888\n"
"v_mfma_f32_16x16x32_bf16 v[68:71], a[76:79], v[204:207], v[218:221]\n"
"v_exp_f32 v193, v193\n"
"v_mfma_f32_16x16x32_bf16 v[68:71], a[72:75], v[200:203], v[68:71]\n"
"ds_read_b128 a[20:23], v244 offset:21824\n"
"v_perm_b32 v212, v193, v192, s72\n"
"v_mfma_f32_16x16x32_bf16 v[72:75], a[80:83], v[200:203], v[218:221]\n"
"s_add_u32 m0, s54, 33032\n"
"v_exp_f32 v194, v194\n"
"v_mfma_f32_16x16x32_bf16 v[72:75], a[84:87], v[204:207], v[72:75]\n"
"buffer_load_dwordx4 v248, s[40:43], s52 offen offset:1024 lds\n"
"v_mfma_f32_16x16x32_bf16 v[76:79], a[92:95], v[204:207], v[218:221]\n"
"v_exp_f32 v195, v195\n"
"v_mfma_f32_16x16x32_bf16 v[76:79], a[88:91], v[200:203], v[76:79]\n"
"ds_read_b128 a[16:19], v244 offset:21760\n"
"v_perm_b32 v213, v195, v194, s72\n"
"v_smfmac_f32_16x16x64_bf16 a[136:139], a[144:147], v[200:207], v251\n"
"s_waitcnt lgkmcnt(5)\n"
"ds_read_b128 a[32:35], v222 offset:17408\n"
"v_mfma_f32_16x16x32_bf16 v[192:195], a[0:3], v[128:131], -4.0\n"
"v_exp_f32 v224, v224\n"
"v_mfma_f32_16x16x32_bf16 v[192:195], a[4:7], v[132:135], v[192:195]\n"
"ds_read_b128 a[36:39], v222 offset:17472\n"
"ds_read_b128 a[40:43], v222 offset:17536\n"
"v_mfma_f32_16x16x32_bf16 v[192:195], a[8:11], v[136:139], v[192:195]\n"
"s_waitcnt lgkmcnt(7)\n"
"v_exp_f32 v225, v225\n"
"v_mfma_f32_16x16x32_bf16 v[192:195], a[12:15], v[140:143], v[192:195]\n"
"s_waitcnt vmcnt(6) lgkmcnt(5)\n"
"ds_read_b128 a[44:47], v222 offset:17600\n"
"v_perm_b32 v214, v225, v224, s72\n"
"v_mfma_f32_16x16x32_bf16 v[196:199], a[28:31], v[140:143], -4.0\n"
"s_add_u32 m0, s54, 34960\n"
"v_exp_f32 v226, v226\n"
"v_mfma_f32_16x16x32_bf16 v[196:199], a[24:27], v[136:139], v[196:199]\n"
"s_waitcnt lgkmcnt(5)\n"
"buffer_load_dwordx4 v248, s[40:43], s52 offen offset:128 lds\n"
"v_mfma_f32_16x16x32_bf16 v[196:199], a[20:23], v[132:135], v[196:199]\n"
"s_waitcnt lgkmcnt(4)\n"
"v_exp_f32 v227, v227\n"
"v_mfma_f32_16x16x32_bf16 v[196:199], a[16:19], v[128:131], v[196:199]\n"
"s_barrier\n"
"ds_read_b128 a[60:63], v222 offset:21952\n"
"v_perm_b32 v215, v227, v226, s72\n"
"v_mfma_f32_16x16x32_bf16 v[80:83], a[96:99], v[200:203], v[218:221]\n"
"v_exp_f32 v192, v192\n"
"v_mfma_f32_16x16x32_bf16 v[80:83], a[100:103], v[204:207], v[80:83]\n"
"ds_read_b128 a[56:59], v222 offset:21888\n"
"ds_read_b128 a[52:55], v222 offset:21824\n"
"v_mfma_f32_16x16x32_bf16 v[84:87], a[108:111], v[204:207], v[218:221]\n"
"v_exp_f32 v193, v193\n"
"v_mfma_f32_16x16x32_bf16 v[84:87], a[104:107], v[200:203], v[84:87]\n"
"ds_read_b128 a[48:51], v222 offset:21760\n"
"ds_read_b64_tr_b16 a[64:65], v245 offset:16512\n"
"v_mfma_f32_16x16x32_bf16 v[88:91], a[112:115], v[200:203], v[218:221]\n"
"s_add_u32 m0, s54, 34968\n"
"v_exp_f32 v194, v194\n"
"v_mfma_f32_16x16x32_bf16 v[88:91], a[116:119], v[204:207], v[88:91]\n"
"buffer_load_dwordx4 v248, s[40:43], s52 offen offset:1152 lds\n"
"v_mfma_f32_16x16x32_bf16 v[92:95], a[124:127], v[204:207], v[218:221]\n"
"s_add_u32 s52, s52, 16384\n"
"v_exp_f32 v195, v195\n"
"v_mfma_f32_16x16x32_bf16 v[92:95], a[120:123], v[200:203], v[92:95]\n"
"s_waitcnt lgkmcnt(5)\n"
"v_perm_b32 v200, v193, v192, s72\n"
"v_perm_b32 v201, v195, v194, s72\n"
"s_getreg_b32 s73, hwreg(HW_REG_XCC_ID, 0, 3)\n s_lshl_b32 s73, s73, 2\n s_lshr_b32 s73, 0x9050905, s73\n s_and_b32 s73, s73, 15\n s_cbranch_scc0 Lgoxt3S%=\n Lslxt3S%=:\n s_sleep 4\n s_sub_u32 s73, s73, 1\n s_cmp_lg_u32 s73, 0\n s_cbranch_scc1 Lslxt3S%=\n Lgoxt3S%=:\n"
"v_mfma_f32_16x16x32_bf16 v[224:227], a[32:35], v[128:131], -4.0\n"
"v_exp_f32 v196, v196\n"
"v_mfma_f32_16x16x32_bf16 v[224:227], a[36:39], v[132:135], v[224:227]\n"
"ds_read_b64_tr_b16 a[66:67], v245 offset:20640\n"
"ds_read_b64_tr_b16 a[72:73], v245 offset:16520\n"
"v_mfma_f32_16x16x32_bf16 v[224:227], a[40:43], v[136:139], v[224:227]\n"
"v_exp_f32 v197, v197\n"
"v_mfma_f32_16x16x32_bf16 v[224:227], a[44:47], v[140:143], v[224:227]\n"
"s_waitcnt lgkmcnt(5)\n"
"ds_read_b64_tr_b16 a[74:75], v245 offset:20648\n"
"v_perm_b32 v202, v197, v196, s72\n"
"v_mfma_f32_16x16x32_bf16 v[228:231], a[60:63], v[140:143], -4.0\n"
"s_add_u32 m0, s55, 0\n"
"v_exp_f32 v198, v198\n"
"v_mfma_f32_16x16x32_bf16 v[228:231], a[56:59], v[136:139], v[228:231]\n"
"s_waitcnt lgkmcnt(5)\n"
"buffer_load_dwordx4 v249, s[44:47], s76 offen offset:0 nt lds\n"
"v_mfma_f32_16x16x32_bf16 v[228:231], a[52:55], v[132:135], v[228:231]\n"
"s_waitcnt lgkmcnt(3)\n"
"v_exp_f32 v199, v199\n"
"v_mfma_f32_16x16x32_bf16 v[228:231], a[48:51], v[128:131], v[228:231]\n"
"ds_read_b64_tr_b16 a[80:81], v245 offset:16768\n"
"v_perm_b32 v203, v199, v198, s72\n"
"v_mfma_f32_16x16x32_bf16 a[140:143], a[144:147], v[212:215], a[140:143]\n"
"ds_read_b64_tr_b16 a[82:83], v245 offset:20896\n"
"ds_read_b64_tr_b16 a[88:89], v245 offset:16776\n"
"v_mfma_f32_16x16x32_bf16 v[96:99], a[68:71], v[212:215], v[218:221]\n"
"v_exp_f32 v224, v224\n"
"v_mfma_f32_16x16x32_bf16 v[100:103], a[76:79], v[212:215], v[218:221]\n"
"ds_read_b64_tr_b16 a[68:69], v255 offset:16512\n"
"ds_read_b64_tr_b16 a[70:71], v255 offset:20640\n"
"v_mfma_f32_16x16x32_bf16 v[104:107], a[84:87], v[212:215], v[218:221]\n"
"v_exp_f32 v225, v225\n"
"v_mfma_f32_16x16x32_bf16 v[108:111], a[92:95], v[212:215], v[218:221]\n"
"ds_read_b64_tr_b16 a[76:77], v255 offset:16520\n"
"v_perm_b32 v204, v225, v224, s72\n"
"v_mfma_f32_16x16x32_bf16 v[112:115], a[100:103], v[212:215], v[218:221]\n"
"s_add_u32 m0, s55, 8\n"
"v_exp_f32 v226, v226\n"
"v_mfma_f32_16x16x32_bf16 v[116:119], a[108:111], v[212:215], v[218:221]\n"
"buffer_load_dwordx4 v249, s[44:47], s76 offen offset:1024 nt lds\n"
"v_mfma_f32_16x16x32_bf16 v[120:123], a[116:119], v[212:215], v[218:221]\n"
"v_exp_f32 v227, v227\n"
"v_mfma_f32_16x16x32_bf16 v[124:127], a[124:127], v[212:215], v[218:221]\n"
"ds_read_b64_tr_b16 a[78:79], v255 offset:20648\n"
"v_perm_b32 v205, v227, v226, s72\n"
"v_mfma_f32_16x16x32_bf16 v[192:195], a[0:3], v[144:147], -4.0\n"
"v_exp_f32 v228, v228\n"
"v_mfma_f32_16x16x32_bf16 v[192:195], a[4:7], v[148:151], v[192:195]\n"
"ds_read_b64_tr_b16 a[84:85], v255 offset:16768\n"
"ds_read_b64_tr_b16 a[86:87], v255 offset:20896\n"
"v_mfma_f32_16x16x32_bf16 v[192:195], a[8:11], v[152:155], v[192:195]\n"
"v_exp_f32 v229, v229\n"
"v_mfma_f32_16x16x32_bf16 v[192:195], a[12:15], v[156:159], v[192:195]\n"
"ds_read_b64_tr_b16 a[92:93], v255 offset:16776\n"
"v_perm_b32 v206, v229, v228, s72\n"
"v_mfma_f32_16x16x32_bf16 v[196:199], a[28:31], v[156:159], -4.0\n"
"s_add_u32 m0, s55, 16\n"
"v_exp_f32 v230, v230\n"
"v_mfma_f32_16x16x32_bf16 v[196:199], a[24:27], v[152:155], v[196:199]\n"
"buffer_load_dwordx4 v249, s[44:47], s76 offen offset:2048 nt lds\n"
"v_mfma_f32_16x16x32_bf16 v[196:199], a[20:23], v[148:151], v[196:199]\n"
"v_exp_f32 v231, v231\n"
"v_mfma_f32_16x16x32_bf16 v[196:199], a[16:19], v[144:147], v[196:199]\n"
"ds_read_b64_tr_b16 a[94:95], v255 offset:20904\n"
"v_perm_b32 v207, v231, v230, s72\n"
"v_mfma_f32_16x16x32_bf16 v[224:227], a[32:35], v[144:147], -4.0\n"
"v_exp_f32 v192, v192\n"
"v_mfma_f32_16x16x32_bf16 v[224:227], a[36:39], v[148:151], v[224:227]\n"
"ds_read_b64_tr_b16 a[90:91], v245 offset:20904\n"
"ds_read_b64_tr_b16 a[96:97], v245 offset:18576\n"
"v_mfma_f32_16x16x32_bf16 v[224:227], a[40:43], v[152:155], v[224:227]\n"
"v_exp_f32 v193, v193\n"
"v_mfma_f32_16x16x32_bf16 v[224:227], a[44:47], v[156:159], v[224:227]\n"
"ds_read_b64_tr_b16 a[98:99], v245 offset:22704\n"
"v_perm_b32 v208, v193, v192, s72\n"
"v_mfma_f32_16x16x32_bf16 v[228:231], a[60:63], v[156:159], -4.0\n"
"s_add_u32 m0, s55, 24\n"
"v_exp_f32 v194, v194\n"
"v_mfma_f32_16x16x32_bf16 v[228:231], a[56:59], v[152:155], v[228:231]\n"
"buffer_load_dwordx4 v249, s[44:47], s76 offen offset:3072 nt lds\n"
"v_mfma_f32_16x16x32_bf16 v[228:231], a[52:55], v[148:151], v[228:231]\n"
"s_add_u32 s76, s76, 4096\n"
"v_exp_f32 v195, v195\n"
"v_mfma_f32_16x16x32_bf16 v[228:231], a[48:51], v[144:147], v[228:231]\n"
"ds_read_b64_tr_b16 a[100:101], v255 offset:18576\n"
"v_perm_b32 v209, v195, v194, s72\n"
"v_smfmac_f32_16x16x64_bf16 a[128:131], a[144:147], v[200:207], v251\n"
"s_waitcnt lgkmcnt(5)\n"
"ds_read_b64_tr_b16 a[102:103], v255 offset:22704\n"
"v_mfma_f32_16x16x32_bf16 v[0:3], a[64:67], v[200:203], v[0:3]\n"
"v_exp_f32 v196, v196\n"
"v_mfma_f32_16x16x32_bf16 v[0:3], a[68:71], v[204:207], v[0:3]\n"
"ds_read_b64_tr_b16 a[108:109], v255 offset:18584\n"
"ds_read_b64_tr_b16 a[110:111], v255 offset:22712\n"
"v_mfma_f32_16x16x32_bf16 v[4:7], a[76:79], v[204:207], v[4:7]\n"
"v_exp_f32 v197, v197\n"
"v_mfma_f32_16x16x32_bf16 v[4:7], a[72:75], v[200:203], v[4:7]\n"
"ds_read_b64_tr_b16 a[104:105], v245 offset:18584\n"
"v_perm_b32 v210, v197, v196, s72\n"
"v_mfma_f32_16x16x32_bf16 v[8:11], a[80:83], v[200:203], v[8:11]\n"
"s_add_u32 m0, s53, 52224\n"
"v_exp_f32 v198, v198\n"
"v_mfma_f32_16x16x32_bf16 v[8:11], a[84:87], v[204:207], v[8:11]\n"
"s_waitcnt lgkmcnt(5)\n"
"buffer_load_dwordx4 v247, s[36:39], s52 offen offset:0 lds\n"
"v_mfma_f32_16x16x32_bf16 v[12:15], a[92:95], v[204:207], v[12:15]\n"
"v_exp_f32 v199, v199\n"
"v_mfma_f32_16x16x32_bf16 v[12:15], a[88:91], v[200:203], v[12:15]\n"
"ds_read_b64_tr_b16 a[106:107], v245 offset:22712\n"
"ds_read_b64_tr_b16 a[112:113], v245 offset:18832\n"
"ds_read_b64_tr_b16 a[114:115], v245 offset:22960\n"
"ds_read_b64_tr_b16 a[116:117], v255 offset:18832\n"
"ds_read_b64_tr_b16 a[118:119], v255 offset:22960\n"
"ds_read_b64_tr_b16 a[124:125], v255 offset:18840\n"
"ds_read_b64_tr_b16 a[126:127], v255 offset:22968\n"
"v_perm_b32 v211, v199, v198, s72\n"
"v_mfma_f32_16x16x32_bf16 v[16:19], a[96:99], v[200:203], v[16:19]\n"
"s_waitcnt lgkmcnt(10)\n"
"v_exp_f32 v224, v224\n"
"v_mfma_f32_16x16x32_bf16 v[16:19], a[100:103], v[204:207], v[16:19]\n"
"s_waitcnt lgkmcnt(8)\n"
"ds_read_b64_tr_b16 a[120:121], v245 offset:18840\n"
"ds_read_b64_tr_b16 a[122:123], v245 offset:22968\n"
"v_mfma_f32_16x16x32_bf16 v[20:23], a[108:111], v[204:207], v[20:23]\n"
"s_waitcnt lgkmcnt(8)\n"
"v_exp_f32 v225, v225\n"
"v_mfma_f32_16x16x32_bf16 v[20:23], a[104:107], v[200:203], v[20:23]\n"
"s_waitcnt lgkmcnt(4)\n"
"v_perm_b32 v212, v225, v224, s72\n"
"v_mfma_f32_16x16x32_bf16 v[24:27], a[112:115], v[200:203], v[24:27]\n"
"s_add_u32 m0, s53, 52288\n"
"v_exp_f32 v226, v226\n"
"v_mfma_f32_16x16x32_bf16 v[24:27], a[116:119], v[204:207], v[24:27]\n"
"s_waitcnt lgkmcnt(2)\n"
"buffer_load_dwordx4 v247, s[36:39], s52 offen offset:1024 lds\n"
"v_mfma_f32_16x16x32_bf16 v[28:31], a[124:127], v[204:207], v[28:31]\n"
"s_waitcnt lgkmcnt(0)\n"
"v_exp_f32 v227, v227\n"
"v_mfma_f32_16x16x32_bf16 v[28:31], a[120:123], v[200:203], v[28:31]\n"
"v_perm_b32 v213, v227, v226, s72\n"
"v_mfma_f32_16x16x32_bf16 v[192:195], a[0:3], v[160:163], -4.0\n"
"s_add_u32 m0, s53, 52352\n"
"v_exp_f32 v228, v228\n"
"v_mfma_f32_16x16x32_bf16 v[192:195], a[4:7], v[164:167], v[192:195]\n"
"buffer_load_dwordx4 v247, s[36:39], s52 offen offset:2048 lds\n"
"v_mfma_f32_16x16x32_bf16 v[192:195], a[8:11], v[168:171], v[192:195]\n"
"v_exp_f32 v229, v229\n"
"v_mfma_f32_16x16x32_bf16 v[192:195], a[12:15], v[172:175], v[192:195]\n"
"v_perm_b32 v214, v229, v228, s72\n"
"v_mfma_f32_16x16x32_bf16 v[196:199], a[28:31], v[172:175], -4.0\n"
"s_add_u32 m0, s53, 52416\n"
"v_exp_f32 v230, v230\n"
"v_mfma_f32_16x16x32_bf16 v[196:199], a[24:27], v[168:171], v[196:199]\n"
"buffer_load_dwordx4 v247, s[36:39], s52 offen offset:3072 lds\n"
"v_mfma_f32_16x16x32_bf16 v[196:199], a[20:23], v[164:167], v[196:199]\n"
"v_exp_f32 v231, v231\n"
"v_mfma_f32_16x16x32_bf16 v[196:199], a[16:19], v[160:163], v[196:199]\n"
"v_perm_b32 v215, v231, v230, s72\n"
"v_mfma_f32_16x16x32_bf16 v[224:227], a[32:35], v[160:163], -4.0\n"
"v_exp_f32 v192, v192\n"
"v_mfma_f32_16x16x32_bf16 v[224:227], a[36:39], v[164:167], v[224:227]\n"
"v_mfma_f32_16x16x32_bf16 v[224:227], a[40:43], v[168:171], v[224:227]\n"
"v_exp_f32 v193, v193\n"
"v_mfma_f32_16x16x32_bf16 v[224:227], a[44:47], v[172:175], v[224:227]\n"
"v_perm_b32 v200, v193, v192, s72\n"
"v_mfma_f32_16x16x32_bf16 v[228:231], a[60:63], v[172:175], -4.0\n"
"s_add_u32 m0, s54, 49536\n"
"v_exp_f32 v194, v194\n"
"v_mfma_f32_16x16x32_bf16 v[228:231], a[56:59], v[168:171], v[228:231]\n"
"buffer_load_dwordx4 v248, s[40:43], s52 offen offset:0 lds\n"
"v_mfma_f32_16x16x32_bf16 v[228:231], a[52:55], v[164:167], v[228:231]\n"
"v_exp_f32 v195, v195\n"
"v_mfma_f32_16x16x32_bf16 v[228:231], a[48:51], v[160:163], v[228:231]\n"
"s_waitcnt vmcnt(9) lgkmcnt(0)\n"
"v_perm_b32 v201, v195, v194, s72\n"
"v_smfmac_f32_16x16x64_bf16 a[132:135], a[144:147], v[208:215], v251\n"
"v_mfma_f32_16x16x32_bf16 v[32:35], a[64:67], v[208:211], v[32:35]\n"
"v_exp_f32 v196, v196\n"
"v_mfma_f32_16x16x32_bf16 v[32:35], a[68:71], v[212:215], v[32:35]\n"
"s_barrier\n"
"ds_read_b128 a[0:3], v244 offset:34816\n"
"ds_read_b128 a[4:7], v244 offset:34880\n"
"v_mfma_f32_16x16x32_bf16 v[36:39], a[76:79], v[212:215], v[36:39]\n"
"v_exp_f32 v197, v197\n"
"v_mfma_f32_16x16x32_bf16 v[36:39], a[72:75], v[208:211], v[36:39]\n"
"ds_read_b128 a[8:11], v244 offset:34944\n"
"v_perm_b32 v202, v197, v196, s72\n"
"v_mfma_f32_16x16x32_bf16 v[40:43], a[80:83], v[208:211], v[40:43]\n"
"s_add_u32 m0, s54, 49544\n"
"v_exp_f32 v198, v198\n"
"v_mfma_f32_16x16x32_bf16 v[40:43], a[84:87], v[212:215], v[40:43]\n"
"buffer_load_dwordx4 v248, s[40:43], s52 offen offset:1024 lds\n"
"v_mfma_f32_16x16x32_bf16 v[44:47], a[92:95], v[212:215], v[44:47]\n"
"v_exp_f32 v199, v199\n"
"v_mfma_f32_16x16x32_bf16 v[44:47], a[88:91], v[208:211], v[44:47]\n"
"ds_read_b128 a[12:15], v244 offset:35008\n"
"v_perm_b32 v203, v199, v198, s72\n"
"v_mfma_f32_16x16x32_bf16 v[48:51], a[96:99], v[208:211], v[48:51]\n"
"v_exp_f32 v224, v224\n"
"v_mfma_f32_16x16x32_bf16 v[48:51], a[100:103], v[212:215], v[48:51]\n"
"ds_read_b128 a[28:31], v244 offset:39360\n"
"ds_read_b128 a[24:27], v244 offset:39296\n"
"v_mfma_f32_16x16x32_bf16 v[52:55], a[108:111], v[212:215], v[52:55]\n"
"v_exp_f32 v225, v225\n"
"v_mfma_f32_16x16x32_bf16 v[52:55], a[104:107], v[208:211], v[52:55]\n"
"ds_read_b128 a[20:23], v244 offset:39232\n"
"v_perm_b32 v204, v225, v224, s72\n"
"v_mfma_f32_16x16x32_bf16 v[56:59], a[112:115], v[208:211], v[56:59]\n"
"s_add_u32 m0, s54, 51472\n"
"v_exp_f32 v226, v226\n"
"v_mfma_f32_16x16x32_bf16 v[56:59], a[116:119], v[212:215], v[56:59]\n"
"buffer_load_dwordx4 v248, s[40:43], s52 offen offset:128 lds\n"
"v_mfma_f32_16x16x32_bf16 v[60:63], a[124:127], v[212:215], v[60:63]\n"
"v_exp_f32 v227, v227\n"
"v_mfma_f32_16x16x32_bf16 v[60:63], a[120:123], v[208:211], v[60:63]\n"
"ds_read_b128 a[16:19], v244 offset:39168\n"
"v_perm_b32 v205, v227, v226, s72\n"
"v_mfma_f32_16x16x32_bf16 v[192:195], a[32:35], v[176:179], -4.0\n"
"v_exp_f32 v228, v228\n"
"v_mfma_f32_16x16x32_bf16 v[192:195], a[36:39], v[180:183], v[192:195]\n"
"ds_read_b128 a[32:35], v222 offset:34816\n"
"ds_read_b128 a[36:39], v222 offset:34880\n"
"v_mfma_f32_16x16x32_bf16 v[192:195], a[40:43], v[184:187], v[192:195]\n"
"v_exp_f32 v229, v229\n"
"v_mfma_f32_16x16x32_bf16 v[192:195], a[44:47], v[188:191], v[192:195]\n"
"ds_read_b128 a[40:43], v222 offset:34944\n"
"v_perm_b32 v206, v229, v228, s72\n"
"v_mfma_f32_16x16x32_bf16 v[224:227], a[60:63], v[188:191], -4.0\n"
"s_add_u32 m0, s54, 51480\n"
"v_exp_f32 v230, v230\n"
"v_mfma_f32_16x16x32_bf16 v[224:227], a[56:59], v[184:187], v[224:227]\n"
"buffer_load_dwordx4 v248, s[40:43], s52 offen offset:1152 lds\n"
"v_mfma_f32_16x16x32_bf16 v[224:227], a[52:55], v[180:183], v[224:227]\n"
"s_add_u32 s52, s52, 16384\n"
"v_exp_f32 v231, v231\n"
"v_mfma_f32_16x16x32_bf16 v[224:227], a[48:51], v[176:179], v[224:227]\n"
"ds_read_b128 a[44:47], v222 offset:35008\n"
"v_perm_b32 v207, v231, v230, s72\n"
"v_mfma_f32_16x16x32_bf16 v[64:67], a[64:67], v[200:203], v[64:67]\n"
"v_exp_f32 v192, v192\n"
"v_mfma_f32_16x16x32_bf16 v[64:67], a[68:71], v[204:207], v[64:67]\n"
"ds_read_b128 a[60:63], v222 offset:39360\n"
"ds_read_b128 a[56:59], v222 offset:39296\n"
"v_mfma_f32_16x16x32_bf16 v[68:71], a[76:79], v[204:207], v[68:71]\n"
"v_exp_f32 v193, v193\n"
"v_mfma_f32_16x16x32_bf16 v[68:71], a[72:75], v[200:203], v[68:71]\n"
"ds_read_b128 a[52:55], v222 offset:39232\n"
"v_perm_b32 v212, v193, v192, s72\n"
"v_mfma_f32_16x16x32_bf16 v[72:75], a[80:83], v[200:203], v[72:75]\n"
"s_add_u32 m0, s53, 0\n"
"v_exp_f32 v194, v194\n"
"v_mfma_f32_16x16x32_bf16 v[72:75], a[84:87], v[204:207], v[72:75]\n"
"buffer_load_dwordx4 v247, s[36:39], s52 offen offset:0 lds\n"
"v_mfma_f32_16x16x32_bf16 v[76:79], a[92:95], v[204:207], v[76:79]\n"
"v_exp_f32 v195, v195\n"
"v_mfma_f32_16x16x32_bf16 v[76:79], a[88:91], v[200:203], v[76:79]\n"
"ds_read_b128 a[48:51], v222 offset:39168\n"
"v_perm_b32 v213, v195, v194, s72\n"
"v_smfmac_f32_16x16x64_bf16 a[136:139], a[144:147], v[200:207], v251\n"
"s_waitcnt lgkmcnt(5)\n"
"ds_read_b64_tr_b16 a[64:65], v245 offset:33024\n"
"v_mfma_f32_16x16x32_bf16 v[192:195], a[0:3], v[128:131], -4.0\n"
"Lhop0_3_%=:\n s_cbranch_execz Lhop0_7_%=\n"
"v_exp_f32 v224, v224\n"
"v_mfma_f32_16x16x32_bf16 v[192:195], a[4:7], v[132:135], v[192:195]\n"
"ds_read_b64_tr_b16 a[66:67], v245 offset:37152\n"
"ds_read_b64_tr_b16 a[72:73], v245 offset:33032\n"
"v_mfma_f32_16x16x32_bf16 v[192:195], a[8:11], v[136:139], v[192:195]\n"
"v_exp_f32 v225, v225\n"
"v_mfma_f32_16x16x32_bf16 v[192:195], a[12:15], v[140:143], v[192:195]\n"
"ds_read_b64_tr_b16 a[74:75], v245 offset:37160\n"
"v_perm_b32 v214, v225, v224, s72\n"
"v_mfma_f32_16x16x32_bf16 v[196:199], a[28:31], v[140:143], -4.0\n"
"s_add_u32 m0, s53, 64\n"
"v_exp_f32 v226, v226\n"
"v_mfma_f32_16x16x32_bf16 v[196:199], a[24:27], v[136:139], v[196:199]\n"
"buffer_load_dwordx4 v247, s[36:39], s52 offen offset:1024 lds\n"
"v_mfma_f32_16x16x32_bf16 v[196:199], a[20:23], v[132:135], v[196:199]\n"
"v_exp_f32 v227, v227\n"
"v_mfma_f32_16x16x32_bf16 v[196:199], a[16:19], v[128:131], v[196:199]\n"
"ds_read_b64_tr_b16 a[80:81], v245 offset:33280\n"
"v_perm_b32 v215, v227, v226, s72\n"
"v_mfma_f32_16x16x32_bf16 v[80:83], a[96:99], v[200:203], v[80:83]\n"
"v_exp_f32 v192, v192\n"
"v_mfma_f32_16x16x32_bf16 v[80:83], a[100:103], v[204:207], v[80:83]\n"
"ds_read_b64_tr_b16 a[82:83], v245 offset:37408\n"
"ds_read_b64_tr_b16 a[88:89], v245 offset:33288\n"
"v_mfma_f32_16x16x32_bf16 v[84:87], a[108:111], v[204:207], v[84:87]\n"
"v_exp_f32 v193, v193\n"
"v_mfma_f32_16x16x32_bf16 v[84:87], a[104:107], v[200:203], v[84:87]\n"
"ds_read_b64_tr_b16 a[90:91], v245 offset:37416\n"
"ds_read_b64_tr_b16 a[96:97], v245 offset:35088\n"
"v_mfma_f32_16x16x32_bf16 v[88:91], a[112:115], v[200:203], v[88:91]\n"
"s_add_u32 m0, s53, 128\n"
"v_exp_f32 v194, v194\n"
"v_mfma_f32_16x16x32_bf16 v[88:91], a[116:119], v[204:207], v[88:91]\n"
"buffer_load_dwordx4 v247, s[36:39], s52 offen offset:2048 lds\n"
"v_mfma_f32_16x16x32_bf16 v[92:95], a[124:127], v[204:207], v[92:95]\n"
"v_exp_f32 v195, v195\n"
"v_mfma_f32_16x16x32_bf16 v[92:95], a[120:123], v[200:203], v[92:95]\n"
"v_perm_b32 v200, v193, v192, s72\n"
"v_perm_b32 v201, v195, v194, s72\n"
"v_mfma_f32_16x16x32_bf16 v[224:227], a[32:35], v[128:131], -4.0\n"
"v_exp_f32 v196, v196\n"
"v_mfma_f32_16x16x32_bf16 v[224:227], a[36:39], v[132:135], v[224:227]\n"
"ds_read_b64_tr_b16 a[98:99], v245 offset:39216\n"
"ds_read_b64_tr_b16 a[104:105], v245 offset:35096\n"
"v_mfma_f32_16x16x32_bf16 v[224:227], a[40:43], v[136:139], v[224:227]\n"
"s_waitcnt lgkmcnt(6)\n"
"v_exp_f32 v197, v197\n"
"v_mfma_f32_16x16x32_bf16 v[224:227], a[44:47], v[140:143], v[224:227]\n"
"ds_read_b64_tr_b16 a[106:107], v245 offset:39224\n"
"v_perm_b32 v202, v197, v196, s72\n"
"v_mfma_f32_16x16x32_bf16 v[228:231], a[60:63], v[140:143], -4.0\n"
"s_add_u32 m0, s53, 192\n"
"v_exp_f32 v198, v198\n"
"v_mfma_f32_16x16x32_bf16 v[228:231], a[56:59], v[136:139], v[228:231]\n"
"buffer_load_dwordx4 v247, s[36:39], s52 offen offset:3072 lds\n"
"v_mfma_f32_16x16x32_bf16 v[228:231], a[52:55], v[132:135], v[228:231]\n"
"v_exp_f32 v199, v199\n"
"v_mfma_f32_16x16x32_bf16 v[228:231], a[48:51], v[128:131], v[228:231]\n"
"ds_read_b64_tr_b16 a[112:113], v245 offset:35344\n"
"v_perm_b32 v203, v199, v198, s72\n"
"v_mfma_f32_16x16x32_bf16 a[140:143], a[144:147], v[212:215], a[140:143]\n"
"ds_read_b64_tr_b16 a[114:115], v245 offset:39472\n"
"ds_read_b64_tr_b16 a[120:121], v245 offset:35352\n"
"v_mfma_f32_16x16x32_bf16 v[96:99], a[68:71], v[212:215], v[96:99]\n"
"v_exp_f32 v224, v224\n"
"v_mfma_f32_16x16x32_bf16 v[100:103], a[76:79], v[212:215], v[100:103]\n"
"ds_read_b64_tr_b16 a[68:69], v255 offset:33024\n"
"ds_read_b64_tr_b16 a[70:71], v255 offset:37152\n"
"v_mfma_f32_16x16x32_bf16 v[104:107], a[84:87], v[212:215], v[104:107]\n"
"v_exp_f32 v225, v225\n"
"v_mfma_f32_16x16x32_bf16 v[108:111], a[92:95], v[212:215], v[108:111]\n"
"ds_read_b64_tr_b16 a[76:77], v255 offset:33032\n"
"v_perm_b32 v204, v225, v224, s72\n"
"v_mfma_f32_16x16x32_bf16 v[112:115], a[100:103], v[212:215], v[112:115]\n"
"s_add_u32 m0, s54, 0\n"
"v_exp_f32 v226, v226\n"
"v_mfma_f32_16x16x32_bf16 v[116:119], a[108:111], v[212:215], v[116:119]\n"
"buffer_load_dwordx4 v248, s[40:43], s52 offen offset:0 lds\n"
"v_mfma_f32_16x16x32_bf16 v[120:123], a[116:119], v[212:215], v[120:123]\n"
"v_exp_f32 v227, v227\n"
"v_mfma_f32_16x16x32_bf16 v[124:127], a[124:127], v[212:215], v[124:127]\n"
"ds_read_b64_tr_b16 a[78:79], v255 offset:37160\n"
"v_perm_b32 v205, v227, v226, s72\n"
"v_mfma_f32_16x16x32_bf16 v[192:195], a[0:3], v[144:147], -4.0\n"
"v_exp_f32 v228, v228\n"
"v_mfma_f32_16x16x32_bf16 v[192:195], a[4:7], v[148:151], v[192:195]\n"
"ds_read_b64_tr_b16 a[84:85], v255 offset:33280\n"
"ds_read_b64_tr_b16 a[86:87], v255 offset:37408\n"
"v_mfma_f32_16x16x32_bf16 v[192:195], a[8:11], v[152:155], v[192:195]\n"
"v_exp_f32 v229, v229\n"
"v_mfma_f32_16x16x32_bf16 v[192:195], a[12:15], v[156:159], v[192:195]\n"
"ds_read_b64_tr_b16 a[92:93], v255 offset:33288\n"
"v_perm_b32 v206, v229, v228, s72\n"
"v_mfma_f32_16x16x32_bf16 v[196:199], a[28:31], v[156:159], -4.0\n"
"s_add_u32 m0, s54, 8\n"
"v_exp_f32 v230, v230\n"
"v_mfma_f32_16x16x32_bf16 v[196:199], a[24:27], v[152:155], v[196:199]\n"
"buffer_load_dwordx4 v248, s[40:43], s52 offen offset:1024 lds\n"
"v_mfma_f32_16x16x32_bf16 v[196:199], a[20:23], v[148:151], v[196:199]\n"
"v_exp_f32 v231, v231\n"
"v_mfma_f32_16x16x32_bf16 v[196:199], a[16:19], v[144:147], v[196:199]\n"
"ds_read_b64_tr_b16 a[94:95], v255 offset:37416\n"
"v_perm_b32 v207, v231, v230, s72\n"
"v_mfma_f32_16x16x32_bf16 v[224:227], a[32:35], v[144:147], -4.0\n"
"v_exp_f32 v192, v192\n"
"v_mfma_f32_16x16x32_bf16 v[224:227], a[36:39], v[148:151], v[224:227]\n"
"ds_read_b64_tr_b16 a[100:101], v255 offset:35088\n"
"ds_read_b64_tr_b16 a[102:103], v255 offset:39216\n"
"v_mfma_f32_16x16x32_bf16 v[224:227], a[40:43], v[152:155], v[224:227]\n"
"v_exp_f32 v193, v193\n"
"v_mfma_f32_16x16x32_bf16 v[224:227], a[44:47], v[156:159], v[224:227]\n"
"ds_read_b64_tr_b16 a[108:109], v255 offset:35096\n"
"v_perm_b32 v208, v193, v192, s72\n"
"v_mfma_f32_16x16x32_bf16 v[228:231], a[60:63], v[156:159], -4.0\n"
"s_add_u32 m0, s54, 1936\n"
"v_exp_f32 v194, v194\n"
"v_mfma_f32_16x16x32_bf16 v[228:231], a[56:59], v[152:155], v[228:231]\n"
"buffer_load_dwordx4 v248, s[40:43], s52 offen offset:128 lds\n"
"v_mfma_f32_16x16x32_bf16 v[228:231], a[52:55], v[148:151], v[228:231]\n"
"v_exp_f32 v195, v195\n"
"v_mfma_f32_16x16x32_bf16 v[228:231], a[48:51], v[144:147], v[228:231]\n"
"ds_read_b64_tr_b16 a[110:111], v255 offset:39224\n"
"v_perm_b32 v209, v195, v194, s72\n"
"v_smfmac_f32_16x16x64_bf16 a[128:131], a[144:147], v[200:207], v251\n"
"ds_read_b64_tr_b16 a[116:117], v255 offset:35344\n"
"v_mfma_f32_16x16x32_bf16 v[0:3], a[64:67], v[200:203], v[0:3]\n"
"s_waitcnt lgkmcnt(6)\n"
"v_exp_f32 v196, v196\n"
"v_mfma_f32_16x16x32_bf16 v[0:3], a[68:71], v[204:207], v[0:3]\n"
"ds_read_b64_tr_b16 a[118:119], v255 offset:39472\n"
"ds_read_b64_tr_b16 a[124:125], v255 offset:35352\n"
"v_mfma_f32_16x16x32_bf16 v[4:7], a[76:79], v[204:207], v[4:7]\n"
"v_exp_f32 v197, v197\n"
"v_mfma_f32_16x16x32_bf16 v[4:7], a[72:75], v[200:203], v[4:7]\n"
"ds_read_b64_tr_b16 a[126:127], v255 offset:39480\n"
"v_perm_b32 v210, v197, v196, s72\n"
"v_mfma_f32_16x16x32_bf16 v[8:11], a[80:83], v[200:203], v[8:11]\n"
"s_add_u32 m0, s54, 1944\n"
"v_exp_f32 v198, v198\n"
"v_mfma_f32_16x16x32_bf16 v[8:11], a[84:87], v[204:207], v[8:11]\n"
"s_waitcnt lgkmcnt(5)\n"
"buffer_load_dwordx4 v248, s[40:43], s52 offen offset:1152 lds\n"
"v_mfma_f32_16x16x32_bf16 v[12:15], a[92:95], v[204:207], v[12:15]\n"
"s_add_u32 s52, s52, 16384\n"
"v_exp_f32 v199, v199\n"
"v_mfma_f32_16x16x32_bf16 v[12:15], a[88:91], v[200:203], v[12:15]\n"
"s_waitcnt vmcnt(16)\n"
"ds_read_b64_tr_b16 a[122:123], v245 offset:39480\n"
"v_perm_b32 v211, v199, v198, s72\n"
"v_mfma_f32_16x16x32_bf16 v[16:19], a[96:99], v[200:203], v[16:19]\n"
"v_exp_f32 v224, v224\n"
"v_mfma_f32_16x16x32_bf16 v[16:19], a[100:103], v[204:207], v[16:19]\n"
"s_waitcnt lgkmcnt(5)\n"
"ds_read2_b64 a[152:155], v246 offset0:0 offset1:2\n"
"ds_read2_b64 a[156:159], v246 offset0:1 offset1:3\n"
"v_mfma_f32_16x16x32_bf16 v[20:23], a[108:111], v[204:207], v[20:23]\n"
"v_exp_f32 v225, v225\n"
"v_mfma_f32_16x16x32_bf16 v[20:23], a[104:107], v[200:203], v[20:23]\n"
"s_waitcnt lgkmcnt(5)\n"
"ds_read2_b64 a[160:163], v246 offset0:32 offset1:34\n"
"v_perm_b32 v212, v225, v224, s72\n"
"v_mfma_f32_16x16x32_bf16 v[24:27], a[112:115], v[200:203], v[24:27]\n"
"s_add_u32 m0, s53, 17408\n"
"v_exp_f32 v226, v226\n"
"v_mfma_f32_16x16x32_bf16 v[24:27], a[116:119], v[204:207], v[24:27]\n"
"s_waitcnt lgkmcnt(4)\n"
"buffer_load_dwordx4 v247, s[36:39], s52 offen offset:0 lds\n"
"v_mfma_f32_16x16x32_bf16 v[28:31], a[124:127], v[204:207], v[28:31]\n"
"s_waitcnt lgkmcnt(3)\n"
"v_exp_f32 v227, v227\n"
"v_mfma_f32_16x16x32_bf16 v[28:31], a[120:123], v[200:203], v[28:31]\n"
"ds_read2_b64 a[164:167], v246 offset0:33 offset1:35\n"
"v_perm_b32 v213, v227, v226, s72\n"
"v_mfma_f32_16x16x32_bf16 v[192:195], a[0:3], v[160:163], -4.0\n"
"v_exp_f32 v228, v228\n"
"v_mfma_f32_16x16x32_bf16 v[192:195], a[4:7], v[164:167], v[192:195]\n"
"ds_read2_b64 a[168:171], v246 offset0:64 offset1:66\n"
"ds_read2_b64 a[172:175], v246 offset0:65 offset1:67\n"
"v_mfma_f32_16x16x32_bf16 v[192:195], a[8:11], v[168:171], v[192:195]\n"
"v_exp_f32 v229, v229\n"
"v_mfma_f32_16x16x32_bf16 v[192:195], a[12:15], v[172:175], v[192:195]\n"
"ds_read2_b64 a[176:179], v246 offset0:96 offset1:98\n"
"v_perm_b32 v214, v229, v228, s72\n"
"v_mfma_f32_16x16x32_bf16 v[196:199], a[28:31], v[172:175], -4.0\n"
"s_add_u32 m0, s53, 17472\n"
"v_exp_f32 v230, v230\n"
"v_mfma_f32_16x16x32_bf16 v[196:199], a[24:27], v[168:171], v[196:199]\n"
"buffer_load_dwordx4 v247, s[36:39], s52 offen offset:1024 lds\n"
"v_mfma_f32_16x16x32_bf16 v[196:199], a[20:23], v[164:167], v[196:199]\n"
"v_exp_f32 v231, v231\n"
"v_mfma_f32_16x16x32_bf16 v[196:199], a[16:19], v[160:163], v[196:199]\n"
"ds_read2_b64 a[180:183], v246 offset0:97 offset1:99\n"
"v_perm_b32 v215, v231, v230, s72\n"
"v_mfma_f32_16x16x32_bf16 v[224:227], a[32:35], v[160:163], -4.0\n"
"s_add_u32 m0, s53, 17536\n"
"v_exp_f32 v192, v192\n"
"v_mfma_f32_16x16x32_bf16 v[224:227], a[36:39], v[164:167], v[224:227]\n"
"buffer_load_dwordx4 v247, s[36:39], s52 offen offset:2048 lds\n"
"v_mfma_f32_16x16x32_bf16 v[224:227], a[40:43], v[168:171], v[224:227]\n"
"v_exp_f32 v193, v193\n"
"v_mfma_f32_16x16x32_bf16 v[224:227], a[44:47], v[172:175], v[224:227]\n"
"v_perm_b32 v200, v193, v192, s72\n"
"v_mfma_f32_16x16x32_bf16 v[228:231], a[60:63], v[172:175], -4.0\n"
"s_add_u32 m0, s53, 17600\n"
"v_exp_f32 v194, v194\n"
"v_mfma_f32_16x16x32_bf16 v[228:231], a[56:59], v[168:171], v[228:231]\n"
"buffer_load_dwordx4 v247, s[36:39], s52 offen offset:3072 lds\n"
"v_mfma_f32_16x16x32_bf16 v[228:231], a[52:55], v[164:167], v[228:231]\n"
"v_exp_f32 v195, v195\n"
"v_mfma_f32_16x16x32_bf16 v[228:231], a[48:51], v[160:163], v[228:231]\n"
"s_waitcnt vmcnt(12) lgkmcnt(8)\n"
"v_perm_b32 v201, v195, v194, s72\n"
"v_smfmac_f32_16x16x64_bf16 a[132:135], a[144:147], v[208:215], v251\n"
"v_mfma_f32_16x16x32_bf16 v[32:35], a[64:67], v[208:211], v[32:35]\n"
"v_exp_f32 v196, v196\n"
"v_mfma_f32_16x16x32_bf16 v[32:35], a[68:71], v[212:215], v[32:35]\n"
"s_barrier\n"
"ds_read_b128 a[0:3], v244 offset:52224\n"
"ds_read_b128 a[4:7], v244 offset:52288\n"
"v_mfma_f32_16x16x32_bf16 v[36:39], a[76:79], v[212:215], v[36:39]\n"
"v_exp_f32 v197, v197\n"
"v_mfma_f32_16x16x32_bf16 v[36:39], a[72:75], v[208:211], v[36:39]\n"
"ds_read_b128 a[8:11], v244 offset:52352\n"
"v_perm_b32 v202, v197, v196, s72\n"
"v_mfma_f32_16x16x32_bf16 v[40:43], a[80:83], v[208:211], v[40:43]\n"
"s_add_u32 m0, s54, 16512\n"
"v_exp_f32 v198, v198\n"
"v_mfma_f32_16x16x32_bf16 v[40:43], a[84:87], v[212:215], v[40:43]\n"
"buffer_load_dwordx4 v248, s[40:43], s52 offen offset:0 lds\n"
"v_mfma_f32_16x16x32_bf16 v[44:47], a[92:95], v[212:215], v[44:47]\n"
"v_exp_f32 v199, v199\n"
"v_mfma_f32_16x16x32_bf16 v[44:47], a[88:91], v[208:211], v[44:47]\n"
"ds_read_b128 a[12:15], v244 offset:52416\n"
"v_perm_b32 v203, v199, v198, s72\n"
"v_mfma_f32_16x16x32_bf16 v[48:51], a[96:99], v[208:211], v[48:51]\n"
"v_exp_f32 v224, v224\n"
"v_mfma_f32_16x16x32_bf16 v[48:51], a[100:103], v[212:215], v[48:51]\n"
"ds_read_b128 a[28:31], v244 offset:56768\n"
"ds_read_b128 a[24:27], v244 offset:56704\n"
"v_mfma_f32_16x16x32_bf16 v[52:55], a[108:111], v[212:215], v[52:55]\n"
"v_exp_f32 v225, v225\n"
"v_mfma_f32_16x16x32_bf16 v[52:55], a[104:107], v[208:211], v[52:55]\n"
"ds_read_b128 a[20:23], v244 offset:56640\n"
"v_perm_b32 v204, v225, v224, s72\n"
"v_mfma_f32_16x16x32_bf16 v[56:59], a[112:115], v[208:211], v[56:59]\n"
"s_add_u32 m0, s55, 0\n"
"v_exp_f32 v226, v226\n"
"v_mfma_f32_16x16x32_bf16 v[56:59], a[116:119], v[212:215], v[56:59]\n"
"s_waitcnt lgkmcnt(7)\n"
"buffer_load_dwordx4 v249, s[44:47], s76 offen offset:0 nt lds\n"
"v_mfma_f32_16x16x32_bf16 v[60:63], a[124:127], v[212:215], v[60:63]\n"
"v_exp_f32 v227, v227\n"
"v_mfma_f32_16x16x32_bf16 v[60:63], a[120:123], v[208:211], v[60:63]\n"
"ds_read_b128 a[16:19], v244 offset:56576\n"
"v_perm_b32 v205, v227, v226, s72\n"
"s_waitcnt lgkmcnt(0)\n"
"s_branch LBS%=\n"
"LAS%=:\n"
"v_mfma_f32_16x16x32_bf16 v[192:195], a[32:35], v[176:179], -4.0\n"
"Lhop2_0_%=:\n s_cbranch_execz Lhop2_4_%=\n"
"v_exp_f32 v228, v228\n"
"v_mfma_f32_16x16x32_bf16 v[192:195], a[36:39], v[180:183], v[192:195]\n"
"ds_read_b128 a[0:3], v244 offset:17408\n"
"ds_read_b128 a[4:7], v244 offset:17472\n"
"v_mfma_f32_16x16x32_bf16 v[192:195], a[40:43], v[184:187], v[192:195]\n"
"v_exp_f32 v229, v229\n"
"v_mfma_f32_16x16x32_bf16 v[192:195], a[44:47], v[188:191], v[192:195]\n"
"ds_read_b128 a[8:11], v244 offset:17536\n"
"v_perm_b32 v206, v229, v228, s72\n"
"v_mfma_f32_16x16x32_bf16 v[224:227], a[60:63], v[188:191], -4.0\n"
"s_add_u32 m0, s55, 0\n"
"v_exp_f32 v230, v230\n"
"v_mfma_f32_16x16x32_bf16 v[224:227], a[56:59], v[184:187], v[224:227]\n"
"s_waitcnt lgkmcnt(14)\n"
"buffer_load_dwordx4 v249, s[44:47], s76 offen offset:0 nt lds\n"
"v_mfma_f32_16x16x32_bf16 v[224:227], a[52:55], v[180:183], v[224:227]\n"
"v_exp_f32 v231, v231\n"
"v_mfma_f32_16x16x32_bf16 v[224:227], a[48:51], v[176:179], v[224:227]\n"
"ds_read_b128 a[12:15], v244 offset:17600\n"
"v_perm_b32 v207, v231, v230, s72\n"
"v_mfma_f32_16x16x32_bf16 v[64:67], a[64:67], v[200:203], v[218:221]\n"
"v_exp_f32 v192, v192\n"
"v_mfma_f32_16x16x32_bf16 v[64:67], a[68:71], v[204:207], v[64:67]\n"
"ds_read_b128 a[28:31], v244 offset:21952\n"
"ds_read_b128 a[24:27], v244 offset:21888\n"
"v_mfma_f32_16x16x32_bf16 v[68:71], a[76:79], v[204:207], v[218:221]\n"
"v_exp_f32 v193, v193\n"
"v_mfma_f32_16x16x32_bf16 v[68:71], a[72:75], v[200:203], v[68:71]\n"
"ds_read_b128 a[20:23], v244 offset:21824\n"
"v_perm_b32 v212, v193, v192, s72\n"
"v_mfma_f32_16x16x32_bf16 v[72:75], a[80:83], v[200:203], v[218:221]\n"
"s_add_u32 m0, s55, 8\n"
"v_exp_f32 v194, v194\n"
"v_mfma_f32_16x16x32_bf16 v[72:75], a[84:87], v[204:207], v[72:75]\n"
"buffer_load_dwordx4 v249, s[44:47], s76 offen offset:1024 nt lds\n"
"v_mfma_f32_16x16x32_bf16 v[76:79], a[92:95], v[204:207], v[218:221]\n"
"v_exp_f32 v195, v195\n"
"v_mfma_f32_16x16x32_bf16 v[76:79], a[88:91], v[200:203], v[76:79]\n"
"ds_read_b128 a[16:19], v244 offset:21760\n"
"v_perm_b32 v213, v195, v194, s72\n"
"v_smfmac_f32_16x16x64_bf16 a[136:139], a[144:147], v[200:207], v251\n"
"s_waitcnt lgkmcnt(5)\n"
"ds_read_b128 a[32:35], v222 offset:17408\n"
"v_mfma_f32_16x16x32_bf16 v[192:195], a[0:3], v[128:131], -4.0\n"
"v_exp_f32 v224, v224\n"
"v_mfma_f32_16x16x32_bf16 v[192:195], a[4:7], v[132:135], v[192:195]\n"
"ds_read_b128 a[36:39], v222 offset:17472\n"
"ds_read_b128 a[40:43], v222 offset:17536\n"
"v_mfma_f32_16x16x32_bf16 v[192:195], a[8:11], v[136:139], v[192:195]\n"
"s_waitcnt lgkmcnt(7)\n"
"v_exp_f32 v225, v225\n"
"v_mfma_f32_16x16x32_bf16 v[192:195], a[12:15], v[140:143], v[192:195]\n"
"s_waitcnt lgkmcnt(5)\n"
"ds_read_b128 a[44:47], v222 offset:17600\n"
"v_perm_b32 v214, v225, v224, s72\n"
"v_mfma_f32_16x16x32_bf16 v[196:199], a[28:31], v[140:143], -4.0\n"
"s_add_u32 m0, s55, 16\n"
"v_exp_f32 v226, v226\n"
"v_mfma_f32_16x16x32_bf16 v[196:199], a[24:27], v[136:139], v[196:199]\n"
"s_waitcnt lgkmcnt(5)\n"
"buffer_load_dwordx4 v249, s[44:47], s76 offen offset:2048 nt lds\n"
"v_mfma_f32_16x16x32_bf16 v[196:199], a[20:23], v[132:135], v[196:199]\n"
"s_waitcnt lgkmcnt(4)\n"
"v_exp_f32 v227, v227\n"
"v_mfma_f32_16x16x32_bf16 v[196:199], a[16:19], v[128:131], v[196:199]\n"
"ds_read_b128 a[60:63], v222 offset:21952\n"
"v_perm_b32 v215, v227, v226, s72\n"
"v_mfma_f32_16x16x32_bf16 v[80:83], a[96:99], v[200:203], v[218:221]\n"
"s_add_u32 m0, s55, 24\n"
"v_exp_f32 v192, v192\n"
"v_mfma_f32_16x16x32_bf16 v[80:83], a[100:103], v[204:207], v[80:83]\n"
"buffer_load_dwordx4 v249, s[44:47], s76 offen offset:3072 nt lds\n"
"v_mfma_f32_16x16x32_bf16 v[84:87], a[108:111], v[204:207], v[218:221]\n"
"s_add_u32 s76, s76, 4096\n"
"v_exp_f32 v193, v193\n"
"v_mfma_f32_16x16x32_bf16 v[84:87], a[104:107], v[200:203], v[84:87]\n"
"ds_read_b128 a[56:59], v222 offset:21888\n"
"ds_read_b128 a[52:55], v222 offset:21824\n"
"v_mfma_f32_16x16x32_bf16 v[88:91], a[112:115], v[200:203], v[218:221]\n"
"s_add_u32 m0, s54, 49544\n"
"v_exp_f32 v194, v194\n"
"v_mfma_f32_16x16x32_bf16 v[88:91], a[116:119], v[204:207], v[88:91]\n"
"buffer_load_dwordx4 v248, s[40:43], s52 offen offset:1024 lds\n"
"v_mfma_f32_16x16x32_bf16 v[92:95], a[124:127], v[204:207], v[218:221]\n"
"v_exp_f32 v195, v195\n"
"v_mfma_f32_16x16x32_bf16 v[92:95], a[120:123], v[200:203], v[92:95]\n"
"s_waitcnt lgkmcnt(3)\n"
"v_perm_b32 v200, v193, v192, s72\n"
"v_perm_b32 v201, v195, v194, s72\n"
"v_mfma_f32_16x16x32_bf16 v[224:227], a[32:35], v[128:131], -4.0\n"
"v_exp_f32 v196, v196\n"
"v_mfma_f32_16x16x32_bf16 v[224:227], a[36:39], v[132:135], v[224:227]\n"
"ds_read_b128 a[48:51], v222 offset:21760\n"
"ds_read_b64_tr_b16 a[64:65], v245 offset:16512\n"
"v_mfma_f32_16x16x32_bf16 v[224:227], a[40:43], v[136:139], v[224:227]\n"
"v_exp_f32 v197, v197\n"
"v_mfma_f32_16x16x32_bf16 v[224:227], a[44:47], v[140:143], v[224:227]\n"
"s_waitcnt lgkmcnt(3)\n"
"ds_read_b64_tr_b16 a[66:67], v245 offset:20640\n"
"v_perm_b32 v202, v197, v196, s72\n"
"v_mfma_f32_16x16x32_bf16 v[228:231], a[60:63], v[140:143], -4.0\n"
"s_add_u32 m0, s54, 51472\n"
"v_exp_f32 v198, v198\n"
"v_mfma_f32_16x16x32_bf16 v[228:231], a[56:59], v[136:139], v[228:231]\n"
"s_waitcnt lgkmcnt(3)\n"
"buffer_load_dwordx4 v248, s[40:43], s52 offen offset:128 lds\n"
"v_mfma_f32_16x16x32_bf16 v[228:231], a[52:55], v[132:135], v[228:231]\n"
"s_waitcnt lgkmcnt(2)\n"
"v_exp_f32 v199, v199\n"
"v_mfma_f32_16x16x32_bf16 v[228:231], a[48:51], v[128:131], v[228:231]\n"
"ds_read_b64_tr_b16 a[72:73], v245 offset:16520\n"
"v_perm_b32 v203, v199, v198, s72\n"
"v_mfma_f32_16x16x32_bf16 a[140:143], a[144:147], v[212:215], a[140:143]\n"
"ds_read_b64_tr_b16 a[74:75], v245 offset:20648\n"
"ds_read_b64_tr_b16 a[80:81], v245 offset:16768\n"
"v_mfma_f32_16x16x32_bf16 v[96:99], a[68:71], v[212:215], v[218:221]\n"
"v_exp_f32 v224, v224\n"
"v_mfma_f32_16x16x32_bf16 v[100:103], a[76:79], v[212:215], v[218:221]\n"
"ds_read_b64_tr_b16 a[68:69], v255 offset:16512\n"
"ds_read_b64_tr_b16 a[70:71], v255 offset:20640\n"
"v_mfma_f32_16x16x32_bf16 v[104:107], a[84:87], v[212:215], v[218:221]\n"
"v_exp_f32 v225, v225\n"
"v_mfma_f32_16x16x32_bf16 v[108:111], a[92:95], v[212:215], v[218:221]\n"
"ds_read_b64_tr_b16 a[76:77], v255 offset:16520\n"
"v_perm_b32 v204, v225, v224, s72\n"
"v_mfma_f32_16x16x32_bf16 v[112:115], a[100:103], v[212:215], v[218:221]\n"
"v_exp_f32 v226, v226\n"
"v_mfma_f32_16x16x32_bf16 v[116:119], a[108:111], v[212:215], v[218:221]\n"
"ds_read_b64_tr_b16 a[78:79], v255 offset:20648\n"
"ds_read_b64_tr_b16 a[82:83], v245 offset:20896\n"
"v_mfma_f32_16x16x32_bf16 v[120:123], a[116:119], v[212:215], v[218:221]\n"
"v_exp_f32 v227, v227\n"
"v_mfma_f32_16x16x32_bf16 v[124:127], a[124:127], v[212:215], v[218:221]\n"
"ds_read_b64_tr_b16 a[84:85], v255 offset:16768\n"
"v_perm_b32 v205, v227, v226, s72\n"
"v_mfma_f32_16x16x32_bf16 v[192:195], a[0:3], v[144:147], -4.0\n"
"v_exp_f32 v228, v228\n"
"v_mfma_f32_16x16x32_bf16 v[192:195], a[4:7], v[148:151], v[192:195]\n"
"ds_read_b64_tr_b16 a[86:87], v255 offset:20896\n"
"ds_read_b64_tr_b16 a[92:93], v255 offset:16776\n"
"v_mfma_f32_16x16x32_bf16 v[192:195], a[8:11], v[152:155], v[192:195]\n"
"v_exp_f32 v229, v229\n"
"v_mfma_f32_16x16x32_bf16 v[192:195], a[12:15], v[156:159], v[192:195]\n"
"ds_read_b64_tr_b16 a[94:95], v255 offset:20904\n"
"v_perm_b32 v206, v229, v228, s72\n"
"v_mfma_f32_16x16x32_bf16 v[196:199], a[28:31], v[156:159], -4.0\n"
"v_exp_f32 v230, v230\n"
"v_mfma_f32_16x16x32_bf16 v[196:199], a[24:27], v[152:155], v[196:199]\n"
"ds_read_b64_tr_b16 a[88:89], v245 offset:16776\n"
"ds_read_b64_tr_b16 a[90:91], v245 offset:20904\n"
"v_mfma_f32_16x16x32_bf16 v[196:199], a[20:23], v[148:151], v[196:199]\n"
"v_exp_f32 v231, v231\n"
"v_mfma_f32_16x16x32_bf16 v[196:199], a[16:19], v[144:147], v[196:199]\n"
"ds_read_b64_tr_b16 a[96:97], v245 offset:18576\n"
"v_perm_b32 v207, v231, v230, s72\n"
"v_mfma_f32_16x16x32_bf16 v[224:227], a[32:35], v[144:147], -4.0\n"
"v_exp_f32 v192, v192\n"
"v_mfma_f32_16x16x32_bf16 v[224:227], a[36:39], v[148:151], v[224:227]\n"
"ds_read_b64_tr_b16 a[98:99], v245 offset:22704\n"
"ds_read_b64_tr_b16 a[100:101], v255 offset:18576\n"
"v_mfma_f32_16x16x32_bf16 v[224:227], a[40:43], v[152:155], v[224:227]\n"
"v_exp_f32 v193, v193\n"
"v_mfma_f32_16x16x32_bf16 v[224:227], a[44:47], v[156:159], v[224:227]\n"
"ds_read_b64_tr_b16 a[102:103], v255 offset:22704\n"
"v_perm_b32 v208, v193, v192, s72\n"
"v_mfma_f32_16x16x32_bf16 v[228:231], a[60:63], v[156:159], -4.0\n"
"v_exp_f32 v194, v194\n"
"v_mfma_f32_16x16x32_bf16 v[228:231], a[56:59], v[152:155], v[228:231]\n"
"ds_read_b64_tr_b16 a[108:109], v255 offset:18584\n"
"ds_read_b64_tr_b16 a[110:111], v255 offset:22712\n"
"v_mfma_f32_16x16x32_bf16 v[228:231], a[52:55], v[148:151], v[228:231]\n"
"v_exp_f32 v195, v195\n"
"v_mfma_f32_16x16x32_bf16 v[228:231], a[48:51], v[144:147], v[228:231]\n"
"ds_read_b64_tr_b16 a[104:105], v245 offset:18584\n"
"v_perm_b32 v209, v195, v194, s72\n"
"v_smfmac_f32_16x16x64_bf16 a[128:131], a[144:147], v[200:207], v251\n"
"s_waitcnt lgkmcnt(7)\n"
"ds_read_b64_tr_b16 a[106:107], v245 offset:22712\n"
"v_mfma_f32_16x16x32_bf16 v[0:3], a[64:67], v[200:203], v[0:3]\n"
"v_exp_f32 v196, v196\n"
"v_mfma_f32_16x16x32_bf16 v[0:3], a[68:71], v[204:207], v[0:3]\n"
"ds_read_b64_tr_b16 a[112:113], v245 offset:18832\n"
"ds_read_b64_tr_b16 a[114:115], v245 offset:22960\n"
"v_mfma_f32_16x16x32_bf16 v[4:7], a[76:79], v[204:207], v[4:7]\n"
"v_exp_f32 v197, v197\n"
"v_mfma_f32_16x16x32_bf16 v[4:7], a[72:75], v[200:203], v[4:7]\n"
"ds_read_b64_tr_b16 a[116:117], v255 offset:18832\n"
"v_perm_b32 v210, v197, v196, s72\n"
"v_mfma_f32_16x16x32_bf16 v[8:11], a[80:83], v[200:203], v[8:11]\n"
"v_exp_f32 v198, v198\n"
"v_mfma_f32_16x16x32_bf16 v[8:11], a[84:87], v[204:207], v[8:11]\n"
"ds_read_b64_tr_b16 a[118:119], v255 offset:22960\n"
"ds_read_b64_tr_b16 a[124:125], v255 offset:18840\n"
"v_mfma_f32_16x16x32_bf16 v[12:15], a[92:95], v[204:207], v[12:15]\n"
"v_exp_f32 v199, v199\n"
"v_mfma_f32_16x16x32_bf16 v[12:15], a[88:91], v[200:203], v[12:15]\n"
"s_waitcnt lgkmcnt(7)\n"
"ds_read_b64_tr_b16 a[126:127], v255 offset:22968\n"
"v_perm_b32 v211, v199, v198, s72\n"
"v_mfma_f32_16x16x32_bf16 v[16:19], a[96:99], v[200:203], v[16:19]\n"
"v_exp_f32 v224, v224\n"
"v_mfma_f32_16x16x32_bf16 v[16:19], a[100:103], v[204:207], v[16:19]\n"
"ds_read_b64_tr_b16 a[120:121], v245 offset:18840\n"
"ds_read_b64_tr_b16 a[122:123], v245 offset:22968\n"
"v_mfma_f32_16x16x32_bf16 v[20:23], a[108:111], v[204:207], v[20:23]\n"
"s_waitcnt lgkmcnt(8)\n"
"v_exp_f32 v225, v225\n"
"v_mfma_f32_16x16x32_bf16 v[20:23], a[104:107], v[200:203], v[20:23]\n"
"s_waitcnt lgkmcnt(4)\n"
"v_perm_b32 v212, v225, v224, s72\n"
"v_mfma_f32_16x16x32_bf16 v[24:27], a[112:115], v[200:203], v[24:27]\n"
"s_add_u32 m0, s54, 51480\n"
"v_exp_f32 v226, v226\n"
"v_mfma_f32_16x16x32_bf16 v[24:27], a[116:119], v[204:207], v[24:27]\n"
"s_waitcnt lgkmcnt(0)\n"
"buffer_load_dwordx4 v248, s[40:43], s52 offen offset:1152 lds\n"
"v_mfma_f32_16x16x32_bf16 v[28:31], a[124:127], v[204:207], v[28:31]\n"
"s_add_u32 s52, s52, 16384\n"
"v_exp_f32 v227, v227\n"
"v_mfma_f32_16x16x32_bf16 v[28:31], a[120:123], v[200:203], v[28:31]\n"
"buffer_store_dwordx4 a[184:187], v250, s[48:51], s84 offen offset:0 sc1\n"
"v_perm_b32 v213, v227, v226, s72\n"
"v_mfma_f32_16x16x32_bf16 v[192:195], a[0:3], v[160:163], -4.0\n"
"v_exp_f32 v228, v228\n"
"v_mfma_f32_16x16x32_bf16 v[192:195], a[4:7], v[164:167], v[192:195]\n"
"v_mfma_f32_16x16x32_bf16 v[192:195], a[8:11], v[168:171], v[192:195]\n"
"v_exp_f32 v229, v229\n"
"v_mfma_f32_16x16x32_bf16 v[192:195], a[12:15], v[172:175], v[192:195]\n"
"v_perm_b32 v214, v229, v228, s72\n"
"v_mfma_f32_16x16x32_bf16 v[196:199], a[28:31], v[172:175], -4.0\n"
"s_add_u32 m0, s53, 0\n"
"v_exp_f32 v230, v230\n"
"v_mfma_f32_16x16x32_bf16 v[196:199], a[24:27], v[168:171], v[196:199]\n"
"buffer_load_dwordx4 v247, s[36:39], s52 offen offset:0 lds\n"
"v_mfma_f32_16x16x32_bf16 v[196:199], a[20:23], v[164:167], v[196:199]\n"
"v_exp_f32 v231, v231\n"
"v_mfma_f32_16x16x32_bf16 v[196:199], a[16:19], v[160:163], v[196:199]\n"
"v_perm_b32 v215, v231, v230, s72\n"
"v_mfma_f32_16x16x32_bf16 v[224:227], a[32:35], v[160:163], -4.0\n"
"v_exp_f32 v192, v192\n"
"v_mfma_f32_16x16x32_bf16 v[224:227], a[36:39], v[164:167], v[224:227]\n"
"v_mfma_f32_16x16x32_bf16 v[224:227], a[40:43], v[168:171], v[224:227]\n"
"v_exp_f32 v193, v193\n"
"v_mfma_f32_16x16x32_bf16 v[224:227], a[44:47], v[172:175], v[224:227]\n"
"v_perm_b32 v200, v193, v192, s72\n"
"v_mfma_f32_16x16x32_bf16 v[228:231], a[60:63], v[172:175], -4.0\n"
"s_add_u32 m0, s53, 64\n"
"v_exp_f32 v194, v194\n"
"v_mfma_f32_16x16x32_bf16 v[228:231], a[56:59], v[168:171], v[228:231]\n"
"buffer_load_dwordx4 v247, s[36:39], s52 offen offset:1024 lds\n"
"v_mfma_f32_16x16x32_bf16 v[228:231], a[52:55], v[164:167], v[228:231]\n"
"v_exp_f32 v195, v195\n"
"v_mfma_f32_16x16x32_bf16 v[228:231], a[48:51], v[160:163], v[228:231]\n"
"s_waitcnt vmcnt(14) lgkmcnt(0)\n"
"v_perm_b32 v201, v195, v194, s72\n"
"v_smfmac_f32_16x16x64_bf16 a[132:135], a[144:147], v[208:215], v251\n"
"v_mfma_f32_16x16x32_bf16 v[32:35], a[64:67], v[208:211], v[32:35]\n"
"v_exp_f32 v196, v196\n"
"v_mfma_f32_16x16x32_bf16 v[32:35], a[68:71], v[212:215], v[32:35]\n"
"s_barrier\n"
"ds_read_b128 a[0:3], v244 offset:34816\n"
"ds_read_b128 a[4:7], v244 offset:34880\n"
"v_mfma_f32_16x16x32_bf16 v[36:39], a[76:79], v[212:215], v[36:39]\n"
"v_exp_f32 v197, v197\n"
"v_mfma_f32_16x16x32_bf16 v[36:39], a[72:75], v[208:211], v[36:39]\n"
"ds_read_b128 a[8:11], v244 offset:34944\n"
"v_perm_b32 v202, v197, v196, s72\n"
"v_mfma_f32_16x16x32_bf16 v[40:43], a[80:83], v[208:211], v[40:43]\n"
"s_add_u32 m0, s53, 128\n"
"v_exp_f32 v198, v198\n"
"v_mfma_f32_16x16x32_bf16 v[40:43], a[84:87], v[212:215], v[40:43]\n"
"buffer_load_dwordx4 v247, s[36:39], s52 offen offset:2048 lds\n"
"v_mfma_f32_16x16x32_bf16 v[44:47], a[92:95], v[212:215], v[44:47]\n"
"v_exp_f32 v199, v199\n"
"v_mfma_f32_16x16x32_bf16 v[44:47], a[88:91], v[208:211], v[44:47]\n"
"ds_read_b128 a[12:15], v244 offset:35008\n"
"v_perm_b32 v203, v199, v198, s72\n"
"v_mfma_f32_16x16x32_bf16 v[48:51], a[96:99], v[208:211], v[48:51]\n"
"v_exp_f32 v224, v224\n"
"v_mfma_f32_16x16x32_bf16 v[48:51], a[100:103], v[212:215], v[48:51]\n"
"ds_read_b128 a[28:31], v244 offset:39360\n"
"ds_read_b128 a[24:27], v244 offset:39296\n"
"v_mfma_f32_16x16x32_bf16 v[52:55], a[108:111], v[212:215], v[52:55]\n"
"v_exp_f32 v225, v225\n"
"v_mfma_f32_16x16x32_bf16 v[52:55], a[104:107], v[208:211], v[52:55]\n"
"ds_read_b128 a[20:23], v244 offset:39232\n"
"v_perm_b32 v204, v225, v224, s72\n"
"v_mfma_f32_16x16x32_bf16 v[56:59], a[112:115], v[208:211], v[56:59]\n"
"s_add_u32 m0, s53, 192\n"
"v_exp_f32 v226, v226\n"
"v_mfma_f32_16x16x32_bf16 v[56:59], a[116:119], v[212:215], v[56:59]\n"
"buffer_load_dwordx4 v247, s[36:39], s52 offen offset:3072 lds\n"
"v_mfma_f32_16x16x32_bf16 v[60:63], a[124:127], v[212:215], v[60:63]\n"
"v_exp_f32 v227, v227\n"
"v_mfma_f32_16x16x32_bf16 v[60:63], a[120:123], v[208:211], v[60:63]\n"
"buffer_store_dwordx4 a[188:191], v250, s[48:51], s84 offen offset:1024 sc1\n"
"v_perm_b32 v205, v227, v226, s72\n"
"v_mfma_f32_16x16x32_bf16 v[192:195], a[32:35], v[176:179], -4.0\n"
"v_exp_f32 v228, v228\n"
"v_mfma_f32_16x16x32_bf16 v[192:195], a[36:39], v[180:183], v[192:195]\n"
"ds_read_b128 a[16:19], v244 offset:39168\n"
"ds_read_b128 a[32:35], v222 offset:34816\n"
"v_mfma_f32_16x16x32_bf16 v[192:195], a[40:43], v[184:187], v[192:195]\n"
"v_exp_f32 v229, v229\n"
"v_mfma_f32_16x16x32_bf16 v[192:195], a[44:47], v[188:191], v[192:195]\n"
"ds_read_b128 a[36:39], v222 offset:34880\n"
"v_perm_b32 v206, v229, v228, s72\n"
"v_mfma_f32_16x16x32_bf16 v[224:227], a[60:63], v[188:191], -4.0\n"
"s_add_u32 m0, s54, 0\n"
"v_exp_f32 v230, v230\n"
"v_mfma_f32_16x16x32_bf16 v[224:227], a[56:59], v[184:187], v[224:227]\n"
"buffer_load_dwordx4 v248, s[40:43], s52 offen offset:0 lds\n"
"v_mfma_f32_16x16x32_bf16 v[224:227], a[52:55], v[180:183], v[224:227]\n"
"v_exp_f32 v231, v231\n"
"v_mfma_f32_16x16x32_bf16 v[224:227], a[48:51], v[176:179], v[224:227]\n"
"ds_read_b128 a[40:43], v222 offset:34944\n"
"v_perm_b32 v207, v231, v230, s72\n"
"v_mfma_f32_16x16x32_bf16 v[64:67], a[64:67], v[200:203], v[64:67]\n"
"v_exp_f32 v192, v192\n"
"v_mfma_f32_16x16x32_bf16 v[64:67], a[68:71], v[204:207], v[64:67]\n"
"ds_read_b128 a[44:47], v222 offset:35008\n"
"ds_read_b128 a[60:63], v222 offset:39360\n"
"v_mfma_f32_16x16x32_bf16 v[68:71], a[76:79], v[204:207], v[68:71]\n"
"v_exp_f32 v193, v193\n"
"v_mfma_f32_16x16x32_bf16 v[68:71], a[72:75], v[200:203], v[68:71]\n"
"ds_read_b128 a[56:59], v222 offset:39296\n"
"v_perm_b32 v212, v193, v192, s72\n"
"v_mfma_f32_16x16x32_bf16 v[72:75], a[80:83], v[200:203], v[72:75]\n"
"s_add_u32 m0, s54, 8\n"
"v_exp_f32 v194, v194\n"
"v_mfma_f32_16x16x32_bf16 v[72:75], a[84:87], v[204:207], v[72:75]\n"
"Lhop2_1_%=:\n s_cbranch_execz Lhop2_5_%=\n"
"buffer_load_dwordx4 v248, s[40:43], s52 offen offset:1024 lds\n"
"v_mfma_f32_16x16x32_bf16 v[76:79], a[92:95], v[204:207], v[76:79]\n"
"v_exp_f32 v195, v195\n"
"v_mfma_f32_16x16x32_bf16 v[76:79], a[88:91], v[200:203], v[76:79]\n"
"ds_read_b128 a[52:55], v222 offset:39232\n"
"v_perm_b32 v213, v195, v194, s72\n"
"v_smfmac_f32_16x16x64_bf16 a[136:139], a[144:147], v[200:207], v251\n"
"s_waitcnt lgkmcnt(5)\n"
"ds_read_b128 a[48:51], v222 offset:39168\n"
"v_mfma_f32_16x16x32_bf16 v[192:195], a[0:3], v[128:131], -4.0\n"
"v_exp_f32 v224, v224\n"
"v_mfma_f32_16x16x32_bf16 v[192:195], a[4:7], v[132:135], v[192:195]\n"
"ds_read_b64_tr_b16 a[64:65], v245 offset:33024\n"
"ds_read_b64_tr_b16 a[66:67], v245 offset:37152\n"
"v_mfma_f32_16x16x32_bf16 v[192:195], a[8:11], v[136:139], v[192:195]\n"
"v_exp_f32 v225, v225\n"
"v_mfma_f32_16x16x32_bf16 v[192:195], a[12:15], v[140:143], v[192:195]\n"
"ds_read_b64_tr_b16 a[72:73], v245 offset:33032\n"
"v_perm_b32 v214, v225, v224, s72\n"
"v_mfma_f32_16x16x32_bf16 v[196:199], a[28:31], v[140:143], -4.0\n"
"s_add_u32 m0, s54, 1936\n"
"v_exp_f32 v226, v226\n"
"v_mfma_f32_16x16x32_bf16 v[196:199], a[24:27], v[136:139], v[196:199]\n"
"buffer_load_dwordx4 v248, s[40:43], s52 offen offset:128 lds\n"
"v_mfma_f32_16x16x32_bf16 v[196:199], a[20:23], v[132:135], v[196:199]\n"
"v_exp_f32 v227, v227\n"
"v_mfma_f32_16x16x32_bf16 v[196:199], a[16:19], v[128:131], v[196:199]\n"
"buffer_store_dwordx4 a[192:195], v250, s[48:51], s84 offen offset:128 sc1\n"
"v_perm_b32 v215, v227, v226, s72\n"
"v_mfma_f32_16x16x32_bf16 v[80:83], a[96:99], v[200:203], v[80:83]\n"
"v_exp_f32 v192, v192\n"
"v_mfma_f32_16x16x32_bf16 v[80:83], a[100:103], v[204:207], v[80:83]\n"
"ds_read_b64_tr_b16 a[74:75], v245 offset:37160\n"
"ds_read_b64_tr_b16 a[80:81], v245 offset:33280\n"
"v_mfma_f32_16x16x32_bf16 v[84:87], a[108:111], v[204:207], v[84:87]\n"
"v_exp_f32 v193, v193\n"
"v_mfma_f32_16x16x32_bf16 v[84:87], a[104:107], v[200:203], v[84:87]\n"
"ds_read_b64_tr_b16 a[82:83], v245 offset:37408\n"
"ds_read_b64_tr_b16 a[88:89], v245 offset:33288\n"
"v_mfma_f32_16x16x32_bf16 v[88:91], a[112:115], v[200:203], v[88:91]\n"
"s_add_u32 m0, s54, 1944\n"
"v_exp_f32 v194, v194\n"
"v_mfma_f32_16x16x32_bf16 v[88:91], a[116:119], v[204:207], v[88:91]\n"
"buffer_load_dwordx4 v248, s[40:43], s52 offen offset:1152 lds\n"
"v_mfma_f32_16x16x32_bf16 v[92:95], a[124:127], v[204:207], v[92:95]\n"
"s_add_u32 s52, s52, 16384\n"
"v_exp_f32 v195, v195\n"
"v_mfma_f32_16x16x32_bf16 v[92:95], a[120:123], v[200:203], v[92:95]\n"
"v_perm_b32 v200, v193, v192, s72\n"
"v_perm_b32 v201, v195, v194, s72\n"
"v_mfma_f32_16x16x32_bf16 v[224:227], a[32:35], v[128:131], -4.0\n"
"v_exp_f32 v196, v196\n"
"v_mfma_f32_16x16x32_bf16 v[224:227], a[36:39], v[132:135], v[224:227]\n"
"s_waitcnt lgkmcnt(4)\n"
"ds_read_b64_tr_b16 a[90:91], v245 offset:37416\n"
"ds_read_b64_tr_b16 a[96:97], v245 offset:35088\n"
"v_mfma_f32_16x16x32_bf16 v[224:227], a[40:43], v[136:139], v[224:227]\n"
"v_exp_f32 v197, v197\n"
"v_mfma_f32_16x16x32_bf16 v[224:227], a[44:47], v[140:143], v[224:227]\n"
"ds_read_b64_tr_b16 a[98:99], v245 offset:39216\n"
"v_perm_b32 v202, v197, v196, s72\n"
"v_mfma_f32_16x16x32_bf16 v[228:231], a[60:63], v[140:143], -4.0\n"
"v_exp_f32 v198, v198\n"
"v_mfma_f32_16x16x32_bf16 v[228:231], a[56:59], v[136:139], v[228:231]\n"
"ds_read_b64_tr_b16 a[104:105], v245 offset:35096\n"
"ds_read_b64_tr_b16 a[106:107], v245 offset:39224\n"
"v_mfma_f32_16x16x32_bf16 v[228:231], a[52:55], v[132:135], v[228:231]\n"
"v_exp_f32 v199, v199\n"
"v_mfma_f32_16x16x32_bf16 v[228:231], a[48:51], v[128:131], v[228:231]\n"
"ds_read_b64_tr_b16 a[112:113], v245 offset:35344\n"
"v_perm_b32 v203, v199, v198, s72\n"
"v_mfma_f32_16x16x32_bf16 a[140:143], a[144:147], v[212:215], a[140:143]\n"
"ds_read_b64_tr_b16 a[114:115], v245 offset:39472\n"
"ds_read_b64_tr_b16 a[120:121], v245 offset:35352\n"
"v_mfma_f32_16x16x32_bf16 v[96:99], a[68:71], v[212:215], v[96:99]\n"
"v_exp_f32 v224, v224\n"
"v_mfma_f32_16x16x32_bf16 v[100:103], a[76:79], v[212:215], v[100:103]\n"
"ds_read_b64_tr_b16 a[68:69], v255 offset:33024\n"
"ds_read_b64_tr_b16 a[70:71], v255 offset:37152\n"
"v_mfma_f32_16x16x32_bf16 v[104:107], a[84:87], v[212:215], v[104:107]\n"
"v_exp_f32 v225, v225\n"
"v_mfma_f32_16x16x32_bf16 v[108:111], a[92:95], v[212:215], v[108:111]\n"
"ds_read_b64_tr_b16 a[76:77], v255 offset:33032\n"
"v_perm_b32 v204, v225, v224, s72\n"
"v_mfma_f32_16x16x32_bf16 v[112:115], a[100:103], v[212:215], v[112:115]\n"
"v_exp_f32 v226, v226\n"
"v_mfma_f32_16x16x32_bf16 v[116:119], a[108:111], v[212:215], v[116:119]\n"
"ds_read_b64_tr_b16 a[78:79], v255 offset:37160\n"
"ds_read_b64_tr_b16 a[84:85], v255 offset:33280\n"
"v_mfma_f32_16x16x32_bf16 v[120:123], a[116:119], v[212:215], v[120:123]\n"
"v_exp_f32 v227, v227\n"
"v_mfma_f32_16x16x32_bf16 v[124:127], a[124:127], v[212:215], v[124:127]\n"
"ds_read_b64_tr_b16 a[86:87], v255 offset:37408\n"
"v_perm_b32 v205, v227, v226, s72\n"
"v_mfma_f32_16x16x32_bf16 v[192:195], a[0:3], v[144:147], -4.0\n"
"v_exp_f32 v228, v228\n"
"v_mfma_f32_16x16x32_bf16 v[192:195], a[4:7], v[148:151], v[192:195]\n"
"ds_read_b64_tr_b16 a[92:93], v255 offset:33288\n"
"ds_read_b64_tr_b16 a[94:95], v255 offset:37416\n"
"v_mfma_f32_16x16x32_bf16 v[192:195], a[8:11], v[152:155], v[192:195]\n"
"v_exp_f32 v229, v229\n"
"v_mfma_f32_16x16x32_bf16 v[192:195], a[12:15], v[156:159], v[192:195]\n"
"ds_read_b64_tr_b16 a[100:101], v255 offset:35088\n"
"v_perm_b32 v206, v229, v228, s72\n"
"v_mfma_f32_16x16x32_bf16 v[196:199], a[28:31], v[156:159], -4.0\n"
"v_exp_f32 v230, v230\n"
"v_mfma_f32_16x16x32_bf16 v[196:199], a[24:27], v[152:155], v[196:199]\n"
"ds_read_b64_tr_b16 a[102:103], v255 offset:39216\n"
"ds_read_b64_tr_b16 a[108:109], v255 offset:35096\n"
"v_mfma_f32_16x16x32_bf16 v[196:199], a[20:23], v[148:151], v[196:199]\n"
"v_exp_f32 v231, v231\n"
"v_mfma_f32_16x16x32_bf16 v[196:199], a[16:19], v[144:147], v[196:199]\n"
"buffer_store_dwordx4 a[196:199], v250, s[48:51], s84 offen offset:1152 sc1\n"
"v_perm_b32 v207, v231, v230, s72\n"
"v_mfma_f32_16x16x32_bf16 v[224:227], a[32:35], v[144:147], -4.0\n"
"s_add_u32 s84, s84, 4096\n"
"v_exp_f32 v192, v192\n"
"v_mfma_f32_16x16x32_bf16 v[224:227], a[36:39], v[148:151], v[224:227]\n"
"ds_read_b64_tr_b16 a[110:111], v255 offset:39224\n"
"ds_read_b64_tr_b16 a[116:117], v255 offset:35344\n"
"v_mfma_f32_16x16x32_bf16 v[224:227], a[40:43], v[152:155], v[224:227]\n"
"v_exp_f32 v193, v193\n"
"v_mfma_f32_16x16x32_bf16 v[224:227], a[44:47], v[156:159], v[224:227]\n"
"ds_read_b64_tr_b16 a[118:119], v255 offset:39472\n"
"v_perm_b32 v208, v193, v192, s72\n"
"v_mfma_f32_16x16x32_bf16 v[228:231], a[60:63], v[156:159], -4.0\n"
"v_exp_f32 v194, v194\n"
"v_mfma_f32_16x16x32_bf16 v[228:231], a[56:59], v[152:155], v[228:231]\n"
"ds_read_b64_tr_b16 a[124:125], v255 offset:35352\n"
"ds_read_b64_tr_b16 a[126:127], v255 offset:39480\n"
"v_mfma_f32_16x16x32_bf16 v[228:231], a[52:55], v[148:151], v[228:231]\n"
"v_exp_f32 v195, v195\n"
"v_mfma_f32_16x16x32_bf16 v[228:231], a[48:51], v[144:147], v[228:231]\n"
"ds_read_b64_tr_b16 a[122:123], v245 offset:39480\n"
"v_perm_b32 v209, v195, v194, s72\n"
"v_smfmac_f32_16x16x64_bf16 a[128:131], a[144:147], v[200:207], v251\n"
"v_mfma_f32_16x16x32_bf16 v[0:3], a[64:67], v[200:203], v[0:3]\n"
"s_waitcnt lgkmcnt(6)\n"
"v_exp_f32 v196, v196\n"
"v_mfma_f32_16x16x32_bf16 v[0:3], a[68:71], v[204:207], v[0:3]\n"
"v_mfma_f32_16x16x32_bf16 v[4:7], a[76:79], v[204:207], v[4:7]\n"
"v_exp_f32 v197, v197\n"
"v_mfma_f32_16x16x32_bf16 v[4:7], a[72:75], v[200:203], v[4:7]\n"
"v_perm_b32 v210, v197, v196, s72\n"
"v_mfma_f32_16x16x32_bf16 v[8:11], a[80:83], v[200:203], v[8:11]\n"
"v_exp_f32 v198, v198\n"
"v_mfma_f32_16x16x32_bf16 v[8:11], a[84:87], v[204:207], v[8:11]\n"
"v_mfma_f32_16x16x32_bf16 v[12:15], a[92:95], v[204:207], v[12:15]\n"
"v_exp_f32 v199, v199\n"
"v_mfma_f32_16x16x32_bf16 v[12:15], a[88:91], v[200:203], v[12:15]\n"
"s_waitcnt vmcnt(11)\n"
"ds_read2_b64 a[152:155], v246 offset0:0 offset1:2\n"
"v_perm_b32 v211, v199, v198, s72\n"
"v_mfma_f32_16x16x32_bf16 v[16:19], a[96:99], v[200:203], v[16:19]\n"
"v_exp_f32 v224, v224\n"
"v_mfma_f32_16x16x32_bf16 v[16:19], a[100:103], v[204:207], v[16:19]\n"
"s_waitcnt lgkmcnt(1)\n"
"ds_read2_b64 a[156:159], v246 offset0:1 offset1:3\n"
"ds_read2_b64 a[160:163], v246 offset0:32 offset1:34\n"
"v_mfma_f32_16x16x32_bf16 v[20:23], a[108:111], v[204:207], v[20:23]\n"
"v_exp_f32 v225, v225\n"
"v_mfma_f32_16x16x32_bf16 v[20:23], a[104:107], v[200:203], v[20:23]\n"
"ds_read2_b64 a[164:167], v246 offset0:33 offset1:35\n"
"v_perm_b32 v212, v225, v224, s72\n"
"v_mfma_f32_16x16x32_bf16 v[24:27], a[112:115], v[200:203], v[24:27]\n"
"s_add_u32 m0, s53, 17408\n"
"v_exp_f32 v226, v226\n"
"v_mfma_f32_16x16x32_bf16 v[24:27], a[116:119], v[204:207], v[24:27]\n"
"buffer_load_dwordx4 v247, s[36:39], s52 offen offset:0 lds\n"
"v_mfma_f32_16x16x32_bf16 v[28:31], a[124:127], v[204:207], v[28:31]\n"
"v_exp_f32 v227, v227\n"
"v_mfma_f32_16x16x32_bf16 v[28:31], a[120:123], v[200:203], v[28:31]\n"
"ds_read2_b64 a[168:171], v246 offset0:64 offset1:66\n"
"v_perm_b32 v213, v227, v226, s72\n"
"v_mfma_f32_16x16x32_bf16 v[192:195], a[0:3], v[160:163], -4.0\n"
"v_exp_f32 v228, v228\n"
"v_mfma_f32_16x16x32_bf16 v[192:195], a[4:7], v[164:167], v[192:195]\n"
"ds_read2_b64 a[172:175], v246 offset0:65 offset1:67\n"
"ds_read2_b64 a[176:179], v246 offset0:96 offset1:98\n"
"v_mfma_f32_16x16x32_bf16 v[192:195], a[8:11], v[168:171], v[192:195]\n"
"v_exp_f32 v229, v229\n"
"v_mfma_f32_16x16x32_bf16 v[192:195], a[12:15], v[172:175], v[192:195]\n"
"ds_read2_b64 a[180:183], v246 offset0:97 offset1:99\n"
"v_perm_b32 v214, v229, v228, s72\n"
"v_mfma_f32_16x16x32_bf16 v[196:199], a[28:31], v[172:175], -4.0\n"
"s_add_u32 m0, s53, 17472\n"
"v_exp_f32 v230, v230\n"
"v_mfma_f32_16x16x32_bf16 v[196:199], a[24:27], v[168:171], v[196:199]\n"
"buffer_load_dwordx4 v247, s[36:39], s52 offen offset:1024 lds\n"
"v_mfma_f32_16x16x32_bf16 v[196:199], a[20:23], v[164:167], v[196:199]\n"
"v_exp_f32 v231, v231\n"
"v_mfma_f32_16x16x32_bf16 v[196:199], a[16:19], v[160:163], v[196:199]\n"
"v_perm_b32 v215, v231, v230, s72\n"
"v_mfma_f32_16x16x32_bf16 v[224:227], a[32:35], v[160:163], -4.0\n"
"s_add_u32 m0, s53, 17536\n"
"v_exp_f32 v192, v192\n"
"v_mfma_f32_16x16x32_bf16 v[224:227], a[36:39], v[164:167], v[224:227]\n"
"buffer_load_dwordx4 v247, s[36:39], s52 offen offset:2048 lds\n"
"v_mfma_f32_16x16x32_bf16 v[224:227], a[40:43], v[168:171], v[224:227]\n"
"v_exp_f32 v193, v193\n"
"v_mfma_f32_16x16x32_bf16 v[224:227], a[44:47], v[172:175], v[224:227]\n"
"v_perm_b32 v200, v193, v192, s72\n"
"v_mfma_f32_16x16x32_bf16 v[228:231], a[60:63], v[172:175], -4.0\n"
"s_add_u32 m0, s53, 17600\n"
"v_exp_f32 v194, v194\n"
"v_mfma_f32_16x16x32_bf16 v[228:231], a[56:59], v[168:171], v[228:231]\n"
"buffer_load_dwordx4 v247, s[36:39], s52 offen offset:3072 lds\n"
"v_mfma_f32_16x16x32_bf16 v[228:231], a[52:55], v[164:167], v[228:231]\n"
"v_exp_f32 v195, v195\n"
"v_mfma_f32_16x16x32_bf16 v[228:231], a[48:51], v[160:163], v[228:231]\n"
"s_waitcnt vmcnt(12) lgkmcnt(8)\n"
"v_perm_b32 v201, v195, v194, s72\n"
"v_smfmac_f32_16x16x64_bf16 a[132:135], a[144:147], v[208:215], v251\n"
"v_mfma_f32_16x16x32_bf16 v[32:35], a[64:67], v[208:211], v[32:35]\n"
"v_exp_f32 v196, v196\n"
"v_mfma_f32_16x16x32_bf16 v[32:35], a[68:71], v[212:215], v[32:35]\n"
"s_barrier\n"
"ds_read_b128 a[0:3], v244 offset:52224\n"
"ds_read_b128 a[4:7], v244 offset:52288\n"
"v_mfma_f32_16x16x32_bf16 v[36:39], a[76:79], v[212:215], v[36:39]\n"
"v_exp_f32 v197, v197\n"
"v_mfma_f32_16x16x32_bf16 v[36:39], a[72:75], v[208:211], v[36:39]\n"
"ds_read_b128 a[8:11], v244 offset:52352\n"
"v_perm_b32 v202, v197, v196, s72\n"
"v_mfma_f32_16x16x32_bf16 v[40:43], a[80:83], v[208:211], v[40:43]\n"
"s_add_u32 m0, s54, 16512\n"
"v_exp_f32 v198, v198\n"
"v_mfma_f32_16x16x32_bf16 v[40:43], a[84:87], v[212:215], v[40:43]\n"
"buffer_load_dwordx4 v248, s[40:43], s52 offen offset:0 lds\n"
"v_mfma_f32_16x16x32_bf16 v[44:47], a[92:95], v[212:215], v[44:47]\n"
"v_exp_f32 v199, v199\n"
"v_mfma_f32_16x16x32_bf16 v[44:47], a[88:91], v[208:211], v[44:47]\n"
"ds_read_b128 a[12:15], v244 offset:52416\n"
"v_perm_b32 v203, v199, v198, s72\n"
"v_mfma_f32_16x16x32_bf16 v[48:51], a[96:99], v[208:211], v[48:51]\n"
"v_exp_f32 v224, v224\n"
"v_mfma_f32_16x16x32_bf16 v[48:51], a[100:103], v[212:215], v[48:51]\n"
"ds_read_b128 a[28:31], v244 offset:56768\n"
"ds_read_b128 a[24:27], v244 offset:56704\n"
"v_mfma_f32_16x16x32_bf16 v[52:55], a[108:111], v[212:215], v[52:55]\n"
"v_exp_f32 v225, v225\n"
"v_mfma_f32_16x16x32_bf16 v[52:55], a[104:107], v[208:211], v[52:55]\n"
"ds_read_b128 a[20:23], v244 offset:56640\n"
"v_perm_b32 v204, v225, v224, s72\n"
"v_mfma_f32_16x16x32_bf16 v[56:59], a[112:115], v[208:211], v[56:59]\n"
"s_add_u32 m0, s55, 0\n"
"v_exp_f32 v226, v226\n"
"v_mfma_f32_16x16x32_bf16 v[56:59], a[116:119], v[212:215], v[56:59]\n"
"s_waitcnt lgkmcnt(7)\n"
"buffer_load_dwordx4 v249, s[44:47], s76 offen offset:0 nt lds\n"
"v_mfma_f32_16x16x32_bf16 v[60:63], a[124:127], v[212:215], v[60:63]\n"
"v_exp_f32 v227, v227\n"
"v_mfma_f32_16x16x32_bf16 v[60:63], a[120:123], v[208:211], v[60:63]\n"
"ds_read_b128 a[16:19], v244 offset:56576\n"
"v_perm_b32 v205, v227, v226, s72\n"
"s_waitcnt lgkmcnt(0)\n"
"LBS%=:\n"
"v_mfma_f32_16x16x32_bf16 v[192:195], a[32:35], v[176:179], -4.0\n"
"Lhop0_7_%=:\n s_cbranch_execz Lback0_%=\n"
"v_exp_f32 v228, v228\n"
"v_mfma_f32_16x16x32_bf16 v[192:195], a[36:39], v[180:183], v[192:195]\n"
"ds_read_b128 a[32:35], v222 offset:52224\n"
"ds_read_b128 a[36:39], v222 offset:52288\n"
"v_mfma_f32_16x16x32_bf16 v[192:195], a[40:43], v[184:187], v[192:195]\n"
"v_exp_f32 v229, v229\n"
"v_mfma_f32_16x16x32_bf16 v[192:195], a[44:47], v[188:191], v[192:195]\n"
"ds_read_b128 a[40:43], v222 offset:52352\n"
"v_perm_b32 v206, v229, v228, s72\n"
"v_mfma_f32_16x16x32_bf16 v[224:227], a[60:63], v[188:191], -4.0\n"
"s_add_u32 m0, s55, 8\n"
"v_exp_f32 v230, v230\n"
"v_mfma_f32_16x16x32_bf16 v[224:227], a[56:59], v[184:187], v[224:227]\n"
"buffer_load_dwordx4 v249, s[44:47], s76 offen offset:1024 nt lds\n"
"v_mfma_f32_16x16x32_bf16 v[224:227], a[52:55], v[180:183], v[224:227]\n"
"v_exp_f32 v231, v231\n"
"v_mfma_f32_16x16x32_bf16 v[224:227], a[48:51], v[176:179], v[224:227]\n"
"ds_read_b128 a[44:47], v222 offset:52416\n"
"v_perm_b32 v207, v231, v230, s72\n"
"v_mfma_f32_16x16x32_bf16 v[64:67], a[64:67], v[200:203], v[64:67]\n"
"v_exp_f32 v192, v192\n"
"v_mfma_f32_16x16x32_bf16 v[64:67], a[68:71], v[204:207], v[64:67]\n"
"ds_read_b128 a[60:63], v222 offset:56768\n"
"ds_read_b128 a[56:59], v222 offset:56704\n"
"v_mfma_f32_16x16x32_bf16 v[68:71], a[76:79], v[204:207], v[68:71]\n"
"v_exp_f32 v193, v193\n"
"v_mfma_f32_16x16x32_bf16 v[68:71], a[72:75], v[200:203], v[68:71]\n"
"ds_read_b128 a[52:55], v222 offset:56640\n"
"v_perm_b32 v212, v193, v192, s72\n"
"v_mfma_f32_16x16x32_bf16 v[72:75], a[80:83], v[200:203], v[72:75]\n"
"s_add_u32 m0, s55, 16\n"
"v_exp_f32 v194, v194\n"
"v_mfma_f32_16x16x32_bf16 v[72:75], a[84:87], v[204:207], v[72:75]\n"
"buffer_load_dwordx4 v249, s[44:47], s76 offen offset:2048 nt lds\n"
"v_mfma_f32_16x16x32_bf16 v[76:79], a[92:95], v[204:207], v[76:79]\n"
"v_exp_f32 v195, v195\n"
"v_mfma_f32_16x16x32_bf16 v[76:79], a[88:91], v[200:203], v[76:79]\n"
"ds_read_b128 a[48:51], v222 offset:56576\n"
"v_perm_b32 v213, v195, v194, s72\n"
"v_smfmac_f32_16x16x64_bf16 a[136:139], a[144:147], v[200:207], v251\n"
"ds_read_b64_tr_b16 a[64:65], v245 offset:49536\n"
"v_mfma_f32_16x16x32_bf16 v[192:195], a[0:3], v[128:131], -4.0\n"
"v_exp_f32 v224, v224\n"
"v_mfma_f32_16x16x32_bf16 v[192:195], a[4:7], v[132:135], v[192:195]\n"
"ds_read_b64_tr_b16 a[66:67], v245 offset:53664\n"
"ds_read_b64_tr_b16 a[72:73], v245 offset:49544\n"
"v_mfma_f32_16x16x32_bf16 v[192:195], a[8:11], v[136:139], v[192:195]\n"
"v_exp_f32 v225, v225\n"
"v_mfma_f32_16x16x32_bf16 v[192:195], a[12:15], v[140:143], v[192:195]\n"
"ds_read_b64_tr_b16 a[74:75], v245 offset:53672\n"
"v_perm_b32 v214, v225, v224, s72\n"
"v_mfma_f32_16x16x32_bf16 v[196:199], a[28:31], v[140:143], -4.0\n"
"s_add_u32 m0, s55, 24\n"
"v_exp_f32 v226, v226\n"
"v_mfma_f32_16x16x32_bf16 v[196:199], a[24:27], v[136:139], v[196:199]\n"
"buffer_load_dwordx4 v249, s[44:47], s76 offen offset:3072 nt lds\n"
"v_mfma_f32_16x16x32_bf16 v[196:199], a[20:23], v[132:135], v[196:199]\n"
"s_add_u32 s76, s76, 4096\n"
"v_exp_f32 v227, v227\n"
"v_mfma_f32_16x16x32_bf16 v[196:199], a[16:19], v[128:131], v[196:199]\n"
"ds_read_b64_tr_b16 a[80:81], v245 offset:49792\n"
"v_perm_b32 v215, v227, v226, s72\n"
"v_mfma_f32_16x16x32_bf16 v[80:83], a[96:99], v[200:203], v[80:83]\n"
"v_exp_f32 v192, v192\n"
"v_mfma_f32_16x16x32_bf16 v[80:83], a[100:103], v[204:207], v[80:83]\n"
"ds_read_b64_tr_b16 a[82:83], v245 offset:53920\n"
"ds_read_b64_tr_b16 a[88:89], v245 offset:49800\n"
"v_mfma_f32_16x16x32_bf16 v[84:87], a[108:111], v[204:207], v[84:87]\n"
"v_exp_f32 v193, v193\n"
"v_mfma_f32_16x16x32_bf16 v[84:87], a[104:107], v[200:203], v[84:87]\n"
"ds_read_b64_tr_b16 a[90:91], v245 offset:53928\n"
"ds_read_b64_tr_b16 a[96:97], v245 offset:51600\n"
"v_mfma_f32_16x16x32_bf16 v[88:91], a[112:115], v[200:203], v[88:91]\n"
"s_add_u32 m0, s54, 16520\n"
"v_exp_f32 v194, v194\n"
"v_mfma_f32_16x16x32_bf16 v[88:91], a[116:119], v[204:207], v[88:91]\n"
"buffer_load_dwordx4 v248, s[40:43], s52 offen offset:1024 lds\n"
"v_mfma_f32_16x16x32_bf16 v[92:95], a[124:127], v[204:207], v[92:95]\n"
"v_exp_f32 v195, v195\n"
"v_mfma_f32_16x16x32_bf16 v[92:95], a[120:123], v[200:203], v[92:95]\n"
"s_waitcnt lgkmcnt(5)\n"
"v_perm_b32 v200, v193, v192, s72\n"
"v_perm_b32 v201, v195, v194, s72\n"
"v_mfma_f32_16x16x32_bf16 v[224:227], a[32:35], v[128:131], -4.0\n"
"v_exp_f32 v196, v196\n"
"v_mfma_f32_16x16x32_bf16 v[224:227], a[36:39], v[132:135], v[224:227]\n"
"ds_read_b64_tr_b16 a[98:99], v245 offset:55728\n"
"ds_read_b64_tr_b16 a[104:105], v245 offset:51608\n"
"v_mfma_f32_16x16x32_bf16 v[224:227], a[40:43], v[136:139], v[224:227]\n"
"v_exp_f32 v197, v197\n"
"v_mfma_f32_16x16x32_bf16 v[224:227], a[44:47], v[140:143], v[224:227]\n"
"ds_read_b64_tr_b16 a[106:107], v245 offset:55736\n"
"v_perm_b32 v202, v197, v196, s72\n"
"v_mfma_f32_16x16x32_bf16 v[228:231], a[60:63], v[140:143], -4.0\n"
"s_add_u32 m0, s54, 18448\n"
"v_exp_f32 v198, v198\n"
"v_mfma_f32_16x16x32_bf16 v[228:231], a[56:59], v[136:139], v[228:231]\n"
"buffer_load_dwordx4 v248, s[40:43], s52 offen offset:128 lds\n"
"v_mfma_f32_16x16x32_bf16 v[228:231], a[52:55], v[132:135], v[228:231]\n"
"v_exp_f32 v199, v199\n"
"v_mfma_f32_16x16x32_bf16 v[228:231], a[48:51], v[128:131], v[228:231]\n"
"ds_read_b64_tr_b16 a[112:113], v245 offset:51856\n"
"v_perm_b32 v203, v199, v198, s72\n"
"v_mfma_f32_16x16x32_bf16 a[140:143], a[144:147], v[212:215], a[140:143]\n"
"ds_read_b64_tr_b16 a[114:115], v245 offset:55984\n"
"ds_read_b64_tr_b16 a[120:121], v245 offset:51864\n"
"v_mfma_f32_16x16x32_bf16 v[96:99], a[68:71], v[212:215], v[96:99]\n"
"v_exp_f32 v224, v224\n"
"v_mfma_f32_16x16x32_bf16 v[100:103], a[76:79], v[212:215], v[100:103]\n"
"ds_read_b64_tr_b16 a[68:69], v255 offset:49536\n"
"ds_read_b64_tr_b16 a[70:71], v255 offset:53664\n"
"v_mfma_f32_16x16x32_bf16 v[104:107], a[84:87], v[212:215], v[104:107]\n"
"v_exp_f32 v225, v225\n"
"v_mfma_f32_16x16x32_bf16 v[108:111], a[92:95], v[212:215], v[108:111]\n"
"ds_read_b64_tr_b16 a[76:77], v255 offset:49544\n"
"v_perm_b32 v204, v225, v224, s72\n"
"v_mfma_f32_16x16x32_bf16 v[112:115], a[100:103], v[212:215], v[112:115]\n"
"v_exp_f32 v226, v226\n"
"v_mfma_f32_16x16x32_bf16 v[116:119], a[108:111], v[212:215], v[116:119]\n"
"ds_read_b64_tr_b16 a[78:79], v255 offset:53672\n"
"ds_read_b64_tr_b16 a[84:85], v255 offset:49792\n"
"v_mfma_f32_16x16x32_bf16 v[120:123], a[116:119], v[212:215], v[120:123]\n"
"v_exp_f32 v227, v227\n"
"v_mfma_f32_16x16x32_bf16 v[124:127], a[124:127], v[212:215], v[124:127]\n"
"ds_read_b64_tr_b16 a[86:87], v255 offset:53920\n"
"v_perm_b32 v205, v227, v226, s72\n"
"v_mfma_f32_16x16x32_bf16 v[192:195], a[0:3], v[144:147], -4.0\n"
"v_exp_f32 v228, v228\n"
"v_mfma_f32_16x16x32_bf16 v[192:195], a[4:7], v[148:151], v[192:195]\n"
"ds_read_b64_tr_b16 a[92:93], v255 offset:49800\n"
"ds_read_b64_tr_b16 a[94:95], v255 offset:53928\n"
"v_mfma_f32_16x16x32_bf16 v[192:195], a[8:11], v[152:155], v[192:195]\n"
"v_exp_f32 v229, v229\n"
"v_mfma_f32_16x16x32_bf16 v[192:195], a[12:15], v[156:159], v[192:195]\n"
"ds_read_b64_tr_b16 a[100:101], v255 offset:51600\n"
"v_perm_b32 v206, v229, v228, s72\n"
"v_mfma_f32_16x16x32_bf16 v[196:199], a[28:31], v[156:159], -4.0\n"
"v_exp_f32 v230, v230\n"
"v_mfma_f32_16x16x32_bf16 v[196:199], a[24:27], v[152:155], v[196:199]\n"
"ds_read_b64_tr_b16 a[102:103], v255 offset:55728\n"
"ds_read_b64_tr_b16 a[108:109], v255 offset:51608\n"
"v_mfma_f32_16x16x32_bf16 v[196:199], a[20:23], v[148:151], v[196:199]\n"
"v_exp_f32 v231, v231\n"
"v_mfma_f32_16x16x32_bf16 v[196:199], a[16:19], v[144:147], v[196:199]\n"
"ds_read_b64_tr_b16 a[110:111], v255 offset:55736\n"
"v_perm_b32 v207, v231, v230, s72\n"
"v_mfma_f32_16x16x32_bf16 v[224:227], a[32:35], v[144:147], -4.0\n"
"v_exp_f32 v192, v192\n"
"v_mfma_f32_16x16x32_bf16 v[224:227], a[36:39], v[148:151], v[224:227]\n"
"ds_read_b64_tr_b16 a[116:117], v255 offset:51856\n"
"ds_read_b64_tr_b16 a[118:119], v255 offset:55984\n"
"v_mfma_f32_16x16x32_bf16 v[224:227], a[40:43], v[152:155], v[224:227]\n"
"v_exp_f32 v193, v193\n"
"v_mfma_f32_16x16x32_bf16 v[224:227], a[44:47], v[156:159], v[224:227]\n"
"ds_read_b64_tr_b16 a[124:125], v255 offset:51864\n"
"v_perm_b32 v208, v193, v192, s72\n"
"v_mfma_f32_16x16x32_bf16 v[228:231], a[60:63], v[156:159], -4.0\n"
"v_exp_f32 v194, v194\n"
"v_mfma_f32_16x16x32_bf16 v[228:231], a[56:59], v[152:155], v[228:231]\n"
"ds_read_b64_tr_b16 a[126:127], v255 offset:55992\n"
"ds_read_b64_tr_b16 a[122:123], v245 offset:55992\n"
"v_mfma_f32_16x16x32_bf16 v[228:231], a[52:55], v[148:151], v[228:231]\n"
"v_exp_f32 v195, v195\n"
"v_mfma_f32_16x16x32_bf16 v[228:231], a[48:51], v[144:147], v[228:231]\n"
"v_perm_b32 v209, v195, v194, s72\n"
"v_smfmac_f32_16x16x64_bf16 a[128:131], a[144:147], v[200:207], v251\n"
"v_mfma_f32_16x16x32_bf16 v[0:3], a[64:67], v[200:203], v[0:3]\n"
"s_waitcnt lgkmcnt(6)\n"
"v_exp_f32 v196, v196\n"
"v_mfma_f32_16x16x32_bf16 v[0:3], a[68:71], v[204:207], v[0:3]\n"
"v_mfma_f32_16x16x32_bf16 v[4:7], a[76:79], v[204:207], v[4:7]\n"
"v_exp_f32 v197, v197\n"
"v_mfma_f32_16x16x32_bf16 v[4:7], a[72:75], v[200:203], v[4:7]\n"
"v_perm_b32 v210, v197, v196, s72\n"
"v_mfma_f32_16x16x32_bf16 v[8:11], a[80:83], v[200:203], v[8:11]\n"
"v_exp_f32 v198, v198\n"
"v_mfma_f32_16x16x32_bf16 v[8:11], a[84:87], v[204:207], v[8:11]\n"
"v_mfma_f32_16x16x32_bf16 v[12:15], a[92:95], v[204:207], v[12:15]\n"
"v_exp_f32 v199, v199\n"
"v_mfma_f32_16x16x32_bf16 v[12:15], a[88:91], v[200:203], v[12:15]\n"
"v_perm_b32 v211, v199, v198, s72\n"
"v_mfma_f32_16x16x32_bf16 v[16:19], a[96:99], v[200:203], v[16:19]\n"
"v_exp_f32 v224, v224\n"
"v_mfma_f32_16x16x32_bf16 v[16:19], a[100:103], v[204:207], v[16:19]\n"
"s_waitcnt lgkmcnt(0)\n"
"v_mfma_f32_16x16x32_bf16 v[20:23], a[108:111], v[204:207], v[20:23]\n"
"v_exp_f32 v225, v225\n"
"v_mfma_f32_16x16x32_bf16 v[20:23], a[104:107], v[200:203], v[20:23]\n"
"v_perm_b32 v212, v225, v224, s72\n"
"v_mfma_f32_16x16x32_bf16 v[24:27], a[112:115], v[200:203], v[24:27]\n"
"s_add_u32 m0, s54, 18456\n"
"v_exp_f32 v226, v226\n"
"v_mfma_f32_16x16x32_bf16 v[24:27], a[116:119], v[204:207], v[24:27]\n"
"buffer_load_dwordx4 v248, s[40:43], s52 offen offset:1152 lds\n"
"v_mfma_f32_16x16x32_bf16 v[28:31], a[124:127], v[204:207], v[28:31]\n"
"s_add_u32 s52, s52, 16384\n"
"v_exp_f32 v227, v227\n"
"v_mfma_f32_16x16x32_bf16 v[28:31], a[120:123], v[200:203], v[28:31]\n"
"buffer_store_dwordx4 a[216:219], v250, s[48:51], s84 offen offset:0 sc1\n"
"v_perm_b32 v213, v227, v226, s72\n"
"v_mfma_f32_16x16x32_bf16 v[192:195], a[0:3], v[160:163], -4.0\n"
"v_exp_f32 v228, v228\n"
"v_mfma_f32_16x16x32_bf16 v[192:195], a[4:7], v[164:167], v[192:195]\n"
"v_mfma_f32_16x16x32_bf16 v[192:195], a[8:11], v[168:171], v[192:195]\n"
"v_exp_f32 v229, v229\n"
"v_mfma_f32_16x16x32_bf16 v[192:195], a[12:15], v[172:175], v[192:195]\n"
"v_perm_b32 v214, v229, v228, s72\n"
"v_mfma_f32_16x16x32_bf16 v[196:199], a[28:31], v[172:175], -4.0\n"
"s_add_u32 m0, s53, 34816\n"
"v_exp_f32 v230, v230\n"
"v_mfma_f32_16x16x32_bf16 v[196:199], a[24:27], v[168:171], v[196:199]\n"
"buffer_load_dwordx4 v247, s[36:39], s52 offen offset:0 lds\n"
"v_mfma_f32_16x16x32_bf16 v[196:199], a[20:23], v[164:167], v[196:199]\n"
"v_exp_f32 v231, v231\n"
"v_mfma_f32_16x16x32_bf16 v[196:199], a[16:19], v[160:163], v[196:199]\n"
"v_perm_b32 v215, v231, v230, s72\n"
"v_mfma_f32_16x16x32_bf16 v[224:227], a[32:35], v[160:163], -4.0\n"
"v_exp_f32 v192, v192\n"
"v_mfma_f32_16x16x32_bf16 v[224:227], a[36:39], v[164:167], v[224:227]\n"
"v_mfma_f32_16x16x32_bf16 v[224:227], a[40:43], v[168:171], v[224:227]\n"
"v_exp_f32 v193, v193\n"
"v_mfma_f32_16x16x32_bf16 v[224:227], a[44:47], v[172:175], v[224:227]\n"
"v_perm_b32 v200, v193, v192, s72\n"
"v_mfma_f32_16x16x32_bf16 v[228:231], a[60:63], v[172:175], -4.0\n"
"s_add_u32 m0, s53, 34880\n"
"v_exp_f32 v194, v194\n"
"v_mfma_f32_16x16x32_bf16 v[228:231], a[56:59], v[168:171], v[228:231]\n"
"buffer_load_dwordx4 v247, s[36:39], s52 offen offset:1024 lds\n"
"v_mfma_f32_16x16x32_bf16 v[228:231], a[52:55], v[164:167], v[228:231]\n"
"v_exp_f32 v195, v195\n"
"v_mfma_f32_16x16x32_bf16 v[228:231], a[48:51], v[160:163], v[228:231]\n"
"s_waitcnt vmcnt(14) lgkmcnt(0)\n"
"v_perm_b32 v201, v195, v194, s72\n"
"v_smfmac_f32_16x16x64_bf16 a[132:135], a[144:147], v[208:215], v251\n"
"v_mfma_f32_16x16x32_bf16 v[32:35], a[64:67], v[208:211], v[32:35]\n"
"v_exp_f32 v196, v196\n"
"v_mfma_f32_16x16x32_bf16 v[32:35], a[68:71], v[212:215], v[32:35]\n"
"s_barrier\n"
"ds_read_b128 a[0:3], v244 offset:0\n"
"ds_read_b128 a[4:7], v244 offset:64\n"
"v_mfma_f32_16x16x32_bf16 v[36:39], a[76:79], v[212:215], v[36:39]\n"
"v_exp_f32 v197, v197\n"
"v_mfma_f32_16x16x32_bf16 v[36:39], a[72:75], v[208:211], v[36:39]\n"
"ds_read_b128 a[8:11], v244 offset:128\n"
"v_perm_b32 v202, v197, v196, s72\n"
"v_mfma_f32_16x16x32_bf16 v[40:43], a[80:83], v[208:211], v[40:43]\n"
"s_add_u32 m0, s53, 34944\n"
"v_exp_f32 v198, v198\n"
"v_mfma_f32_16x16x32_bf16 v[40:43], a[84:87], v[212:215], v[40:43]\n"
"buffer_load_dwordx4 v247, s[36:39], s52 offen offset:2048 lds\n"
"v_mfma_f32_16x16x32_bf16 v[44:47], a[92:95], v[212:215], v[44:47]\n"
"v_exp_f32 v199, v199\n"
"v_mfma_f32_16x16x32_bf16 v[44:47], a[88:91], v[208:211], v[44:47]\n"
"ds_read_b128 a[12:15], v244 offset:192\n"
"v_perm_b32 v203, v199, v198, s72\n"
"v_mfma_f32_16x16x32_bf16 v[48:51], a[96:99], v[208:211], v[48:51]\n"
"v_exp_f32 v224, v224\n"
"v_mfma_f32_16x16x32_bf16 v[48:51], a[100:103], v[212:215], v[48:51]\n"
"ds_read_b128 a[28:31], v244 offset:4544\n"
"ds_read_b128 a[24:27], v244 offset:4480\n"
"v_mfma_f32_16x16x32_bf16 v[52:55], a[108:111], v[212:215], v[52:55]\n"
"v_exp_f32 v225, v225\n"
"v_mfma_f32_16x16x32_bf16 v[52:55], a[104:107], v[208:211], v[52:55]\n"
"ds_read_b128 a[20:23], v244 offset:4416\n"
"v_perm_b32 v204, v225, v224, s72\n"
"v_mfma_f32_16x16x32_bf16 v[56:59], a[112:115], v[208:211], v[56:59]\n"
"s_add_u32 m0, s53, 35008\n"
"v_exp_f32 v226, v226\n"
"v_mfma_f32_16x16x32_bf16 v[56:59], a[116:119], v[212:215], v[56:59]\n"
"buffer_load_dwordx4 v247, s[36:39], s52 offen offset:3072 lds\n"
"v_mfma_f32_16x16x32_bf16 v[60:63], a[124:127], v[212:215], v[60:63]\n"
"v_exp_f32 v227, v227\n"
"v_mfma_f32_16x16x32_bf16 v[60:63], a[120:123], v[208:211], v[60:63]\n"
"buffer_store_dwordx4 a[220:223], v250, s[48:51], s84 offen offset:1024 sc1\n"
"v_perm_b32 v205, v227, v226, s72\n"
"v_mfma_f32_16x16x32_bf16 v[192:195], a[32:35], v[176:179], -4.0\n"
"v_exp_f32 v228, v228\n"
"v_mfma_f32_16x16x32_bf16 v[192:195], a[36:39], v[180:183], v[192:195]\n"
"ds_read_b128 a[16:19], v244 offset:4352\n"
"ds_read_b128 a[32:35], v222 offset:0\n"
"v_mfma_f32_16x16x32_bf16 v[192:195], a[40:43], v[184:187], v[192:195]\n"
"v_exp_f32 v229, v229\n"
"v_mfma_f32_16x16x32_bf16 v[192:195], a[44:47], v[188:191], v[192:195]\n"
"ds_read_b128 a[36:39], v222 offset:64\n"
"v_perm_b32 v206, v229, v228, s72\n"
"v_mfma_f32_16x16x32_bf16 v[224:227], a[60:63], v[188:191], -4.0\n"
"s_add_u32 m0, s54, 33024\n"
"v_exp_f32 v230, v230\n"
"v_mfma_f32_16x16x32_bf16 v[224:227], a[56:59], v[184:187], v[224:227]\n"
"buffer_load_dwordx4 v248, s[40:43], s52 offen offset:0 lds\n"
"v_mfma_f32_16x16x32_bf16 v[224:227], a[52:55], v[180:183], v[224:227]\n"
"v_exp_f32 v231, v231\n"
"v_mfma_f32_16x16x32_bf16 v[224:227], a[48:51], v[176:179], v[224:227]\n"
"ds_read_b128 a[40:43], v222 offset:128\n"
"v_perm_b32 v207, v231, v230, s72\n"
"v_mfma_f32_16x16x32_bf16 v[64:67], a[64:67], v[200:203], v[64:67]\n"
"v_exp_f32 v192, v192\n"
"v_mfma_f32_16x16x32_bf16 v[64:67], a[68:71], v[204:207], v[64:67]\n"
"ds_read_b128 a[44:47], v222 offset:192\n"
"ds_read_b128 a[60:63], v222 offset:4544\n"
"v_mfma_f32_16x16x32_bf16 v[68:71], a[76:79], v[204:207], v[68:71]\n"
"v_exp_f32 v193, v193\n"
"v_mfma_f32_16x16x32_bf16 v[68:71], a[72:75], v[200:203], v[68:71]\n"
"ds_read_b128 a[56:59], v222 offset:4480\n"
"v_perm_b32 v212, v193, v192, s72\n"
"v_mfma_f32_16x16x32_bf16 v[72:75], a[80:83], v[200:203], v[72:75]\n"
"s_add_u32 m0, s54, 33032\n"
"v_exp_f32 v194, v194\n"
"v_mfma_f32_16x16x32_bf16 v[72:75], a[84:87], v[204:207], v[72:75]\n"
"buffer_load_dwordx4 v248, s[40:43], s52 offen offset:1024 lds\n"
"v_mfma_f32_16x16x32_bf16 v[76:79], a[92:95], v[204:207], v[76:79]\n"
"v_exp_f32 v195, v195\n"
"v_mfma_f32_16x16x32_bf16 v[76:79], a[88:91], v[200:203], v[76:79]\n"
"ds_read_b128 a[52:55], v222 offset:4416\n"
"v_perm_b32 v213, v195, v194, s72\n"
"v_smfmac_f32_16x16x64_bf16 a[136:139], a[144:147], v[200:207], v251\n"
"s_waitcnt lgkmcnt(5)\n"
"ds_read_b128 a[48:51], v222 offset:4352\n"
"v_mfma_f32_16x16x32_bf16 v[192:195], a[0:3], v[128:131], -4.0\n"
"Lhop1_0_%=:\n s_cbranch_execz Lhop1_4_%=\n"
"v_exp_f32 v224, v224\n"
"v_mfma_f32_16x16x32_bf16 v[192:195], a[4:7], v[132:135], v[192:195]\n"
"ds_read_b64_tr_b16 a[64:65], v245 offset:0\n"
"ds_read_b64_tr_b16 a[66:67], v245 offset:4128\n"
"v_mfma_f32_16x16x32_bf16 v[192:195], a[8:11], v[136:139], v[192:195]\n"
"v_exp_f32 v225, v225\n"
"v_mfma_f32_16x16x32_bf16 v[192:195], a[12:15], v[140:143], v[192:195]\n"
"ds_read_b64_tr_b16 a[72:73], v245 offset:8\n"
"v_perm_b32 v214, v225, v224, s72\n"
"v_mfma_f32_16x16x32_bf16 v[196:199], a[28:31], v[140:143], -4.0\n"
"s_add_u32 m0, s54, 34960\n"
"v_exp_f32 v226, v226\n"
"v_mfma_f32_16x16x32_bf16 v[196:199], a[24:27], v[136:139], v[196:199]\n"
"buffer_load_dwordx4 v248, s[40:43], s52 offen offset:128 lds\n"
"v_mfma_f32_16x16x32_bf16 v[196:199], a[20:23], v[132:135], v[196:199]\n"
"v_exp_f32 v227, v227\n"
"v_mfma_f32_16x16x32_bf16 v[196:199], a[16:19], v[128:131], v[196:199]\n"
"buffer_store_dwordx4 a[224:227], v250, s[48:51], s84 offen offset:128 sc1\n"
"v_perm_b32 v215, v227, v226, s72\n"
"v_mfma_f32_16x16x32_bf16 v[80:83], a[96:99], v[200:203], v[80:83]\n"
"v_exp_f32 v192, v192\n"
"v_mfma_f32_16x16x32_bf16 v[80:83], a[100:103], v[204:207], v[80:83]\n"
"ds_read_b64_tr_b16 a[74:75], v245 offset:4136\n"
"ds_read_b64_tr_b16 a[80:81], v245 offset:256\n"
"v_mfma_f32_16x16x32_bf16 v[84:87], a[108:111], v[204:207], v[84:87]\n"
"v_exp_f32 v193, v193\n"
"v_mfma_f32_16x16x32_bf16 v[84:87], a[104:107], v[200:203], v[84:87]\n"
"ds_read_b64_tr_b16 a[82:83], v245 offset:4384\n"
"ds_read_b64_tr_b16 a[88:89], v245 offset:264\n"
"v_mfma_f32_16x16x32_bf16 v[88:91], a[112:115], v[200:203], v[88:91]\n"
"s_add_u32 m0, s54, 34968\n"
"v_exp_f32 v194, v194\n"
"v_mfma_f32_16x16x32_bf16 v[88:91], a[116:119], v[204:207], v[88:91]\n"
"buffer_load_dwordx4 v248, s[40:43], s52 offen offset:1152 lds\n"
"v_mfma_f32_16x16x32_bf16 v[92:95], a[124:127], v[204:207], v[92:95]\n"
"s_add_u32 s52, s52, 16384\n"
"v_exp_f32 v195, v195\n"
"v_mfma_f32_16x16x32_bf16 v[92:95], a[120:123], v[200:203], v[92:95]\n"
"v_perm_b32 v200, v193, v192, s72\n"
"v_perm_b32 v201, v195, v194, s72\n"
"v_mfma_f32_16x16x32_bf16 v[224:227], a[32:35], v[128:131], -4.0\n"
"v_exp_f32 v196, v196\n"
"v_mfma_f32_16x16x32_bf16 v[224:227], a[36:39], v[132:135], v[224:227]\n"
"s_waitcnt lgkmcnt(4)\n"
"ds_read_b64_tr_b16 a[90:91], v245 offset:4392\n"
"ds_read_b64_tr_b16 a[96:97], v245 offset:2064\n"
"v_mfma_f32_16x16x32_bf16 v[224:227], a[40:43], v[136:139], v[224:227]\n"
"v_exp_f32 v197, v197\n"
"v_mfma_f32_16x16x32_bf16 v[224:227], a[44:47], v[140:143], v[224:227]\n"
"ds_read_b64_tr_b16 a[98:99], v245 offset:6192\n"
"v_perm_b32 v202, v197, v196, s72\n"
"v_mfma_f32_16x16x32_bf16 v[228:231], a[60:63], v[140:143], -4.0\n"
"v_exp_f32 v198, v198\n"
"v_mfma_f32_16x16x32_bf16 v[228:231], a[56:59], v[136:139], v[228:231]\n"
"ds_read_b64_tr_b16 a[104:105], v245 offset:2072\n"
"ds_read_b64_tr_b16 a[106:107], v245 offset:6200\n"
"v_mfma_f32_16x16x32_bf16 v[228:231], a[52:55], v[132:135], v[228:231]\n"
"v_exp_f32 v199, v199\n"
"v_mfma_f32_16x16x32_bf16 v[228:231], a[48:51], v[128:131], v[228:231]\n"
"ds_read_b64_tr_b16 a[112:113], v245 offset:2320\n"
"v_perm_b32 v203, v199, v198, s72\n"
"v_mfma_f32_16x16x32_bf16 a[140:143], a[144:147], v[212:215], a[140:143]\n"
"ds_read_b64_tr_b16 a[114:115], v245 offset:6448\n"
"ds_read_b64_tr_b16 a[120:121], v245 offset:2328\n"
"v_mfma_f32_16x16x32_bf16 v[96:99], a[68:71], v[212:215], v[96:99]\n"
"v_exp_f32 v224, v224\n"
"v_mfma_f32_16x16x32_bf16 v[100:103], a[76:79], v[212:215], v[100:103]\n"
"ds_read_b64_tr_b16 a[68:69], v255 offset:0\n"
"ds_read_b64_tr_b16 a[70:71], v255 offset:4128\n"
"v_mfma_f32_16x16x32_bf16 v[104:107], a[84:87], v[212:215], v[104:107]\n"
"v_exp_f32 v225, v225\n"
"v_mfma_f32_16x16x32_bf16 v[108:111], a[92:95], v[212:215], v[108:111]\n"
"ds_read_b64_tr_b16 a[76:77], v255 offset:8\n"
"v_perm_b32 v204, v225, v224, s72\n"
"v_mfma_f32_16x16x32_bf16 v[112:115], a[100:103], v[212:215], v[112:115]\n"
"v_exp_f32 v226, v226\n"
"v_mfma_f32_16x16x32_bf16 v[116:119], a[108:111], v[212:215], v[116:119]\n"
"ds_read_b64_tr_b16 a[78:79], v255 offset:4136\n"
"ds_read_b64_tr_b16 a[84:85], v255 offset:256\n"
"v_mfma_f32_16x16x32_bf16 v[120:123], a[116:119], v[212:215], v[120:123]\n"
"v_exp_f32 v227, v227\n"
"v_mfma_f32_16x16x32_bf16 v[124:127], a[124:127], v[212:215], v[124:127]\n"
"ds_read_b64_tr_b16 a[86:87], v255 offset:4384\n"
"v_perm_b32 v205, v227, v226, s72\n"
"v_mfma_f32_16x16x32_bf16 v[192:195], a[0:3], v[144:147], -4.0\n"
"v_exp_f32 v228, v228\n"
"v_mfma_f32_16x16x32_bf16 v[192:195], a[4:7], v[148:151], v[192:195]\n"
"ds_read_b64_tr_b16 a[92:93], v255 offset:264\n"
"ds_read_b64_tr_b16 a[94:95], v255 offset:4392\n"
"v_mfma_f32_16x16x32_bf16 v[192:195], a[8:11], v[152:155], v[192:195]\n"
"v_exp_f32 v229, v229\n"
"v_mfma_f32_16x16x32_bf16 v[192:195], a[12:15], v[156:159], v[192:195]\n"
"ds_read_b64_tr_b16 a[100:101], v255 offset:2064\n"
"v_perm_b32 v206, v229, v228, s72\n"
"v_mfma_f32_16x16x32_bf16 v[196:199], a[28:31], v[156:159], -4.0\n"
"v_exp_f32 v230, v230\n"
"v_mfma_f32_16x16x32_bf16 v[196:199], a[24:27], v[152:155], v[196:199]\n"
"ds_read_b64_tr_b16 a[102:103], v255 offset:6192\n"
"ds_read_b64_tr_b16 a[108:109], v255 offset:2072\n"
"v_mfma_f32_16x16x32_bf16 v[196:199], a[20:23], v[148:151], v[196:199]\n"
"v_exp_f32 v231, v231\n"
"v_mfma_f32_16x16x32_bf16 v[196:199], a[16:19], v[144:147], v[196:199]\n"
"buffer_store_dwordx4 a[228:231], v250, s[48:51], s84 offen offset:1152 sc1\n"
"v_perm_b32 v207, v231, v230, s72\n"
"v_mfma_f32_16x16x32_bf16 v[224:227], a[32:35], v[144:147], -4.0\n"
"s_mov_b32 s84, s85\n"
"v_exp_f32 v192, v192\n"
"v_mfma_f32_16x16x32_bf16 v[224:227], a[36:39], v[148:151], v[224:227]\n"
"ds_read_b64_tr_b16 a[110:111], v255 offset:6200\n"
"ds_read_b64_tr_b16 a[116:117], v255 offset:2320\n"
"v_mfma_f32_16x16x32_bf16 v[224:227], a[40:43], v[152:155], v[224:227]\n"
"v_exp_f32 v193, v193\n"
"v_mfma_f32_16x16x32_bf16 v[224:227], a[44:47], v[156:159], v[224:227]\n"
"ds_read_b64_tr_b16 a[118:119], v255 offset:6448\n"
"v_perm_b32 v208, v193, v192, s72\n"
"v_mfma_f32_16x16x32_bf16 v[228:231], a[60:63], v[156:159], -4.0\n"
"v_exp_f32 v194, v194\n"
"v_mfma_f32_16x16x32_bf16 v[228:231], a[56:59], v[152:155], v[228:231]\n"
"ds_read_b64_tr_b16 a[124:125], v255 offset:2328\n"
"ds_read_b64_tr_b16 a[126:127], v255 offset:6456\n"
"v_mfma_f32_16x16x32_bf16 v[228:231], a[52:55], v[148:151], v[228:231]\n"
"v_exp_f32 v195, v195\n"
"v_mfma_f32_16x16x32_bf16 v[228:231], a[48:51], v[144:147], v[228:231]\n"
"ds_read_b64_tr_b16 a[122:123], v245 offset:6456\n"
"v_perm_b32 v209, v195, v194, s72\n"
"v_smfmac_f32_16x16x64_bf16 a[128:131], a[144:147], v[200:207], v251\n"
"v_mfma_f32_16x16x32_bf16 v[0:3], a[64:67], v[200:203], v[0:3]\n"
"s_waitcnt lgkmcnt(6)\n"
"v_exp_f32 v196, v196\n"
"v_mfma_f32_16x16x32_bf16 v[0:3], a[68:71], v[204:207], v[0:3]\n"
"v_mfma_f32_16x16x32_bf16 v[4:7], a[76:79], v[204:207], v[4:7]\n"
"v_exp_f32 v197, v197\n"
"v_mfma_f32_16x16x32_bf16 v[4:7], a[72:75], v[200:203], v[4:7]\n"
"v_perm_b32 v210, v197, v196, s72\n"
"v_mfma_f32_16x16x32_bf16 v[8:11], a[80:83], v[200:203], v[8:11]\n"
"v_exp_f32 v198, v198\n"
"v_mfma_f32_16x16x32_bf16 v[8:11], a[84:87], v[204:207], v[8:11]\n"
"v_mfma_f32_16x16x32_bf16 v[12:15], a[92:95], v[204:207], v[12:15]\n"
"v_exp_f32 v199, v199\n"
"v_mfma_f32_16x16x32_bf16 v[12:15], a[88:91], v[200:203], v[12:15]\n"
"s_waitcnt vmcnt(11)\n"
"ds_read2_b64 a[184:187], v246 offset0:0 offset1:2\n"
"v_perm_b32 v211, v199, v198, s72\n"
"v_mfma_f32_16x16x32_bf16 v[16:19], a[96:99], v[200:203], v[16:19]\n"
"v_exp_f32 v224, v224\n"
"v_mfma_f32_16x16x32_bf16 v[16:19], a[100:103], v[204:207], v[16:19]\n"
"s_waitcnt lgkmcnt(1)\n"
"ds_read2_b64 a[188:191], v246 offset0:1 offset1:3\n"
"ds_read2_b64 a[192:195], v246 offset0:32 offset1:34\n"
"v_mfma_f32_16x16x32_bf16 v[20:23], a[108:111], v[204:207], v[20:23]\n"
"v_exp_f32 v225, v225\n"
"v_mfma_f32_16x16x32_bf16 v[20:23], a[104:107], v[200:203], v[20:23]\n"
"ds_read2_b64 a[196:199], v246 offset0:33 offset1:35\n"
"v_perm_b32 v212, v225, v224, s72\n"
"v_mfma_f32_16x16x32_bf16 v[24:27], a[112:115], v[200:203], v[24:27]\n"
"s_add_u32 m0, s53, 52224\n"
"v_exp_f32 v226, v226\n"
"v_mfma_f32_16x16x32_bf16 v[24:27], a[116:119], v[204:207], v[24:27]\n"
"buffer_load_dwordx4 v247, s[36:39], s52 offen offset:0 lds\n"
"v_mfma_f32_16x16x32_bf16 v[28:31], a[124:127], v[204:207], v[28:31]\n"
"v_exp_f32 v227, v227\n"
"v_mfma_f32_16x16x32_bf16 v[28:31], a[120:123], v[200:203], v[28:31]\n"
"ds_read2_b64 a[200:203], v246 offset0:64 offset1:66\n"
"v_perm_b32 v213, v227, v226, s72\n"
"v_mfma_f32_16x16x32_bf16 v[192:195], a[0:3], v[160:163], -4.0\n"
"v_exp_f32 v228, v228\n"
"v_mfma_f32_16x16x32_bf16 v[192:195], a[4:7], v[164:167], v[192:195]\n"
"ds_read2_b64 a[204:207], v246 offset0:65 offset1:67\n"
"ds_read2_b64 a[208:211], v246 offset0:96 offset1:98\n"
"v_mfma_f32_16x16x32_bf16 v[192:195], a[8:11], v[168:171], v[192:195]\n"
"v_exp_f32 v229, v229\n"
"v_mfma_f32_16x16x32_bf16 v[192:195], a[12:15], v[172:175], v[192:195]\n"
"ds_read2_b64 a[212:215], v246 offset0:97 offset1:99\n"
"v_perm_b32 v214, v229, v228, s72\n"
"v_mfma_f32_16x16x32_bf16 v[196:199], a[28:31], v[172:175], -4.0\n"
"s_add_u32 m0, s53, 52288\n"
"v_exp_f32 v230, v230\n"
"v_mfma_f32_16x16x32_bf16 v[196:199], a[24:27], v[168:171], v[196:199]\n"
"buffer_load_dwordx4 v247, s[36:39], s52 offen offset:1024 lds\n"
"v_mfma_f32_16x16x32_bf16 v[196:199], a[20:23], v[164:167], v[196:199]\n"
"v_exp_f32 v231, v231\n"
"v_mfma_f32_16x16x32_bf16 v[196:199], a[16:19], v[160:163], v[196:199]\n"
"v_perm_b32 v215, v231, v230, s72\n"
"v_mfma_f32_16x16x32_bf16 v[224:227], a[32:35], v[160:163], -4.0\n"
"s_add_u32 m0, s53, 52352\n"
"v_exp_f32 v192, v192\n"
"v_mfma_f32_16x16x32_bf16 v[224:227], a[36:39], v[164:167], v[224:227]\n"
"buffer_load_dwordx4 v247, s[36:39], s52 offen offset:2048 lds\n"
"v_mfma_f32_16x16x32_bf16 v[224:227], a[40:43], v[168:171], v[224:227]\n"
"v_exp_f32 v193, v193\n"
"v_mfma_f32_16x16x32_bf16 v[224:227], a[44:47], v[172:175], v[224:227]\n"
"v_perm_b32 v200, v193, v192, s72\n"
"v_mfma_f32_16x16x32_bf16 v[228:231], a[60:63], v[172:175], -4.0\n"
"s_add_u32 m0, s53, 52416\n"
"v_exp_f32 v194, v194\n"
"v_mfma_f32_16x16x32_bf16 v[228:231], a[56:59], v[168:171], v[228:231]\n"
"buffer_load_dwordx4 v247, s[36:39], s52 offen offset:3072 lds\n"
"v_mfma_f32_16x16x32_bf16 v[228:231], a[52:55], v[164:167], v[228:231]\n"
"v_exp_f32 v195, v195\n"
"v_mfma_f32_16x16x32_bf16 v[228:231], a[48:51], v[160:163], v[228:231]\n"
"s_waitcnt vmcnt(12) lgkmcnt(8)\n"
"v_perm_b32 v201, v195, v194, s72\n"
"v_smfmac_f32_16x16x64_bf16 a[132:135], a[144:147], v[208:215], v251\n"
"s_cmp_lt_u32 s56, 3\n"
"v_mfma_f32_16x16x32_bf16 v[32:35], a[64:67], v[208:211], v[32:35]\n"
"s_cselect_b32 s38, s38, 0\n"
"v_exp_f32 v196, v196\n"
"v_mfma_f32_16x16x32_bf16 v[32:35], a[68:71], v[212:215], v[32:35]\n"
"s_barrier\n"
"ds_read_b128 a[0:3], v244 offset:17408\n"
"ds_read_b128 a[4:7], v244 offset:17472\n"
"v_mfma_f32_16x16x32_bf16 v[36:39], a[76:79], v[212:215], v[36:39]\n"
"v_exp_f32 v197, v197\n"
"v_mfma_f32_16x16x32_bf16 v[36:39], a[72:75], v[208:211], v[36:39]\n"
"ds_read_b128 a[8:11], v244 offset:17536\n"
"v_perm_b32 v202, v197, v196, s72\n"
"v_mfma_f32_16x16x32_bf16 v[40:43], a[80:83], v[208:211], v[40:43]\n"
"s_add_u32 m0, s54, 49536\n"
"v_exp_f32 v198, v198\n"
"v_mfma_f32_16x16x32_bf16 v[40:43], a[84:87], v[212:215], v[40:43]\n"
"buffer_load_dwordx4 v248, s[40:43], s52 offen offset:0 lds\n"
"v_mfma_f32_16x16x32_bf16 v[44:47], a[92:95], v[212:215], v[44:47]\n"
"v_exp_f32 v199, v199\n"
"v_mfma_f32_16x16x32_bf16 v[44:47], a[88:91], v[208:211], v[44:47]\n"
"ds_read_b128 a[12:15], v244 offset:17600\n"
"v_perm_b32 v203, v199, v198, s72\n"
"v_mfma_f32_16x16x32_bf16 v[48:51], a[96:99], v[208:211], v[48:51]\n"
"v_exp_f32 v224, v224\n"
"v_mfma_f32_16x16x32_bf16 v[48:51], a[100:103], v[212:215], v[48:51]\n"
"ds_read_b128 a[28:31], v244 offset:21952\n"
"ds_read_b128 a[24:27], v244 offset:21888\n"
"v_mfma_f32_16x16x32_bf16 v[52:55], a[108:111], v[212:215], v[52:55]\n"
"v_exp_f32 v225, v225\n"
"v_mfma_f32_16x16x32_bf16 v[52:55], a[104:107], v[208:211], v[52:55]\n"
"ds_read_b128 a[20:23], v244 offset:21824\n"
"v_perm_b32 v204, v225, v224, s72\n"
"v_mfma_f32_16x16x32_bf16 v[56:59], a[112:115], v[208:211], v[56:59]\n"
"s_add_u32 m0, s55, 0\n"
"v_exp_f32 v226, v226\n"
"v_mfma_f32_16x16x32_bf16 v[56:59], a[116:119], v[212:215], v[56:59]\n"
"s_waitcnt lgkmcnt(7)\n"
"buffer_load_dwordx4 v249, s[44:47], s76 offen offset:0 nt lds\n"
"v_mfma_f32_16x16x32_bf16 v[60:63], a[124:127], v[212:215], v[60:63]\n"
"v_exp_f32 v227, v227\n"
"v_mfma_f32_16x16x32_bf16 v[60:63], a[120:123], v[208:211], v[60:63]\n"
"ds_read_b128 a[16:19], v244 offset:21760\n"
"v_perm_b32 v205, v227, v226, s72\n"
"v_mfma_f32_16x16x32_bf16 v[192:195], a[32:35], v[176:179], -4.0\n"
"v_exp_f32 v228, v228\n"
"v_mfma_f32_16x16x32_bf16 v[192:195], a[36:39], v[180:183], v[192:195]\n"
"ds_read_b128 a[32:35], v222 offset:17408\n"
"ds_read_b128 a[36:39], v222 offset:17472\n"
"v_mfma_f32_16x16x32_bf16 v[192:195], a[40:43], v[184:187], v[192:195]\n"
"v_exp_f32 v229, v229\n"
"v_mfma_f32_16x16x32_bf16 v[192:195], a[44:47], v[188:191], v[192:195]\n"
"ds_read_b128 a[40:43], v222 offset:17536\n"
"v_perm_b32 v206, v229, v228, s72\n"
"v_mfma_f32_16x16x32_bf16 v[224:227], a[60:63], v[188:191], -4.0\n"
"s_add_u32 m0, s55, 8\n"
"v_exp_f32 v230, v230\n"
"v_mfma_f32_16x16x32_bf16 v[224:227], a[56:59], v[184:187], v[224:227]\n"
"buffer_load_dwordx4 v249, s[44:47], s76 offen offset:1024 nt lds\n"
"v_mfma_f32_16x16x32_bf16 v[224:227], a[52:55], v[180:183], v[224:227]\n"
"v_exp_f32 v231, v231\n"
"v_mfma_f32_16x16x32_bf16 v[224:227], a[48:51], v[176:179], v[224:227]\n"
"ds_read_b128 a[44:47], v222 offset:17600\n"
"v_perm_b32 v207, v231, v230, s72\n"
"v_mfma_f32_16x16x32_bf16 v[64:67], a[64:67], v[200:203], v[64:67]\n"
"v_exp_f32 v192, v192\n"
"v_mfma_f32_16x16x32_bf16 v[64:67], a[68:71], v[204:207], v[64:67]\n"
"ds_read_b128 a[60:63], v222 offset:21952\n"
"ds_read_b128 a[56:59], v222 offset:21888\n"
"v_mfma_f32_16x16x32_bf16 v[68:71], a[76:79], v[204:207], v[68:71]\n"
"v_exp_f32 v193, v193\n"
"v_mfma_f32_16x16x32_bf16 v[68:71], a[72:75], v[200:203], v[68:71]\n"
"ds_read_b128 a[52:55], v222 offset:21824\n"
"v_perm_b32 v212, v193, v192, s72\n"
"v_mfma_f32_16x16x32_bf16 v[72:75], a[80:83], v[200:203], v[72:75]\n"
"s_add_u32 m0, s55, 16\n"
"v_exp_f32 v194, v194\n"
"v_mfma_f32_16x16x32_bf16 v[72:75], a[84:87], v[204:207], v[72:75]\n"
"buffer_load_dwordx4 v249, s[44:47], s76 offen offset:2048 nt lds\n"
"v_mfma_f32_16x16x32_bf16 v[76:79], a[92:95], v[204:207], v[76:79]\n"
"v_exp_f32 v195, v195\n"
"v_mfma_f32_16x16x32_bf16 v[76:79], a[88:91], v[200:203], v[76:79]\n"
"ds_read_b128 a[48:51], v222 offset:21760\n"
"v_perm_b32 v213, v195, v194, s72\n"
"v_smfmac_f32_16x16x64_bf16 a[136:139], a[144:147], v[200:207], v251\n"
"s_waitcnt lgkmcnt(5)\n"
"ds_read_b64_tr_b16 a[64:65], v245 offset:16512\n"
"v_mfma_f32_16x16x32_bf16 v[192:195], a[0:3], v[128:131], -4.0\n"
"v_exp_f32 v224, v224\n"
"v_mfma_f32_16x16x32_bf16 v[192:195], a[4:7], v[132:135], v[192:195]\n"
"ds_read_b64_tr_b16 a[66:67], v245 offset:20640\n"
"ds_read_b64_tr_b16 a[72:73], v245 offset:16520\n"
"v_mfma_f32_16x16x32_bf16 v[192:195], a[8:11], v[136:139], v[192:195]\n"
"v_exp_f32 v225, v225\n"
"v_mfma_f32_16x16x32_bf16 v[192:195], a[12:15], v[140:143], v[192:195]\n"
"ds_read_b64_tr_b16 a[74:75], v245 offset:20648\n"
"v_perm_b32 v214, v225, v224, s72\n"
"v_mfma_f32_16x16x32_bf16 v[196:199], a[28:31], v[140:143], -4.0\n"
"s_add_u32 m0, s55, 24\n"
"v_exp_f32 v226, v226\n"
"v_mfma_f32_16x16x32_bf16 v[196:199], a[24:27], v[136:139], v[196:199]\n"
"buffer_load_dwordx4 v249, s[44:47], s76 offen offset:3072 nt lds\n"
"v_mfma_f32_16x16x32_bf16 v[196:199], a[20:23], v[132:135], v[196:199]\n"
"s_mov_b32 s76, s77\n"
"v_exp_f32 v227, v227\n"
"v_mfma_f32_16x16x32_bf16 v[196:199], a[16:19], v[128:131], v[196:199]\n"
"ds_read_b64_tr_b16 a[80:81], v245 offset:16768\n"
"v_perm_b32 v215, v227, v226, s72\n"
"v_mfma_f32_16x16x32_bf16 v[80:83], a[96:99], v[200:203], v[80:83]\n"
"v_exp_f32 v192, v192\n"
"v_mfma_f32_16x16x32_bf16 v[80:83], a[100:103], v[204:207], v[80:83]\n"
"ds_read_b64_tr_b16 a[82:83], v245 offset:20896\n"
"ds_read_b64_tr_b16 a[88:89], v245 offset:16776\n"
"v_mfma_f32_16x16x32_bf16 v[84:87], a[108:111], v[204:207], v[84:87]\n"
"v_exp_f32 v193, v193\n"
"v_mfma_f32_16x16x32_bf16 v[84:87], a[104:107], v[200:203], v[84:87]\n"
"ds_read_b64_tr_b16 a[90:91], v245 offset:20904\n"
"ds_read_b64_tr_b16 a[96:97], v245 offset:18576\n"
"v_mfma_f32_16x16x32_bf16 v[88:91], a[112:115], v[200:203], v[88:91]\n"
"s_add_u32 m0, s54, 49544\n"
"v_exp_f32 v194, v194\n"
"v_mfma_f32_16x16x32_bf16 v[88:91], a[116:119], v[204:207], v[88:91]\n"
"buffer_load_dwordx4 v248, s[40:43], s52 offen offset:1024 lds\n"
"v_mfma_f32_16x16x32_bf16 v[92:95], a[124:127], v[204:207], v[92:95]\n"
"v_exp_f32 v195, v195\n"
"v_mfma_f32_16x16x32_bf16 v[92:95], a[120:123], v[200:203], v[92:95]\n"
"v_perm_b32 v200, v193, v192, s72\n"
"v_perm_b32 v201, v195, v194, s72\n"
"v_mfma_f32_16x16x32_bf16 v[224:227], a[32:35], v[128:131], -4.0\n"
"v_exp_f32 v196, v196\n"
"v_mfma_f32_16x16x32_bf16 v[224:227], a[36:39], v[132:135], v[224:227]\n"
"ds_read_b64_tr_b16 a[98:99], v245 offset:22704\n"
"ds_read_b64_tr_b16 a[104:105], v245 offset:18584\n"
"v_mfma_f32_16x16x32_bf16 v[224:227], a[40:43], v[136:139], v[224:227]\n"
"s_waitcnt lgkmcnt(6)\n"
"v_exp_f32 v197, v197\n"
"v_mfma_f32_16x16x32_bf16 v[224:227], a[44:47], v[140:143], v[224:227]\n"
"ds_read_b64_tr_b16 a[106:107], v245 offset:22712\n"
"v_perm_b32 v202, v197, v196, s72\n"
"v_mfma_f32_16x16x32_bf16 v[228:231], a[60:63], v[140:143], -4.0\n"
"s_add_u32 m0, s54, 51472\n"
"v_exp_f32 v198, v198\n"
"v_mfma_f32_16x16x32_bf16 v[228:231], a[56:59], v[136:139], v[228:231]\n"
"buffer_load_dwordx4 v248, s[40:43], s52 offen offset:128 lds\n"
"v_mfma_f32_16x16x32_bf16 v[228:231], a[52:55], v[132:135], v[228:231]\n"
"v_exp_f32 v199, v199\n"
"v_mfma_f32_16x16x32_bf16 v[228:231], a[48:51], v[128:131], v[228:231]\n"
"ds_read_b64_tr_b16 a[112:113], v245 offset:18832\n"
"v_perm_b32 v203, v199, v198, s72\n"
"v_mfma_f32_16x16x32_bf16 a[140:143], a[144:147], v[212:215], a[140:143]\n"
"ds_read_b64_tr_b16 a[114:115], v245 offset:22960\n"
"ds_read_b64_tr_b16 a[120:121], v245 offset:18840\n"
"v_mfma_f32_16x16x32_bf16 v[96:99], a[68:71], v[212:215], v[96:99]\n"
"v_exp_f32 v224, v224\n"
"v_mfma_f32_16x16x32_bf16 v[100:103], a[76:79], v[212:215], v[100:103]\n"
"ds_read_b64_tr_b16 a[68:69], v255 offset:16512\n"
"ds_read_b64_tr_b16 a[70:71], v255 offset:20640\n"
"v_mfma_f32_16x16x32_bf16 v[104:107], a[84:87], v[212:215], v[104:107]\n"
"v_exp_f32 v225, v225\n"
"v_mfma_f32_16x16x32_bf16 v[108:111], a[92:95], v[212:215], v[108:111]\n"
"ds_read_b64_tr_b16 a[76:77], v255 offset:16520\n"
"v_perm_b32 v204, v225, v224, s72\n"
"v_mfma_f32_16x16x32_bf16 v[112:115], a[100:103], v[212:215], v[112:115]\n"
"v_exp_f32 v226, v226\n"
"v_mfma_f32_16x16x32_bf16 v[116:119], a[108:111], v[212:215], v[116:119]\n"
"ds_read_b64_tr_b16 a[78:79], v255 offset:20648\n"
"ds_read_b64_tr_b16 a[84:85], v255 offset:16768\n"
"v_mfma_f32_16x16x32_bf16 v[120:123], a[116:119], v[212:215], v[120:123]\n"
"v_exp_f32 v227, v227\n"
"v_mfma_f32_16x16x32_bf16 v[124:127], a[124:127], v[212:215], v[124:127]\n"
"ds_read_b64_tr_b16 a[86:87], v255 offset:20896\n"
"v_perm_b32 v205, v227, v226, s72\n"
"v_mfma_f32_16x16x32_bf16 v[192:195], a[0:3], v[144:147], -4.0\n"
"v_exp_f32 v228, v228\n"
"v_mfma_f32_16x16x32_bf16 v[192:195], a[4:7], v[148:151], v[192:195]\n"
"ds_read_b64_tr_b16 a[92:93], v255 offset:16776\n"
"ds_read_b64_tr_b16 a[94:95], v255 offset:20904\n"
"v_mfma_f32_16x16x32_bf16 v[192:195], a[8:11], v[152:155], v[192:195]\n"
"v_exp_f32 v229, v229\n"
"v_mfma_f32_16x16x32_bf16 v[192:195], a[12:15], v[156:159], v[192:195]\n"
"ds_read_b64_tr_b16 a[100:101], v255 offset:18576\n"
"v_perm_b32 v206, v229, v228, s72\n"
"v_mfma_f32_16x16x32_bf16 v[196:199], a[28:31], v[156:159], -4.0\n"
"v_exp_f32 v230, v230\n"
"v_mfma_f32_16x16x32_bf16 v[196:199], a[24:27], v[152:155], v[196:199]\n"
"ds_read_b64_tr_b16 a[102:103], v255 offset:22704\n"
"ds_read_b64_tr_b16 a[108:109], v255 offset:18584\n"
"v_mfma_f32_16x16x32_bf16 v[196:199], a[20:23], v[148:151], v[196:199]\n"
"v_exp_f32 v231, v231\n"
"v_mfma_f32_16x16x32_bf16 v[196:199], a[16:19], v[144:147], v[196:199]\n"
"ds_read_b64_tr_b16 a[110:111], v255 offset:22712\n"
"v_perm_b32 v207, v231, v230, s72\n"
"v_mfma_f32_16x16x32_bf16 v[224:227], a[32:35], v[144:147], -4.0\n"
"v_exp_f32 v192, v192\n"
"v_mfma_f32_16x16x32_bf16 v[224:227], a[36:39], v[148:151], v[224:227]\n"
"ds_read_b64_tr_b16 a[116:117], v255 offset:18832\n"
"ds_read_b64_tr_b16 a[118:119], v255 offset:22960\n"
"v_mfma_f32_16x16x32_bf16 v[224:227], a[40:43], v[152:155], v[224:227]\n"
"v_exp_f32 v193, v193\n"
"v_mfma_f32_16x16x32_bf16 v[224:227], a[44:47], v[156:159], v[224:227]\n"
"ds_read_b64_tr_b16 a[124:125], v255 offset:18840\n"
"v_perm_b32 v208, v193, v192, s72\n"
"v_mfma_f32_16x16x32_bf16 v[228:231], a[60:63], v[156:159], -4.0\n"
"v_exp_f32 v194, v194\n"
"v_mfma_f32_16x16x32_bf16 v[228:231], a[56:59], v[152:155], v[228:231]\n"
"ds_read_b64_tr_b16 a[126:127], v255 offset:22968\n"
"ds_read_b64_tr_b16 a[122:123], v245 offset:22968\n"
"v_mfma_f32_16x16x32_bf16 v[228:231], a[52:55], v[148:151], v[228:231]\n"
"v_exp_f32 v195, v195\n"
"v_mfma_f32_16x16x32_bf16 v[228:231], a[48:51], v[144:147], v[228:231]\n"
"v_perm_b32 v209, v195, v194, s72\n"
"v_smfmac_f32_16x16x64_bf16 a[128:131], a[144:147], v[200:207], v251\n"
"v_mfma_f32_16x16x32_bf16 v[0:3], a[64:67], v[200:203], v[0:3]\n"
"s_waitcnt lgkmcnt(6)\n"
"v_exp_f32 v196, v196\n"
"v_mfma_f32_16x16x32_bf16 v[0:3], a[68:71], v[204:207], v[0:3]\n"
"v_mfma_f32_16x16x32_bf16 v[4:7], a[76:79], v[204:207], v[4:7]\n"
"v_exp_f32 v197, v197\n"
"v_mfma_f32_16x16x32_bf16 v[4:7], a[72:75], v[200:203], v[4:7]\n"
"v_perm_b32 v210, v197, v196, s72\n"
"v_mfma_f32_16x16x32_bf16 v[8:11], a[80:83], v[200:203], v[8:11]\n"
"v_exp_f32 v198, v198\n"
"v_mfma_f32_16x16x32_bf16 v[8:11], a[84:87], v[204:207], v[8:11]\n"
"v_mfma_f32_16x16x32_bf16 v[12:15], a[92:95], v[204:207], v[12:15]\n"
"v_exp_f32 v199, v199\n"
"v_mfma_f32_16x16x32_bf16 v[12:15], a[88:91], v[200:203], v[12:15]\n"
"v_perm_b32 v211, v199, v198, s72\n"
"v_mfma_f32_16x16x32_bf16 v[16:19], a[96:99], v[200:203], v[16:19]\n"
"v_exp_f32 v224, v224\n"
"v_mfma_f32_16x16x32_bf16 v[16:19], a[100:103], v[204:207], v[16:19]\n"
"s_waitcnt lgkmcnt(0)\n"
"v_mfma_f32_16x16x32_bf16 v[20:23], a[108:111], v[204:207], v[20:23]\n"
"v_exp_f32 v225, v225\n"
"v_mfma_f32_16x16x32_bf16 v[20:23], a[104:107], v[200:203], v[20:23]\n"
"v_perm_b32 v212, v225, v224, s72\n"
"v_mfma_f32_16x16x32_bf16 v[24:27], a[112:115], v[200:203], v[24:27]\n"
"s_add_u32 m0, s54, 51480\n"
"v_exp_f32 v226, v226\n"
"v_mfma_f32_16x16x32_bf16 v[24:27], a[116:119], v[204:207], v[24:27]\n"
"buffer_load_dwordx4 v248, s[40:43], s52 offen offset:1152 lds\n"
"v_mfma_f32_16x16x32_bf16 v[28:31], a[124:127], v[204:207], v[28:31]\n"
"s_add_u32 s52, s52, 4286464000\n"
"v_exp_f32 v227, v227\n"
"v_mfma_f32_16x16x32_bf16 v[28:31], a[120:123], v[200:203], v[28:31]\n"
"s_mov_b32 s42, s38\n"
"buffer_store_dwordx4 a[232:235], v250, s[48:51], s84 offen offset:0 sc1\n"
"v_perm_b32 v213, v227, v226, s72\n"
"v_mfma_f32_16x16x32_bf16 v[192:195], a[0:3], v[160:163], -4.0\n"
"v_exp_f32 v228, v228\n"
"v_mfma_f32_16x16x32_bf16 v[192:195], a[4:7], v[164:167], v[192:195]\n"
"v_mfma_f32_16x16x32_bf16 v[192:195], a[8:11], v[168:171], v[192:195]\n"
"v_exp_f32 v229, v229\n"
"v_mfma_f32_16x16x32_bf16 v[192:195], a[12:15], v[172:175], v[192:195]\n"
"v_perm_b32 v214, v229, v228, s72\n"
"v_mfma_f32_16x16x32_bf16 v[196:199], a[28:31], v[172:175], -4.0\n"
"s_add_u32 m0, s53, 0\n"
"v_exp_f32 v230, v230\n"
"v_mfma_f32_16x16x32_bf16 v[196:199], a[24:27], v[168:171], v[196:199]\n"
"buffer_load_dwordx4 v247, s[36:39], s52 offen offset:0 lds\n"
"v_mfma_f32_16x16x32_bf16 v[196:199], a[20:23], v[164:167], v[196:199]\n"
"v_exp_f32 v231, v231\n"
"v_mfma_f32_16x16x32_bf16 v[196:199], a[16:19], v[160:163], v[196:199]\n"
"v_perm_b32 v215, v231, v230, s72\n"
"v_mfma_f32_16x16x32_bf16 v[224:227], a[32:35], v[160:163], -4.0\n"
"v_exp_f32 v192, v192\n"
"v_mfma_f32_16x16x32_bf16 v[224:227], a[36:39], v[164:167], v[224:227]\n"
"v_mfma_f32_16x16x32_bf16 v[224:227], a[40:43], v[168:171], v[224:227]\n"
"v_exp_f32 v193, v193\n"
"v_mfma_f32_16x16x32_bf16 v[224:227], a[44:47], v[172:175], v[224:227]\n"
"v_perm_b32 v200, v193, v192, s72\n"
"v_mfma_f32_16x16x32_bf16 v[228:231], a[60:63], v[172:175], -4.0\n"
"s_add_u32 m0, s53, 64\n"
"v_exp_f32 v194, v194\n"
"v_mfma_f32_16x16x32_bf16 v[228:231], a[56:59], v[168:171], v[228:231]\n"
"buffer_load_dwordx4 v247, s[36:39], s52 offen offset:1024 lds\n"
"v_mfma_f32_16x16x32_bf16 v[228:231], a[52:55], v[164:167], v[228:231]\n"
"v_exp_f32 v195, v195\n"
"v_mfma_f32_16x16x32_bf16 v[228:231], a[48:51], v[160:163], v[228:231]\n"
"s_waitcnt vmcnt(14) lgkmcnt(0)\n"
"v_perm_b32 v201, v195, v194, s72\n"
"v_smfmac_f32_16x16x64_bf16 a[132:135], a[144:147], v[208:215], v251\n"
"v_mfma_f32_16x16x32_bf16 v[32:35], a[64:67], v[208:211], v[32:35]\n"
"v_exp_f32 v196, v196\n"
"v_mfma_f32_16x16x32_bf16 v[32:35], a[68:71], v[212:215], v[32:35]\n"
"s_barrier\n"
"ds_read_b128 a[0:3], v244 offset:34816\n"
"ds_read_b128 a[4:7], v244 offset:34880\n"
"v_mfma_f32_16x16x32_bf16 v[36:39], a[76:79], v[212:215], v[36:39]\n"
"v_exp_f32 v197, v197\n"
"v_mfma_f32_16x16x32_bf16 v[36:39], a[72:75], v[208:211], v[36:39]\n"
"ds_read_b128 a[8:11], v244 offset:34944\n"
"v_perm_b32 v202, v197, v196, s72\n"
"v_mfma_f32_16x16x32_bf16 v[40:43], a[80:83], v[208:211], v[40:43]\n"
"s_add_u32 m0, s53, 128\n"
"v_exp_f32 v198, v198\n"
"v_mfma_f32_16x16x32_bf16 v[40:43], a[84:87], v[212:215], v[40:43]\n"
"buffer_load_dwordx4 v247, s[36:39], s52 offen offset:2048 lds\n"
"v_mfma_f32_16x16x32_bf16 v[44:47], a[92:95], v[212:215], v[44:47]\n"
"v_exp_f32 v199, v199\n"
"v_mfma_f32_16x16x32_bf16 v[44:47], a[88:91], v[208:211], v[44:47]\n"
"ds_read_b128 a[12:15], v244 offset:35008\n"
"v_perm_b32 v203, v199, v198, s72\n"
"v_mfma_f32_16x16x32_bf16 v[48:51], a[96:99], v[208:211], v[48:51]\n"
"v_exp_f32 v224, v224\n"
"v_mfma_f32_16x16x32_bf16 v[48:51], a[100:103], v[212:215], v[48:51]\n"
"ds_read_b128 a[28:31], v244 offset:39360\n"
"ds_read_b128 a[24:27], v244 offset:39296\n"
"v_mfma_f32_16x16x32_bf16 v[52:55], a[108:111], v[212:215], v[52:55]\n"
"v_exp_f32 v225, v225\n"
"v_mfma_f32_16x16x32_bf16 v[52:55], a[104:107], v[208:211], v[52:55]\n"
"ds_read_b128 a[20:23], v244 offset:39232\n"
"v_perm_b32 v204, v225, v224, s72\n"
"v_mfma_f32_16x16x32_bf16 v[56:59], a[112:115], v[208:211], v[56:59]\n"
"s_add_u32 m0, s53, 192\n"
"v_exp_f32 v226, v226\n"
"v_mfma_f32_16x16x32_bf16 v[56:59], a[116:119], v[212:215], v[56:59]\n"
"buffer_load_dwordx4 v247, s[36:39], s52 offen offset:3072 lds\n"
"v_mfma_f32_16x16x32_bf16 v[60:63], a[124:127], v[212:215], v[60:63]\n"
"v_exp_f32 v227, v227\n"
"v_mfma_f32_16x16x32_bf16 v[60:63], a[120:123], v[208:211], v[60:63]\n"
"buffer_store_dwordx4 a[236:239], v250, s[48:51], s84 offen offset:1024 sc1\n"
"v_perm_b32 v205, v227, v226, s72\n"
"v_mfma_f32_16x16x32_bf16 v[192:195], a[32:35], v[176:179], -4.0\n"
"s_cmp_eq_u32 s70, s56\n"
"v_exp_f32 v228, v228\n"
"v_mfma_f32_16x16x32_bf16 v[192:195], a[36:39], v[180:183], v[192:195]\n"
"s_cselect_b32 s50, 0, s50\n"
"ds_read_b128 a[16:19], v244 offset:39168\n"
"ds_read_b128 a[32:35], v222 offset:34816\n"
"v_mfma_f32_16x16x32_bf16 v[192:195], a[40:43], v[184:187], v[192:195]\n"
"v_exp_f32 v229, v229\n"
"v_mfma_f32_16x16x32_bf16 v[192:195], a[44:47], v[188:191], v[192:195]\n"
"ds_read_b128 a[36:39], v222 offset:34880\n"
"v_perm_b32 v206, v229, v228, s72\n"
"v_mfma_f32_16x16x32_bf16 v[224:227], a[60:63], v[188:191], -4.0\n"
"s_add_u32 m0, s54, 0\n"
"v_exp_f32 v230, v230\n"
"v_mfma_f32_16x16x32_bf16 v[224:227], a[56:59], v[184:187], v[224:227]\n"
"buffer_load_dwordx4 v248, s[40:43], s52 offen offset:0 lds\n"
"v_mfma_f32_16x16x32_bf16 v[224:227], a[52:55], v[180:183], v[224:227]\n"
"v_exp_f32 v231, v231\n"
"v_mfma_f32_16x16x32_bf16 v[224:227], a[48:51], v[176:179], v[224:227]\n"
"ds_read_b128 a[40:43], v222 offset:34944\n"
"v_perm_b32 v207, v231, v230, s72\n"
"v_mfma_f32_16x16x32_bf16 v[64:67], a[64:67], v[200:203], v[64:67]\n"
"v_exp_f32 v192, v192\n"
"v_mfma_f32_16x16x32_bf16 v[64:67], a[68:71], v[204:207], v[64:67]\n"
"ds_read_b128 a[44:47], v222 offset:35008\n"
"ds_read_b128 a[60:63], v222 offset:39360\n"
"v_mfma_f32_16x16x32_bf16 v[68:71], a[76:79], v[204:207], v[68:71]\n"
"v_exp_f32 v193, v193\n"
"v_mfma_f32_16x16x32_bf16 v[68:71], a[72:75], v[200:203], v[68:71]\n"
"ds_read_b128 a[56:59], v222 offset:39296\n"
"v_perm_b32 v212, v193, v192, s72\n"
"v_mfma_f32_16x16x32_bf16 v[72:75], a[80:83], v[200:203], v[72:75]\n"
"s_add_u32 m0, s54, 8\n"
"v_exp_f32 v194, v194\n"
"v_mfma_f32_16x16x32_bf16 v[72:75], a[84:87], v[204:207], v[72:75]\n"
"buffer_load_dwordx4 v248, s[40:43], s52 offen offset:1024 lds\n"
"v_mfma_f32_16x16x32_bf16 v[76:79], a[92:95], v[204:207], v[76:79]\n"
"v_exp_f32 v195, v195\n"
"v_mfma_f32_16x16x32_bf16 v[76:79], a[88:91], v[200:203], v[76:79]\n"
"ds_read_b128 a[52:55], v222 offset:39232\n"
"v_perm_b32 v213, v195, v194, s72\n"
"v_smfmac_f32_16x16x64_bf16 a[136:139], a[144:147], v[200:207], v251\n"
"s_waitcnt lgkmcnt(5)\n"
"ds_read_b128 a[48:51], v222 offset:39168\n"
"v_mfma_f32_16x16x32_bf16 v[192:195], a[0:3], v[128:131], -4.0\n"
"v_exp_f32 v224, v224\n"
"v_mfma_f32_16x16x32_bf16 v[192:195], a[4:7], v[132:135], v[192:195]\n"
"ds_read_b64_tr_b16 a[64:65], v245 offset:33024\n"
"ds_read_b64_tr_b16 a[66:67], v245 offset:37152\n"
"v_mfma_f32_16x16x32_bf16 v[192:195], a[8:11], v[136:139], v[192:195]\n"
"v_exp_f32 v225, v225\n"
"v_mfma_f32_16x16x32_bf16 v[192:195], a[12:15], v[140:143], v[192:195]\n"
"ds_read_b64_tr_b16 a[72:73], v245 offset:33032\n"
"v_perm_b32 v214, v225, v224, s72\n"
"v_mfma_f32_16x16x32_bf16 v[196:199], a[28:31], v[140:143], -4.0\n"
"s_add_u32 m0, s54, 1936\n"
"v_exp_f32 v226, v226\n"
"v_mfma_f32_16x16x32_bf16 v[196:199], a[24:27], v[136:139], v[196:199]\n"
"Lhop1_1_%=:\n s_cbranch_execz Lhop1_5_%=\n"
"buffer_load_dwordx4 v248, s[40:43], s52 offen offset:128 lds\n"
"v_mfma_f32_16x16x32_bf16 v[196:199], a[20:23], v[132:135], v[196:199]\n"
"v_exp_f32 v227, v227\n"
"v_mfma_f32_16x16x32_bf16 v[196:199], a[16:19], v[128:131], v[196:199]\n"
"buffer_store_dwordx4 a[240:243], v250, s[48:51], s84 offen offset:128 sc1\n"
"v_perm_b32 v215, v227, v226, s72\n"
"v_mfma_f32_16x16x32_bf16 v[80:83], a[96:99], v[200:203], v[80:83]\n"
"v_exp_f32 v192, v192\n"
"v_mfma_f32_16x16x32_bf16 v[80:83], a[100:103], v[204:207], v[80:83]\n"
"ds_read_b64_tr_b16 a[74:75], v245 offset:37160\n"
"ds_read_b64_tr_b16 a[80:81], v245 offset:33280\n"
"v_mfma_f32_16x16x32_bf16 v[84:87], a[108:111], v[204:207], v[84:87]\n"
"v_exp_f32 v193, v193\n"
"v_mfma_f32_16x16x32_bf16 v[84:87], a[104:107], v[200:203], v[84:87]\n"
"ds_read_b64_tr_b16 a[82:83], v245 offset:37408\n"
"ds_read_b64_tr_b16 a[88:89], v245 offset:33288\n"
"v_mfma_f32_16x16x32_bf16 v[88:91], a[112:115], v[200:203], v[88:91]\n"
"s_add_u32 m0, s54, 1944\n"
"v_exp_f32 v194, v194\n"
"v_mfma_f32_16x16x32_bf16 v[88:91], a[116:119], v[204:207], v[88:91]\n"
"buffer_load_dwordx4 v248, s[40:43], s52 offen offset:1152 lds\n"
"v_mfma_f32_16x16x32_bf16 v[92:95], a[124:127], v[204:207], v[92:95]\n"
"s_add_u32 s52, s52, 16384\n"
"v_exp_f32 v195, v195\n"
"v_mfma_f32_16x16x32_bf16 v[92:95], a[120:123], v[200:203], v[92:95]\n"
"v_perm_b32 v200, v193, v192, s72\n"
"v_perm_b32 v201, v195, v194, s72\n"
"v_mfma_f32_16x16x32_bf16 v[224:227], a[32:35], v[128:131], -4.0\n"
"v_exp_f32 v196, v196\n"
"v_mfma_f32_16x16x32_bf16 v[224:227], a[36:39], v[132:135], v[224:227]\n"
"s_waitcnt lgkmcnt(4)\n"
"ds_read_b64_tr_b16 a[90:91], v245 offset:37416\n"
"ds_read_b64_tr_b16 a[96:97], v245 offset:35088\n"
"v_mfma_f32_16x16x32_bf16 v[224:227], a[40:43], v[136:139], v[224:227]\n"
"v_exp_f32 v197, v197\n"
"v_mfma_f32_16x16x32_bf16 v[224:227], a[44:47], v[140:143], v[224:227]\n"
"ds_read_b64_tr_b16 a[98:99], v245 offset:39216\n"
"v_perm_b32 v202, v197, v196, s72\n"
"v_mfma_f32_16x16x32_bf16 v[228:231], a[60:63], v[140:143], -4.0\n"
"v_exp_f32 v198, v198\n"
"v_mfma_f32_16x16x32_bf16 v[228:231], a[56:59], v[136:139], v[228:231]\n"
"ds_read_b64_tr_b16 a[104:105], v245 offset:35096\n"
"ds_read_b64_tr_b16 a[106:107], v245 offset:39224\n"
"v_mfma_f32_16x16x32_bf16 v[228:231], a[52:55], v[132:135], v[228:231]\n"
"v_exp_f32 v199, v199\n"
"v_mfma_f32_16x16x32_bf16 v[228:231], a[48:51], v[128:131], v[228:231]\n"
"ds_read_b64_tr_b16 a[112:113], v245 offset:35344\n"
"v_perm_b32 v203, v199, v198, s72\n"
"v_mfma_f32_16x16x32_bf16 a[140:143], a[144:147], v[212:215], a[140:143]\n"
"ds_read_b64_tr_b16 a[114:115], v245 offset:39472\n"
"ds_read_b64_tr_b16 a[120:121], v245 offset:35352\n"
"v_mfma_f32_16x16x32_bf16 v[96:99], a[68:71], v[212:215], v[96:99]\n"
"v_exp_f32 v224, v224\n"
"v_mfma_f32_16x16x32_bf16 v[100:103], a[76:79], v[212:215], v[100:103]\n"
"ds_read_b64_tr_b16 a[68:69], v255 offset:33024\n"
"ds_read_b64_tr_b16 a[70:71], v255 offset:37152\n"
"v_mfma_f32_16x16x32_bf16 v[104:107], a[84:87], v[212:215], v[104:107]\n"
"v_exp_f32 v225, v225\n"
"v_mfma_f32_16x16x32_bf16 v[108:111], a[92:95], v[212:215], v[108:111]\n"
"ds_read_b64_tr_b16 a[76:77], v255 offset:33032\n"
"v_perm_b32 v204, v225, v224, s72\n"
"v_mfma_f32_16x16x32_bf16 v[112:115], a[100:103], v[212:215], v[112:115]\n"
"v_exp_f32 v226, v226\n"
"v_mfma_f32_16x16x32_bf16 v[116:119], a[108:111], v[212:215], v[116:119]\n"
"ds_read_b64_tr_b16 a[78:79], v255 offset:37160\n"
"ds_read_b64_tr_b16 a[84:85], v255 offset:33280\n"
"v_mfma_f32_16x16x32_bf16 v[120:123], a[116:119], v[212:215], v[120:123]\n"
"v_exp_f32 v227, v227\n"
"v_mfma_f32_16x16x32_bf16 v[124:127], a[124:127], v[212:215], v[124:127]\n"
"ds_read_b64_tr_b16 a[86:87], v255 offset:37408\n"
"v_perm_b32 v205, v227, v226, s72\n"
"v_mfma_f32_16x16x32_bf16 v[192:195], a[0:3], v[144:147], -4.0\n"
"v_exp_f32 v228, v228\n"
"v_mfma_f32_16x16x32_bf16 v[192:195], a[4:7], v[148:151], v[192:195]\n"
"ds_read_b64_tr_b16 a[92:93], v255 offset:33288\n"
"ds_read_b64_tr_b16 a[94:95], v255 offset:37416\n"
"v_mfma_f32_16x16x32_bf16 v[192:195], a[8:11], v[152:155], v[192:195]\n"
"v_exp_f32 v229, v229\n"
"v_mfma_f32_16x16x32_bf16 v[192:195], a[12:15], v[156:159], v[192:195]\n"
"ds_read_b64_tr_b16 a[100:101], v255 offset:35088\n"
"v_perm_b32 v206, v229, v228, s72\n"
"v_mfma_f32_16x16x32_bf16 v[196:199], a[28:31], v[156:159], -4.0\n"
"v_exp_f32 v230, v230\n"
"v_mfma_f32_16x16x32_bf16 v[196:199], a[24:27], v[152:155], v[196:199]\n"
"ds_read_b64_tr_b16 a[102:103], v255 offset:39216\n"
"ds_read_b64_tr_b16 a[108:109], v255 offset:35096\n"
"v_mfma_f32_16x16x32_bf16 v[196:199], a[20:23], v[148:151], v[196:199]\n"
"v_exp_f32 v231, v231\n"
"v_mfma_f32_16x16x32_bf16 v[196:199], a[16:19], v[144:147], v[196:199]\n"
"buffer_store_dwordx4 a[244:247], v250, s[48:51], s84 offen offset:1152 sc1\n"
"v_perm_b32 v207, v231, v230, s72\n"
"v_mfma_f32_16x16x32_bf16 v[224:227], a[32:35], v[144:147], -4.0\n"
"s_mov_b64 s[84:85], s[86:87]\n"
"v_exp_f32 v192, v192\n"
"v_mfma_f32_16x16x32_bf16 v[224:227], a[36:39], v[148:151], v[224:227]\n"
"s_mov_b64 s[86:87], s[88:89]\n"
"ds_read_b64_tr_b16 a[110:111], v255 offset:39224\n"
"ds_read_b64_tr_b16 a[116:117], v255 offset:35344\n"
"v_mfma_f32_16x16x32_bf16 v[224:227], a[40:43], v[152:155], v[224:227]\n"
"s_mov_b64 s[88:89], s[90:91]\n"
"v_exp_f32 v193, v193\n"
"v_mfma_f32_16x16x32_bf16 v[224:227], a[44:47], v[156:159], v[224:227]\n"
"s_mov_b64 s[90:91], s[92:93]\n"
"ds_read_b64_tr_b16 a[118:119], v255 offset:39472\n"
"v_perm_b32 v208, v193, v192, s72\n"
"v_mfma_f32_16x16x32_bf16 v[228:231], a[60:63], v[156:159], -4.0\n"
"s_mov_b32 s50, 65011712\n"
"v_exp_f32 v194, v194\n"
"v_mfma_f32_16x16x32_bf16 v[228:231], a[56:59], v[152:155], v[228:231]\n"
"ds_read_b64_tr_b16 a[124:125], v255 offset:35352\n"
"ds_read_b64_tr_b16 a[126:127], v255 offset:39480\n"
"v_mfma_f32_16x16x32_bf16 v[228:231], a[52:55], v[148:151], v[228:231]\n"
"v_exp_f32 v195, v195\n"
"v_mfma_f32_16x16x32_bf16 v[228:231], a[48:51], v[144:147], v[228:231]\n"
"ds_read_b64_tr_b16 a[122:123], v245 offset:39480\n"
"v_perm_b32 v209, v195, v194, s72\n"
"v_smfmac_f32_16x16x64_bf16 a[128:131], a[144:147], v[200:207], v251\n"
"v_mfma_f32_16x16x32_bf16 v[0:3], a[64:67], v[200:203], v[0:3]\n"
"s_waitcnt lgkmcnt(6)\n"
"v_exp_f32 v196, v196\n"
"v_mfma_f32_16x16x32_bf16 v[0:3], a[68:71], v[204:207], v[0:3]\n"
"v_mfma_f32_16x16x32_bf16 v[4:7], a[76:79], v[204:207], v[4:7]\n"
"v_exp_f32 v197, v197\n"
"v_mfma_f32_16x16x32_bf16 v[4:7], a[72:75], v[200:203], v[4:7]\n"
"v_perm_b32 v210, v197, v196, s72\n"
"v_mfma_f32_16x16x32_bf16 v[8:11], a[80:83], v[200:203], v[8:11]\n"
"v_exp_f32 v198, v198\n"
"v_mfma_f32_16x16x32_bf16 v[8:11], a[84:87], v[204:207], v[8:11]\n"
"v_mfma_f32_16x16x32_bf16 v[12:15], a[92:95], v[204:207], v[12:15]\n"
"v_exp_f32 v199, v199\n"
"v_mfma_f32_16x16x32_bf16 v[12:15], a[88:91], v[200:203], v[12:15]\n"
"s_waitcnt vmcnt(11)\n"
"ds_read2_b64 a[216:219], v246 offset0:0 offset1:2\n"
"v_perm_b32 v211, v199, v198, s72\n"
"v_mfma_f32_16x16x32_bf16 v[16:19], a[96:99], v[200:203], v[16:19]\n"
"v_exp_f32 v224, v224\n"
"v_mfma_f32_16x16x32_bf16 v[16:19], a[100:103], v[204:207], v[16:19]\n"
"s_waitcnt lgkmcnt(1)\n"
"ds_read2_b64 a[220:223], v246 offset0:1 offset1:3\n"
"ds_read2_b64 a[224:227], v246 offset0:32 offset1:34\n"
"v_mfma_f32_16x16x32_bf16 v[20:23], a[108:111], v[204:207], v[20:23]\n"
"v_exp_f32 v225, v225\n"
"v_mfma_f32_16x16x32_bf16 v[20:23], a[104:107], v[200:203], v[20:23]\n"
"ds_read2_b64 a[228:231], v246 offset0:33 offset1:35\n"
"v_perm_b32 v212, v225, v224, s72\n"
"v_mfma_f32_16x16x32_bf16 v[24:27], a[112:115], v[200:203], v[24:27]\n"
"s_add_u32 m0, s53, 17408\n"
"v_exp_f32 v226, v226\n"
"v_mfma_f32_16x16x32_bf16 v[24:27], a[116:119], v[204:207], v[24:27]\n"
"buffer_load_dwordx4 v247, s[36:39], s52 offen offset:0 lds\n"
"v_mfma_f32_16x16x32_bf16 v[28:31], a[124:127], v[204:207], v[28:31]\n"
"v_exp_f32 v227, v227\n"
"v_mfma_f32_16x16x32_bf16 v[28:31], a[120:123], v[200:203], v[28:31]\n"
"ds_read2_b64 a[232:235], v246 offset0:64 offset1:66\n"
"v_perm_b32 v213, v227, v226, s72\n"
"v_mfma_f32_16x16x32_bf16 v[192:195], a[0:3], v[160:163], -4.0\n"
"v_exp_f32 v228, v228\n"
"v_mfma_f32_16x16x32_bf16 v[192:195], a[4:7], v[164:167], v[192:195]\n"
"ds_read2_b64 a[236:239], v246 offset0:65 offset1:67\n"
"ds_read2_b64 a[240:243], v246 offset0:96 offset1:98\n"
"v_mfma_f32_16x16x32_bf16 v[192:195], a[8:11], v[168:171], v[192:195]\n"
"v_exp_f32 v229, v229\n"
"v_mfma_f32_16x16x32_bf16 v[192:195], a[12:15], v[172:175], v[192:195]\n"
"ds_read2_b64 a[244:247], v246 offset0:97 offset1:99\n"
"v_perm_b32 v214, v229, v228, s72\n"
"v_mfma_f32_16x16x32_bf16 v[196:199], a[28:31], v[172:175], -4.0\n"
"s_add_u32 m0, s53, 17472\n"
"v_exp_f32 v230, v230\n"
"v_mfma_f32_16x16x32_bf16 v[196:199], a[24:27], v[168:171], v[196:199]\n"
"buffer_load_dwordx4 v247, s[36:39], s52 offen offset:1024 lds\n"
"v_mfma_f32_16x16x32_bf16 v[196:199], a[20:23], v[164:167], v[196:199]\n"
"v_exp_f32 v231, v231\n"
"v_mfma_f32_16x16x32_bf16 v[196:199], a[16:19], v[160:163], v[196:199]\n"
"v_perm_b32 v215, v231, v230, s72\n"
"v_mfma_f32_16x16x32_bf16 v[224:227], a[32:35], v[160:163], -4.0\n"
"s_add_u32 m0, s53, 17536\n"
"v_exp_f32 v192, v192\n"
"v_mfma_f32_16x16x32_bf16 v[224:227], a[36:39], v[164:167], v[224:227]\n"
"buffer_load_dwordx4 v247, s[36:39], s52 offen offset:2048 lds\n"
"v_mfma_f32_16x16x32_bf16 v[224:227], a[40:43], v[168:171], v[224:227]\n"
"v_exp_f32 v193, v193\n"
"v_mfma_f32_16x16x32_bf16 v[224:227], a[44:47], v[172:175], v[224:227]\n"
"v_perm_b32 v200, v193, v192, s72\n"
"v_mfma_f32_16x16x32_bf16 v[228:231], a[60:63], v[172:175], -4.0\n"
"s_add_u32 m0, s53, 17600\n"
"v_exp_f32 v194, v194\n"
"v_mfma_f32_16x16x32_bf16 v[228:231], a[56:59], v[168:171], v[228:231]\n"
"buffer_load_dwordx4 v247, s[36:39], s52 offen offset:3072 lds\n"
"v_mfma_f32_16x16x32_bf16 v[228:231], a[52:55], v[164:167], v[228:231]\n"
"v_exp_f32 v195, v195\n"
"v_mfma_f32_16x16x32_bf16 v[228:231], a[48:51], v[160:163], v[228:231]\n"
"s_waitcnt vmcnt(12) lgkmcnt(8)\n"
"v_perm_b32 v201, v195, v194, s72\n"
"v_smfmac_f32_16x16x64_bf16 a[132:135], a[144:147], v[208:215], v251\n"
"v_mfma_f32_16x16x32_bf16 v[32:35], a[64:67], v[208:211], v[32:35]\n"
"v_exp_f32 v196, v196\n"
"v_mfma_f32_16x16x32_bf16 v[32:35], a[68:71], v[212:215], v[32:35]\n"
"s_barrier\n"
"ds_read_b128 a[0:3], v244 offset:52224\n"
"ds_read_b128 a[4:7], v244 offset:52288\n"
"v_mfma_f32_16x16x32_bf16 v[36:39], a[76:79], v[212:215], v[36:39]\n"
"v_exp_f32 v197, v197\n"
"v_mfma_f32_16x16x32_bf16 v[36:39], a[72:75], v[208:211], v[36:39]\n"
"ds_read_b128 a[8:11], v244 offset:52352\n"
"v_perm_b32 v202, v197, v196, s72\n"
"v_mfma_f32_16x16x32_bf16 v[40:43], a[80:83], v[208:211], v[40:43]\n"
"s_add_u32 m0, s55, 0\n"
"v_exp_f32 v198, v198\n"
"v_mfma_f32_16x16x32_bf16 v[40:43], a[84:87], v[212:215], v[40:43]\n"
"s_waitcnt lgkmcnt(3)\n"
"buffer_load_dwordx4 v249, s[44:47], s76 offen offset:0 nt lds\n"
"v_mfma_f32_16x16x32_bf16 v[44:47], a[92:95], v[212:215], v[44:47]\n"
"v_exp_f32 v199, v199\n"
"v_mfma_f32_16x16x32_bf16 v[44:47], a[88:91], v[208:211], v[44:47]\n"
"ds_read_b128 a[12:15], v244 offset:52416\n"
"v_perm_b32 v203, v199, v198, s72\n"
"v_mfma_f32_16x16x32_bf16 v[48:51], a[96:99], v[208:211], v[48:51]\n"
"v_exp_f32 v224, v224\n"
"v_mfma_f32_16x16x32_bf16 v[48:51], a[100:103], v[212:215], v[48:51]\n"
"ds_read_b128 a[28:31], v244 offset:56768\n"
"ds_read_b128 a[24:27], v244 offset:56704\n"
"v_mfma_f32_16x16x32_bf16 v[52:55], a[108:111], v[212:215], v[52:55]\n"
"v_exp_f32 v225, v225\n"
"v_mfma_f32_16x16x32_bf16 v[52:55], a[104:107], v[208:211], v[52:55]\n"
"ds_read_b128 a[20:23], v244 offset:56640\n"
"v_perm_b32 v204, v225, v224, s72\n"
"v_mfma_f32_16x16x32_bf16 v[56:59], a[112:115], v[208:211], v[56:59]\n"
"s_add_u32 m0, s55, 8\n"
"v_exp_f32 v226, v226\n"
"v_mfma_f32_16x16x32_bf16 v[56:59], a[116:119], v[212:215], v[56:59]\n"
"buffer_load_dwordx4 v249, s[44:47], s76 offen offset:1024 nt lds\n"
"v_mfma_f32_16x16x32_bf16 v[60:63], a[124:127], v[212:215], v[60:63]\n"
"v_exp_f32 v227, v227\n"
"v_mfma_f32_16x16x32_bf16 v[60:63], a[120:123], v[208:211], v[60:63]\n"
"ds_read_b128 a[16:19], v244 offset:56576\n"
"v_perm_b32 v205, v227, v226, s72\n"
"v_mfma_f32_16x16x32_bf16 v[192:195], a[32:35], v[176:179], -4.0\n"
"v_exp_f32 v228, v228\n"
"v_mfma_f32_16x16x32_bf16 v[192:195], a[36:39], v[180:183], v[192:195]\n"
"ds_read_b128 a[32:35], v222 offset:52224\n"
"ds_read_b128 a[36:39], v222 offset:52288\n"
"v_mfma_f32_16x16x32_bf16 v[192:195], a[40:43], v[184:187], v[192:195]\n"
"v_exp_f32 v229, v229\n"
"v_mfma_f32_16x16x32_bf16 v[192:195], a[44:47], v[188:191], v[192:195]\n"
"ds_read_b128 a[40:43], v222 offset:52352\n"
"v_perm_b32 v206, v229, v228, s72\n"
"v_mfma_f32_16x16x32_bf16 v[224:227], a[60:63], v[188:191], -4.0\n"
"s_add_u32 m0, s55, 16\n"
"v_exp_f32 v230, v230\n"
"v_mfma_f32_16x16x32_bf16 v[224:227], a[56:59], v[184:187], v[224:227]\n"
"buffer_load_dwordx4 v249, s[44:47], s76 offen offset:2048 nt lds\n"
"v_mfma_f32_16x16x32_bf16 v[224:227], a[52:55], v[180:183], v[224:227]\n"
"v_exp_f32 v231, v231\n"
"v_mfma_f32_16x16x32_bf16 v[224:227], a[48:51], v[176:179], v[224:227]\n"
"ds_read_b128 a[44:47], v222 offset:52416\n"
"v_perm_b32 v207, v231, v230, s72\n"
"v_mfma_f32_16x16x32_bf16 v[64:67], a[64:67], v[200:203], v[64:67]\n"
"v_exp_f32 v192, v192\n"
"v_mfma_f32_16x16x32_bf16 v[64:67], a[68:71], v[204:207], v[64:67]\n"
"ds_read_b128 a[60:63], v222 offset:56768\n"
"ds_read_b128 a[56:59], v222 offset:56704\n"
"v_mfma_f32_16x16x32_bf16 v[68:71], a[76:79], v[204:207], v[68:71]\n"
"v_exp_f32 v193, v193\n"
"v_mfma_f32_16x16x32_bf16 v[68:71], a[72:75], v[200:203], v[68:71]\n"
"ds_read_b128 a[52:55], v222 offset:56640\n"
"v_perm_b32 v212, v193, v192, s72\n"
"v_mfma_f32_16x16x32_bf16 v[72:75], a[80:83], v[200:203], v[72:75]\n"
"s_add_u32 m0, s55, 24\n"
"v_exp_f32 v194, v194\n"
"v_mfma_f32_16x16x32_bf16 v[72:75], a[84:87], v[204:207], v[72:75]\n"
"buffer_load_dwordx4 v249, s[44:47], s76 offen offset:3072 nt lds\n"
"v_mfma_f32_16x16x32_bf16 v[76:79], a[92:95], v[204:207], v[76:79]\n"
"s_mov_b64 s[76:77], s[78:79]\n"
"v_exp_f32 v195, v195\n"
"v_mfma_f32_16x16x32_bf16 v[76:79], a[88:91], v[200:203], v[76:79]\n"
"s_mov_b64 s[78:79], s[80:81]\n"
"ds_read_b128 a[48:51], v222 offset:56576\n"
"v_perm_b32 v213, v195, v194, s72\n"
"v_smfmac_f32_16x16x64_bf16 a[136:139], a[144:147], v[200:207], v251\n"
"s_mov_b64 s[80:81], s[82:83]\n"
"ds_read_b64_tr_b16 a[64:65], v245 offset:49536\n"
"s_waitcnt lgkmcnt(14)\n"
"v_mfma_f32_16x16x32_bf16 v[192:195], a[0:3], v[128:131], -4.0\n"
"s_cmp_lt_u32 s56, 2\n"
"v_exp_f32 v224, v224\n"
"v_mfma_f32_16x16x32_bf16 v[192:195], a[4:7], v[132:135], v[192:195]\n"
"s_cselect_b32 s46, 65011712, 0\n"
"ds_read_b64_tr_b16 a[66:67], v245 offset:53664\n"
"ds_read_b64_tr_b16 a[72:73], v245 offset:49544\n"
"v_mfma_f32_16x16x32_bf16 v[192:195], a[8:11], v[136:139], v[192:195]\n"
"s_waitcnt lgkmcnt(7)\n"
"v_exp_f32 v225, v225\n"
"v_mfma_f32_16x16x32_bf16 v[192:195], a[12:15], v[140:143], v[192:195]\n"
"ds_read_b64_tr_b16 a[74:75], v245 offset:53672\n"
"v_perm_b32 v214, v225, v224, s72\n"
"v_mfma_f32_16x16x32_bf16 v[196:199], a[28:31], v[140:143], -4.0\n"
"s_add_u32 m0, s54, 16512\n"
"v_exp_f32 v226, v226\n"
"v_mfma_f32_16x16x32_bf16 v[196:199], a[24:27], v[136:139], v[196:199]\n"
"buffer_load_dwordx4 v248, s[40:43], s52 offen offset:0 lds\n"
"v_mfma_f32_16x16x32_bf16 v[196:199], a[20:23], v[132:135], v[196:199]\n"
"v_exp_f32 v227, v227\n"
"v_mfma_f32_16x16x32_bf16 v[196:199], a[16:19], v[128:131], v[196:199]\n"
"ds_read_b64_tr_b16 a[80:81], v245 offset:49792\n"
"v_perm_b32 v215, v227, v226, s72\n"
"v_mfma_f32_16x16x32_bf16 v[80:83], a[96:99], v[200:203], v[80:83]\n"
"v_exp_f32 v192, v192\n"
"v_mfma_f32_16x16x32_bf16 v[80:83], a[100:103], v[204:207], v[80:83]\n"
"ds_read_b64_tr_b16 a[82:83], v245 offset:53920\n"
"ds_read_b64_tr_b16 a[88:89], v245 offset:49800\n"
"v_mfma_f32_16x16x32_bf16 v[84:87], a[108:111], v[204:207], v[84:87]\n"
"v_exp_f32 v193, v193\n"
"v_mfma_f32_16x16x32_bf16 v[84:87], a[104:107], v[200:203], v[84:87]\n"
"ds_read_b64_tr_b16 a[90:91], v245 offset:53928\n"
"ds_read_b64_tr_b16 a[96:97], v245 offset:51600\n"
"v_mfma_f32_16x16x32_bf16 v[88:91], a[112:115], v[200:203], v[88:91]\n"
"s_add_u32 m0, s54, 16520\n"
"v_exp_f32 v194, v194\n"
"v_mfma_f32_16x16x32_bf16 v[88:91], a[116:119], v[204:207], v[88:91]\n"
"buffer_load_dwordx4 v248, s[40:43], s52 offen offset:1024 lds\n"
"v_mfma_f32_16x16x32_bf16 v[92:95], a[124:127], v[204:207], v[92:95]\n"
"v_exp_f32 v195, v195\n"
"v_mfma_f32_16x16x32_bf16 v[92:95], a[120:123], v[200:203], v[92:95]\n"
"v_perm_b32 v200, v193, v192, s72\n"
"v_perm_b32 v201, v195, v194, s72\n"
"v_mfma_f32_16x16x32_bf16 v[224:227], a[32:35], v[128:131], -4.0\n"
"v_exp_f32 v196, v196\n"
"v_mfma_f32_16x16x32_bf16 v[224:227], a[36:39], v[132:135], v[224:227]\n"
"ds_read_b64_tr_b16 a[98:99], v245 offset:55728\n"
"ds_read_b64_tr_b16 a[104:105], v245 offset:51608\n"
"v_mfma_f32_16x16x32_bf16 v[224:227], a[40:43], v[136:139], v[224:227]\n"
"v_exp_f32 v197, v197\n"
"v_mfma_f32_16x16x32_bf16 v[224:227], a[44:47], v[140:143], v[224:227]\n"
"s_waitcnt lgkmcnt(6)\n"
"ds_read_b64_tr_b16 a[106:107], v245 offset:55736\n"
"v_perm_b32 v202, v197, v196, s72\n"
"v_mfma_f32_16x16x32_bf16 v[228:231], a[60:63], v[140:143], -4.0\n"
"s_add_u32 m0, s54, 18448\n"
"v_exp_f32 v198, v198\n"
"v_mfma_f32_16x16x32_bf16 v[228:231], a[56:59], v[136:139], v[228:231]\n"
"buffer_load_dwordx4 v248, s[40:43], s52 offen offset:128 lds\n"
"v_mfma_f32_16x16x32_bf16 v[228:231], a[52:55], v[132:135], v[228:231]\n"
"v_exp_f32 v199, v199\n"
"v_mfma_f32_16x16x32_bf16 v[228:231], a[48:51], v[128:131], v[228:231]\n"
"ds_read_b64_tr_b16 a[112:113], v245 offset:51856\n"
"v_perm_b32 v203, v199, v198, s72\n"
"v_mfma_f32_16x16x32_bf16 a[140:143], a[144:147], v[212:215], a[140:143]\n"
"ds_read_b64_tr_b16 a[114:115], v245 offset:55984\n"
"ds_read_b64_tr_b16 a[120:121], v245 offset:51864\n"
"v_mfma_f32_16x16x32_bf16 v[96:99], a[68:71], v[212:215], v[96:99]\n"
"v_exp_f32 v224, v224\n"
"v_mfma_f32_16x16x32_bf16 v[100:103], a[76:79], v[212:215], v[100:103]\n"
"ds_read_b64_tr_b16 a[68:69], v255 offset:49536\n"
"ds_read_b64_tr_b16 a[70:71], v255 offset:53664\n"
"v_mfma_f32_16x16x32_bf16 v[104:107], a[84:87], v[212:215], v[104:107]\n"
"v_exp_f32 v225, v225\n"
"v_mfma_f32_16x16x32_bf16 v[108:111], a[92:95], v[212:215], v[108:111]\n"
"ds_read_b64_tr_b16 a[76:77], v255 offset:49544\n"
"v_perm_b32 v204, v225, v224, s72\n"
"v_mfma_f32_16x16x32_bf16 v[112:115], a[100:103], v[212:215], v[112:115]\n"
"v_exp_f32 v226, v226\n"
"v_mfma_f32_16x16x32_bf16 v[116:119], a[108:111], v[212:215], v[116:119]\n"
"ds_read_b64_tr_b16 a[78:79], v255 offset:53672\n"
"ds_read_b64_tr_b16 a[84:85], v255 offset:49792\n"
"v_mfma_f32_16x16x32_bf16 v[120:123], a[116:119], v[212:215], v[120:123]\n"
"v_exp_f32 v227, v227\n"
"v_mfma_f32_16x16x32_bf16 v[124:127], a[124:127], v[212:215], v[124:127]\n"
"s_cmp_eq_u32 s56, 3\n"
"ds_read_b64_tr_b16 a[86:87], v255 offset:53920\n"
"v_perm_b32 v205, v227, v226, s72\n"
"s_cbranch_scc1 LTS%=\n"
"v_mfma_f32_16x16x32_bf16 v[192:195], a[0:3], v[144:147], -4.0\n"
"v_exp_f32 v228, v228\n"
"v_mfma_f32_16x16x32_bf16 v[192:195], a[4:7], v[148:151], v[192:195]\n"
"ds_read_b64_tr_b16 a[92:93], v255 offset:49800\n"
"ds_read_b64_tr_b16 a[94:95], v255 offset:53928\n"
"v_mfma_f32_16x16x32_bf16 v[192:195], a[8:11], v[152:155], v[192:195]\n"
"v_exp_f32 v229, v229\n"
"v_mfma_f32_16x16x32_bf16 v[192:195], a[12:15], v[156:159], v[192:195]\n"
"ds_read_b64_tr_b16 a[100:101], v255 offset:51600\n"
"v_perm_b32 v206, v229, v228, s72\n"
"v_mfma_f32_16x16x32_bf16 v[196:199], a[28:31], v[156:159], -4.0\n"
"v_exp_f32 v230, v230\n"
"v_mfma_f32_16x16x32_bf16 v[196:199], a[24:27], v[152:155], v[196:199]\n"
"ds_read_b64_tr_b16 a[102:103], v255 offset:55728\n"
"ds_read_b64_tr_b16 a[108:109], v255 offset:51608\n"
"v_mfma_f32_16x16x32_bf16 v[196:199], a[20:23], v[148:151], v[196:199]\n"
"v_exp_f32 v231, v231\n"
"v_mfma_f32_16x16x32_bf16 v[196:199], a[16:19], v[144:147], v[196:199]\n"
"ds_read_b64_tr_b16 a[110:111], v255 offset:55736\n"
"v_perm_b32 v207, v231, v230, s72\n"
"v_mfma_f32_16x16x32_bf16 v[224:227], a[32:35], v[144:147], -4.0\n"
"v_exp_f32 v192, v192\n"
"v_mfma_f32_16x16x32_bf16 v[224:227], a[36:39], v[148:151], v[224:227]\n"
"ds_read_b64_tr_b16 a[116:117], v255 offset:51856\n"
"ds_read_b64_tr_b16 a[118:119], v255 offset:55984\n"
"v_mfma_f32_16x16x32_bf16 v[224:227], a[40:43], v[152:155], v[224:227]\n"
"v_exp_f32 v193, v193\n"
"v_mfma_f32_16x16x32_bf16 v[224:227], a[44:47], v[156:159], v[224:227]\n"
"ds_read_b64_tr_b16 a[124:125], v255 offset:51864\n"
"v_perm_b32 v208, v193, v192, s72\n"
"v_mfma_f32_16x16x32_bf16 v[228:231], a[60:63], v[156:159], -4.0\n"
"v_exp_f32 v194, v194\n"
"v_mfma_f32_16x16x32_bf16 v[228:231], a[56:59], v[152:155], v[228:231]\n"
"ds_read_b64_tr_b16 a[126:127], v255 offset:55992\n"
"ds_read_b64_tr_b16 a[122:123], v245 offset:55992\n"
"v_mfma_f32_16x16x32_bf16 v[228:231], a[52:55], v[148:151], v[228:231]\n"
"v_exp_f32 v195, v195\n"
"v_mfma_f32_16x16x32_bf16 v[228:231], a[48:51], v[144:147], v[228:231]\n"
"v_perm_b32 v209, v195, v194, s72\n"
"v_smfmac_f32_16x16x64_bf16 a[128:131], a[144:147], v[200:207], v251\n"
"v_mfma_f32_16x16x32_bf16 v[0:3], a[64:67], v[200:203], v[0:3]\n"
"s_waitcnt lgkmcnt(6)\n"
"v_exp_f32 v196, v196\n"
"v_mfma_f32_16x16x32_bf16 v[0:3], a[68:71], v[204:207], v[0:3]\n"
"v_mfma_f32_16x16x32_bf16 v[4:7], a[76:79], v[204:207], v[4:7]\n"
"v_exp_f32 v197, v197\n"
"v_mfma_f32_16x16x32_bf16 v[4:7], a[72:75], v[200:203], v[4:7]\n"
"v_perm_b32 v210, v197, v196, s72\n"
"v_mfma_f32_16x16x32_bf16 v[8:11], a[80:83], v[200:203], v[8:11]\n"
"v_exp_f32 v198, v198\n"
"v_mfma_f32_16x16x32_bf16 v[8:11], a[84:87], v[204:207], v[8:11]\n"
"s_nop 0\n"
"v_accvgpr_read_b32 v254, a128\n"
"v_accvgpr_read_b32 v252, a129\n"
"v_mfma_f32_16x16x32_bf16 v[12:15], a[92:95], v[204:207], v[12:15]\n"
"v_exp_f32 v199, v199\n"
"v_mfma_f32_16x16x32_bf16 v[12:15], a[88:91], v[200:203], v[12:15]\n"
"v_perm_b32 v211, v199, v198, s72\n"
"v_add_f32 v254, v254, v252\n"
"v_mfma_f32_16x16x32_bf16 v[16:19], a[96:99], v[200:203], v[16:19]\n"
"v_exp_f32 v224, v224\n"
"v_mfma_f32_16x16x32_bf16 v[16:19], a[100:103], v[204:207], v[16:19]\n"
"s_waitcnt lgkmcnt(0)\n"
"v_rcp_f32 v252, v254\n"
"v_mfma_f32_16x16x32_bf16 v[20:23], a[108:111], v[204:207], v[20:23]\n"
"v_exp_f32 v225, v225\n"
"v_mfma_f32_16x16x32_bf16 v[20:23], a[104:107], v[200:203], v[20:23]\n"
"v_perm_b32 v212, v225, v224, s72\n"
"v_accvgpr_write_b32 a128, 0\n"
"v_accvgpr_write_b32 a129, 0\n"
"v_max_f32 v254, v254, v252\n"
"v_cmp_nlt_f32 s[62:63], v254, s58\n"
"v_mul_f32 v223, v218, v252\n"
"v_mul_f32 v254, 0x3b000000, v223\n"
"v_fma_f32 v0, v0, v252, -v223\n"
"v_fma_f32 v1, v1, v252, -v223\n"
"v_fma_f32 v2, v2, v252, -v223\n"
"v_fma_f32 v3, v3, v252, -v223\n"
"v_fma_f32 v4, v4, v252, -v223\n"
"v_fma_f32 v5, v5, v252, -v223\n"
"v_fma_f32 v6, v6, v252, -v223\n"
"v_fma_f32 v7, v7, v252, -v223\n"
"v_max3_f32 v253, abs(v0), abs(v1), abs(v2)\n"
"v_cmp_lt_f32 vcc, v253, v254\n"
"v_cvt_pk_bf16_f32 v0, v0, v1\n"
"v_cvt_pk_bf16_f32 v1, v2, v3\n"
"v_cvt_pk_bf16_f32 v2, v4, v5\n"
"v_cvt_pk_bf16_f32 v3, v6, v7\n"
"v_mfma_f32_16x16x32_bf16 v[24:27], a[112:115], v[200:203], v[24:27]\n"
"s_add_u32 m0, s54, 18456\n"
"v_exp_f32 v226, v226\n"
"v_mfma_f32_16x16x32_bf16 v[24:27], a[116:119], v[204:207], v[24:27]\n"
"s_and_b32 s73, vcc_lo, vcc_hi\n"
"buffer_load_dwordx4 v248, s[40:43], s52 offen offset:1152 lds\n"
"v_mfma_f32_16x16x32_bf16 v[28:31], a[124:127], v[204:207], v[28:31]\n"
"s_add_u32 s52, s52, 16384\n"
"v_exp_f32 v227, v227\n"
"v_mfma_f32_16x16x32_bf16 v[28:31], a[120:123], v[200:203], v[28:31]\n"
"s_lshr_b32 s74, s73, 16\n"
"ds_write_b128 v216, v[0:3] offset:0\n"
"v_perm_b32 v213, v227, v226, s72\n"
"v_fma_f32 v8, v8, v252, -v223\n"
"v_mfma_f32_16x16x32_bf16 v[192:195], a[0:3], v[160:163], -4.0\n"
"s_and_b32 s73, s73, s74\n"
"v_exp_f32 v228, v228\n"
"v_mfma_f32_16x16x32_bf16 v[192:195], a[4:7], v[164:167], v[192:195]\n"
"s_or_b32 s73, s73, s62\n"
"v_fma_f32 v9, v9, v252, -v223\n"
"v_fma_f32 v10, v10, v252, -v223\n"
"v_mfma_f32_16x16x32_bf16 v[192:195], a[8:11], v[168:171], v[192:195]\n"
"s_or_b32 s73, s73, s63\n"
"v_exp_f32 v229, v229\n"
"v_mfma_f32_16x16x32_bf16 v[192:195], a[12:15], v[172:175], v[192:195]\n"
"s_addc_u32 s57, s57, s57\n"
"v_perm_b32 v214, v229, v228, s72\n"
"v_fma_f32 v11, v11, v252, -v223\n"
"v_mfma_f32_16x16x32_bf16 v[232:235], a[148:151], a[152:155], 0\n"
"v_fma_f32 v12, v12, v252, -v223\n"
"v_fma_f32 v13, v13, v252, -v223\n"
"v_mfma_f32_16x16x32_bf16 v[236:239], a[148:151], a[156:159], 0\n"
"v_fma_f32 v14, v14, v252, -v223\n"
"v_fma_f32 v15, v15, v252, -v223\n"
"v_mfma_f32_16x16x32_bf16 v[196:199], a[28:31], v[172:175], -4.0\n"
"s_add_u32 m0, s53, 34816\n"
"v_exp_f32 v230, v230\n"
"v_mfma_f32_16x16x32_bf16 v[196:199], a[24:27], v[168:171], v[196:199]\n"
"buffer_load_dwordx4 v247, s[36:39], s52 offen offset:0 lds\n"
"v_mfma_f32_16x16x32_bf16 v[196:199], a[20:23], v[164:167], v[196:199]\n"
"v_exp_f32 v231, v231\n"
"v_mfma_f32_16x16x32_bf16 v[196:199], a[16:19], v[160:163], v[196:199]\n"
"v_perm_b32 v215, v231, v230, s72\n"
"v_cvt_pk_bf16_f32 v8, v8, v9\n"
"v_cvt_pk_bf16_f32 v9, v10, v11\n"
"v_cvt_pk_bf16_f32 v10, v12, v13\n"
"v_cvt_pk_bf16_f32 v11, v14, v15\n"
"v_mfma_f32_16x16x32_bf16 v[224:227], a[32:35], v[160:163], -4.0\n"
"v_exp_f32 v192, v192\n"
"v_mfma_f32_16x16x32_bf16 v[224:227], a[36:39], v[164:167], v[224:227]\n"
"v_fma_f32 v16, v16, v252, -v223\n"
"v_fma_f32 v17, v17, v252, -v223\n"
"v_mfma_f32_16x16x32_bf16 v[224:227], a[40:43], v[168:171], v[224:227]\n"
"v_exp_f32 v193, v193\n"
"v_mfma_f32_16x16x32_bf16 v[224:227], a[44:47], v[172:175], v[224:227]\n"
"ds_write_b128 v216, v[8:11] offset:64\n"
"v_perm_b32 v200, v193, v192, s72\n"
"v_fma_f32 v18, v18, v252, -v223\n"
"v_mfma_f32_16x16x32_bf16 v[240:243], a[148:151], a[160:163], 0\n"
"ds_read_b128 v[4:7], v217 offset:0\n"
"ds_read_b128 v[12:15], v217 offset:576\n"
"v_cvt_pk_bf16_f32 v128, v232, v233\n"
"v_cvt_pk_bf16_f32 v129, v234, v235\n"
"v_mfma_f32_16x16x32_bf16 v[232:235], a[148:151], a[164:167], 0\n"
"v_cvt_pk_bf16_f32 v130, v236, v237\n"
"v_cvt_pk_bf16_f32 v131, v238, v239\n"
"v_mfma_f32_16x16x32_bf16 v[228:231], a[60:63], v[172:175], -4.0\n"
"s_add_u32 m0, s53, 34880\n"
"v_exp_f32 v194, v194\n"
"v_mfma_f32_16x16x32_bf16 v[228:231], a[56:59], v[168:171], v[228:231]\n"
"buffer_load_dwordx4 v247, s[36:39], s52 offen offset:1024 lds\n"
"v_mfma_f32_16x16x32_bf16 v[228:231], a[52:55], v[164:167], v[228:231]\n"
"v_exp_f32 v195, v195\n"
"v_mfma_f32_16x16x32_bf16 v[228:231], a[48:51], v[160:163], v[228:231]\n"
"s_waitcnt vmcnt(14) lgkmcnt(4)\n"
"v_perm_b32 v201, v195, v194, s72\n"
"v_fma_f32 v19, v19, v252, -v223\n"
"v_smfmac_f32_16x16x64_bf16 a[132:135], a[144:147], v[208:215], v251\n"
"v_fma_f32 v20, v20, v252, -v223\n"
"v_mfma_f32_16x16x32_bf16 v[32:35], a[64:67], v[208:211], v[32:35]\n"
"v_exp_f32 v196, v196\n"
"v_mfma_f32_16x16x32_bf16 v[32:35], a[68:71], v[212:215], v[32:35]\n"
"s_barrier\n"
"ds_read_b128 a[0:3], v244 offset:0\n"
"ds_read_b128 a[4:7], v244 offset:64\n"
"v_fma_f32 v21, v21, v252, -v223\n"
"v_fma_f32 v22, v22, v252, -v223\n"
"v_fma_f32 v23, v23, v252, -v223\n"
"v_cvt_pk_bf16_f32 v16, v16, v17\n"
"v_cvt_pk_bf16_f32 v17, v18, v19\n"
"v_cvt_pk_bf16_f32 v18, v20, v21\n"
"v_cvt_pk_bf16_f32 v19, v22, v23\n"
"v_mfma_f32_16x16x32_bf16 v[36:39], a[76:79], v[212:215], v[36:39]\n"
"v_exp_f32 v197, v197\n"
"v_mfma_f32_16x16x32_bf16 v[36:39], a[72:75], v[208:211], v[36:39]\n"
"ds_read_b128 a[8:11], v244 offset:128\n"
"ds_write_b128 v216, v[16:19] offset:0\n"
"v_perm_b32 v202, v197, v196, s72\n"
"v_mfma_f32_16x16x32_bf16 v[236:239], a[148:151], a[168:171], 0\n"
"v_cvt_pk_bf16_f32 v132, v240, v241\n"
"v_cvt_pk_bf16_f32 v133, v242, v243\n"
"v_mfma_f32_16x16x32_bf16 v[240:243], a[148:151], a[172:175], 0\n"
"v_cvt_pk_bf16_f32 v134, v232, v233\n"
"v_cvt_pk_bf16_f32 v135, v234, v235\n"
"v_mfma_f32_16x16x32_bf16 v[40:43], a[80:83], v[208:211], v[40:43]\n"
"s_add_u32 m0, s53, 34944\n"
"v_exp_f32 v198, v198\n"
"v_mfma_f32_16x16x32_bf16 v[40:43], a[84:87], v[212:215], v[40:43]\n"
"buffer_load_dwordx4 v247, s[36:39], s52 offen offset:2048 lds\n"
"v_mfma_f32_16x16x32_bf16 v[44:47], a[92:95], v[212:215], v[44:47]\n"
"v_exp_f32 v199, v199\n"
"v_mfma_f32_16x16x32_bf16 v[44:47], a[88:91], v[208:211], v[44:47]\n"
"ds_read_b128 a[12:15], v244 offset:192\n"
"v_perm_b32 v203, v199, v198, s72\n"
"v_mfma_f32_16x16x32_bf16 v[48:51], a[96:99], v[208:211], v[48:51]\n"
"v_exp_f32 v224, v224\n"
"v_mfma_f32_16x16x32_bf16 v[48:51], a[100:103], v[212:215], v[48:51]\n"
"ds_read_b128 a[28:31], v244 offset:4544\n"
"ds_read_b128 a[24:27], v244 offset:4480\n"
"v_mfma_f32_16x16x32_bf16 v[52:55], a[108:111], v[212:215], v[52:55]\n"
"Lhop1_4_%=:\n s_cbranch_execz Lback1_%=\n"
"v_exp_f32 v225, v225\n"
"v_mfma_f32_16x16x32_bf16 v[52:55], a[104:107], v[208:211], v[52:55]\n"
"ds_read_b128 a[20:23], v244 offset:4416\n"
"v_perm_b32 v204, v225, v224, s72\n"
"v_fma_f32 v24, v24, v252, -v223\n"
"v_fma_f32 v25, v25, v252, -v223\n"
"v_fma_f32 v26, v26, v252, -v223\n"
"v_fma_f32 v27, v27, v252, -v223\n"
"v_fma_f32 v28, v28, v252, -v223\n"
"v_fma_f32 v29, v29, v252, -v223\n"
"v_fma_f32 v30, v30, v252, -v223\n"
"v_fma_f32 v31, v31, v252, -v223\n"
"v_cvt_pk_bf16_f32 v24, v24, v25\n"
"v_cvt_pk_bf16_f32 v25, v26, v27\n"
"v_cvt_pk_bf16_f32 v26, v28, v29\n"
"v_cvt_pk_bf16_f32 v27, v30, v31\n"
"v_mfma_f32_16x16x32_bf16 v[232:235], a[148:151], a[176:179], 0\n"
"ds_write_b128 v216, v[24:27] offset:64\n"
"v_cvt_pk_bf16_f32 v136, v236, v237\n"
"v_cvt_pk_bf16_f32 v137, v238, v239\n"
"v_mfma_f32_16x16x32_bf16 v[236:239], a[148:151], a[180:183], 0\n"
"v_cvt_pk_bf16_f32 v138, v240, v241\n"
"v_cvt_pk_bf16_f32 v139, v242, v243\n"
"v_mfma_f32_16x16x32_bf16 v[56:59], a[112:115], v[208:211], v[56:59]\n"
"s_add_u32 m0, s53, 35008\n"
"v_exp_f32 v226, v226\n"
"v_mfma_f32_16x16x32_bf16 v[56:59], a[116:119], v[212:215], v[56:59]\n"
"buffer_load_dwordx4 v247, s[36:39], s52 offen offset:3072 lds\n"
"v_mfma_f32_16x16x32_bf16 v[60:63], a[124:127], v[212:215], v[60:63]\n"
"v_exp_f32 v227, v227\n"
"v_mfma_f32_16x16x32_bf16 v[60:63], a[120:123], v[208:211], v[60:63]\n"
"s_waitcnt lgkmcnt(5)\n"
"buffer_store_dwordx4 v[4:7], v250, s[48:51], s84 offen offset:0 sc1\n"
"v_perm_b32 v205, v227, v226, s72\n"
"v_mfma_f32_16x16x32_bf16 v[192:195], a[32:35], v[176:179], -4.0\n"
"v_exp_f32 v228, v228\n"
"v_mfma_f32_16x16x32_bf16 v[192:195], a[36:39], v[180:183], v[192:195]\n"
"s_add_u32 s56, s56, 1\n"
"ds_read_b128 a[16:19], v244 offset:4352\n"
"ds_read_b128 a[32:35], v222 offset:0\n"
"v_mfma_f32_16x16x32_bf16 v[192:195], a[40:43], v[184:187], v[192:195]\n"
"v_exp_f32 v229, v229\n"
"v_mfma_f32_16x16x32_bf16 v[192:195], a[44:47], v[188:191], v[192:195]\n"
"ds_read_b128 a[36:39], v222 offset:64\n"
"ds_read_b128 v[20:23], v217 offset:0\n"
"ds_read_b128 v[28:31], v217 offset:576\n"
"v_perm_b32 v206, v229, v228, s72\n"
"v_mfma_f32_16x16x32_bf16 v[240:243], a[148:151], a[184:187], 0\n"
"v_cvt_pk_bf16_f32 v140, v232, v233\n"
"v_cvt_pk_bf16_f32 v141, v234, v235\n"
"v_mfma_f32_16x16x32_bf16 v[232:235], a[148:151], a[188:191], 0\n"
"v_cvt_pk_bf16_f32 v142, v236, v237\n"
"v_cvt_pk_bf16_f32 v143, v238, v239\n"
"v_mfma_f32_16x16x32_bf16 v[224:227], a[60:63], v[188:191], -4.0\n"
"s_add_u32 m0, s54, 33024\n"
"v_exp_f32 v230, v230\n"
"v_mfma_f32_16x16x32_bf16 v[224:227], a[56:59], v[184:187], v[224:227]\n"
"buffer_load_dwordx4 v248, s[40:43], s52 offen offset:0 lds\n"
"v_mfma_f32_16x16x32_bf16 v[224:227], a[52:55], v[180:183], v[224:227]\n"
"v_exp_f32 v231, v231\n"
"v_mfma_f32_16x16x32_bf16 v[224:227], a[48:51], v[176:179], v[224:227]\n"
"ds_read_b128 a[40:43], v222 offset:128\n"
"v_perm_b32 v207, v231, v230, s72\n"
"v_accvgpr_read_b32 v254, a132\n"
"v_accvgpr_read_b32 v252, a133\n"
"v_add_f32 v254, v254, v252\n"
"v_rcp_f32 v252, v254\n"
"v_accvgpr_write_b32 a132, 0\n"
"v_accvgpr_write_b32 a133, 0\n"
"v_max_f32 v254, v254, v252\n"
"v_cmp_nlt_f32 s[62:63], v254, s58\n"
"v_mul_f32 v223, v218, v252\n"
"v_mul_f32 v254, 0x3b000000, v223\n"
"v_fma_f32 v32, v32, v252, -v223\n"
"v_fma_f32 v33, v33, v252, -v223\n"
"v_fma_f32 v34, v34, v252, -v223\n"
"v_fma_f32 v35, v35, v252, -v223\n"
"v_fma_f32 v36, v36, v252, -v223\n"
"v_fma_f32 v37, v37, v252, -v223\n"
"v_fma_f32 v38, v38, v252, -v223\n"
"v_fma_f32 v39, v39, v252, -v223\n"
"v_max3_f32 v253, abs(v32), abs(v33), abs(v34)\n"
"v_cmp_lt_f32 vcc, v253, v254\n"
"v_cvt_pk_bf16_f32 v32, v32, v33\n"
"v_cvt_pk_bf16_f32 v33, v34, v35\n"
"v_cvt_pk_bf16_f32 v34, v36, v37\n"
"v_cvt_pk_bf16_f32 v35, v38, v39\n"
"v_mfma_f32_16x16x32_bf16 v[64:67], a[64:67], v[200:203], v[64:67]\n"
"s_and_b32 s73, vcc_lo, vcc_hi\n"
"v_exp_f32 v192, v192\n"
"v_mfma_f32_16x16x32_bf16 v[64:67], a[68:71], v[204:207], v[64:67]\n"
"s_lshr_b32 s74, s73, 16\n"
"ds_read_b128 a[44:47], v222 offset:192\n"
"ds_read_b128 a[60:63], v222 offset:4544\n"
"ds_write_b128 v216, v[32:35] offset:0\n"
"v_mfma_f32_16x16x32_bf16 v[68:71], a[76:79], v[204:207], v[68:71]\n"
"s_and_b32 s73, s73, s74\n"
"v_exp_f32 v193, v193\n"
"v_mfma_f32_16x16x32_bf16 v[68:71], a[72:75], v[200:203], v[68:71]\n"
"s_or_b32 s73, s73, s62\n"
"buffer_store_dwordx4 v[12:15], v250, s[48:51], s84 offen offset:1024 sc1\n"
"v_perm_b32 v212, v193, v192, s72\n"
"v_mfma_f32_16x16x32_bf16 v[236:239], a[148:151], a[192:195], 0\n"
"s_or_b32 s73, s73, s63\n"
"v_cvt_pk_bf16_f32 v144, v240, v241\n"
"v_cvt_pk_bf16_f32 v145, v242, v243\n"
"v_mfma_f32_16x16x32_bf16 v[240:243], a[148:151], a[196:199], 0\n"
"s_addc_u32 s57, s57, s57\n"
"v_cvt_pk_bf16_f32 v146, v232, v233\n"
"v_cvt_pk_bf16_f32 v147, v234, v235\n"
"v_mfma_f32_16x16x32_bf16 v[72:75], a[80:83], v[200:203], v[72:75]\n"
"s_add_u32 m0, s54, 33032\n"
"v_exp_f32 v194, v194\n"
"v_mfma_f32_16x16x32_bf16 v[72:75], a[84:87], v[204:207], v[72:75]\n"
"buffer_load_dwordx4 v248, s[40:43], s52 offen offset:1024 lds\n"
"v_mfma_f32_16x16x32_bf16 v[76:79], a[92:95], v[204:207], v[76:79]\n"
"v_exp_f32 v195, v195\n"
"v_mfma_f32_16x16x32_bf16 v[76:79], a[88:91], v[200:203], v[76:79]\n"
"s_waitcnt vmcnt(10)\n"
"ds_read_b128 a[56:59], v222 offset:4480\n"
"v_perm_b32 v213, v195, v194, s72\n"
"v_smfmac_f32_16x16x64_bf16 a[136:139], a[144:147], v[200:207], v251\n"
"ds_read_b128 a[52:55], v222 offset:4416\n"
"v_mfma_f32_16x16x32_bf16 v[192:195], a[0:3], v[128:131], -4.0\n"
"v_exp_f32 v224, v224\n"
"v_mfma_f32_16x16x32_bf16 v[192:195], a[4:7], v[132:135], v[192:195]\n"
"ds_read_b128 a[48:51], v222 offset:4352\n"
"ds_read2_b64 a[152:155], v246 offset0:0 offset1:2\n"
"v_mfma_f32_16x16x32_bf16 v[192:195], a[8:11], v[136:139], v[192:195]\n"
"s_waitcnt lgkmcnt(4)\n"
"v_exp_f32 v225, v225\n"
"v_mfma_f32_16x16x32_bf16 v[192:195], a[12:15], v[140:143], v[192:195]\n"
"ds_read2_b64 a[156:159], v246 offset0:1 offset1:3\n"
"v_perm_b32 v214, v225, v224, s72\n"
"v_fma_f32 v40, v40, v252, -v223\n"
"v_fma_f32 v41, v41, v252, -v223\n"
"v_fma_f32 v42, v42, v252, -v223\n"
"v_fma_f32 v43, v43, v252, -v223\n"
"v_fma_f32 v44, v44, v252, -v223\n"
"v_fma_f32 v45, v45, v252, -v223\n"
"v_fma_f32 v46, v46, v252, -v223\n"
"v_fma_f32 v47, v47, v252, -v223\n"
"v_cvt_pk_bf16_f32 v40, v40, v41\n"
"v_cvt_pk_bf16_f32 v41, v42, v43\n"
"v_cvt_pk_bf16_f32 v42, v44, v45\n"
"v_cvt_pk_bf16_f32 v43, v46, v47\n"
"v_mfma_f32_16x16x32_bf16 v[232:235], a[148:151], a[200:203], 0\n"
"ds_write_b128 v216, v[40:43] offset:64\n"
"v_cvt_pk_bf16_f32 v148, v236, v237\n"
"v_cvt_pk_bf16_f32 v149, v238, v239\n"
"v_mfma_f32_16x16x32_bf16 v[236:239], a[148:151], a[204:207], 0\n"
"v_cvt_pk_bf16_f32 v150, v240, v241\n"
"v_cvt_pk_bf16_f32 v151, v242, v243\n"
"v_mfma_f32_16x16x32_bf16 v[196:199], a[28:31], v[140:143], -4.0\n"
"s_add_u32 m0, s54, 34960\n"
"v_exp_f32 v226, v226\n"
"v_mfma_f32_16x16x32_bf16 v[196:199], a[24:27], v[136:139], v[196:199]\n"
"buffer_load_dwordx4 v248, s[40:43], s52 offen offset:128 lds\n"
"v_mfma_f32_16x16x32_bf16 v[196:199], a[20:23], v[132:135], v[196:199]\n"
"v_exp_f32 v227, v227\n"
"v_mfma_f32_16x16x32_bf16 v[196:199], a[16:19], v[128:131], v[196:199]\n"
"ds_read2_b64 a[160:163], v246 offset0:32 offset1:34\n"
"v_perm_b32 v215, v227, v226, s72\n"
"v_mfma_f32_16x16x32_bf16 v[80:83], a[96:99], v[200:203], v[80:83]\n"
"v_exp_f32 v192, v192\n"
"v_mfma_f32_16x16x32_bf16 v[80:83], a[100:103], v[204:207], v[80:83]\n"
"ds_read2_b64 a[164:167], v246 offset0:33 offset1:35\n"
"ds_read2_b64 a[168:171], v246 offset0:64 offset1:66\n"
"v_mfma_f32_16x16x32_bf16 v[84:87], a[108:111], v[204:207], v[84:87]\n"
"v_exp_f32 v193, v193\n"
"v_mfma_f32_16x16x32_bf16 v[84:87], a[104:107], v[200:203], v[84:87]\n"
"buffer_store_dwordx4 v[20:23], v250, s[48:51], s84 offen offset:128 sc1\n"
"v_mfma_f32_16x16x32_bf16 v[240:243], a[148:151], a[208:211], 0\n"
"v_cvt_pk_bf16_f32 v152, v232, v233\n"
"v_cvt_pk_bf16_f32 v153, v234, v235\n"
"v_fma_f32 v48, v48, v252, -v223\n"
"v_fma_f32 v49, v49, v252, -v223\n"
"v_fma_f32 v50, v50, v252, -v223\n"
"v_fma_f32 v51, v51, v252, -v223\n"
"v_fma_f32 v52, v52, v252, -v223\n"
"v_fma_f32 v53, v53, v252, -v223\n"
"v_fma_f32 v54, v54, v252, -v223\n"
"v_fma_f32 v55, v55, v252, -v223\n"
"v_cvt_pk_bf16_f32 v48, v48, v49\n"
"v_cvt_pk_bf16_f32 v49, v50, v51\n"
"v_cvt_pk_bf16_f32 v50, v52, v53\n"
"v_cvt_pk_bf16_f32 v51, v54, v55\n"
"v_mfma_f32_16x16x32_bf16 v[232:235], a[148:151], a[212:215], 0\n"
"ds_read_b128 a[184:187], v217 offset:0\n"
"ds_read_b128 a[188:191], v217 offset:576\n"
"ds_write_b128 v216, v[48:51] offset:0\n"
"v_cvt_pk_bf16_f32 v154, v236, v237\n"
"v_cvt_pk_bf16_f32 v155, v238, v239\n"
"v_mfma_f32_16x16x32_bf16 v[88:91], a[112:115], v[200:203], v[88:91]\n"
"s_add_u32 m0, s54, 34968\n"
"v_exp_f32 v194, v194\n"
"v_mfma_f32_16x16x32_bf16 v[88:91], a[116:119], v[204:207], v[88:91]\n"
"buffer_load_dwordx4 v248, s[40:43], s52 offen offset:1152 lds\n"
"v_mfma_f32_16x16x32_bf16 v[92:95], a[124:127], v[204:207], v[92:95]\n"
"s_add_u32 s52, s52, 16384\n"
"v_exp_f32 v195, v195\n"
"v_mfma_f32_16x16x32_bf16 v[92:95], a[120:123], v[200:203], v[92:95]\n"
"v_perm_b32 v200, v193, v192, s72\n"
"v_perm_b32 v201, v195, v194, s72\n"
"v_mfma_f32_16x16x32_bf16 v[224:227], a[32:35], v[128:131], -4.0\n"
"v_exp_f32 v196, v196\n"
"v_mfma_f32_16x16x32_bf16 v[224:227], a[36:39], v[132:135], v[224:227]\n"
"ds_read2_b64 a[172:175], v246 offset0:65 offset1:67\n"
"ds_read2_b64 a[176:179], v246 offset0:96 offset1:98\n"
"v_mfma_f32_16x16x32_bf16 v[224:227], a[40:43], v[136:139], v[224:227]\n"
"v_exp_f32 v197, v197\n"
"v_mfma_f32_16x16x32_bf16 v[224:227], a[44:47], v[140:143], v[224:227]\n"
"ds_read2_b64 a[180:183], v246 offset0:97 offset1:99\n"
"v_perm_b32 v202, v197, v196, s72\n"
"v_mfma_f32_16x16x32_bf16 v[236:239], a[148:151], a[216:219], 0\n"
"v_cvt_pk_bf16_f32 v156, v240, v241\n"
"v_cvt_pk_bf16_f32 v157, v242, v243\n"
"v_mfma_f32_16x16x32_bf16 v[240:243], a[148:151], a[220:223], 0\n"
"v_cvt_pk_bf16_f32 v158, v232, v233\n"
"v_cvt_pk_bf16_f32 v159, v234, v235\n"
"v_mfma_f32_16x16x32_bf16 v[228:231], a[60:63], v[140:143], -4.0\n"
"s_waitcnt lgkmcnt(3)\n"
"v_exp_f32 v198, v198\n"
"v_mfma_f32_16x16x32_bf16 v[228:231], a[56:59], v[136:139], v[228:231]\n"
"ds_read_b64_tr_b16 a[64:65], v245 offset:0\n"
"ds_read_b64_tr_b16 a[66:67], v245 offset:4128\n"
"v_mfma_f32_16x16x32_bf16 v[228:231], a[52:55], v[132:135], v[228:231]\n"
"v_exp_f32 v199, v199\n"
"v_mfma_f32_16x16x32_bf16 v[228:231], a[48:51], v[128:131], v[228:231]\n"
"buffer_store_dwordx4 v[28:31], v250, s[48:51], s84 offen offset:1152 sc1\n"
"v_perm_b32 v203, v199, v198, s72\n"
"v_mfma_f32_16x16x32_bf16 a[140:143], a[144:147], v[212:215], a[140:143]\n"
"s_add_u32 s84, s84, 4096\n"
"ds_read_b64_tr_b16 a[72:73], v245 offset:8\n"
"ds_read_b64_tr_b16 a[74:75], v245 offset:4136\n"
"v_mfma_f32_16x16x32_bf16 v[96:99], a[68:71], v[212:215], v[96:99]\n"
"v_exp_f32 v224, v224\n"
"v_fma_f32 v56, v56, v252, -v223\n"
"v_fma_f32 v57, v57, v252, -v223\n"
"v_fma_f32 v58, v58, v252, -v223\n"
"v_fma_f32 v59, v59, v252, -v223\n"
"v_fma_f32 v60, v60, v252, -v223\n"
"v_fma_f32 v61, v61, v252, -v223\n"
"v_fma_f32 v62, v62, v252, -v223\n"
"v_fma_f32 v63, v63, v252, -v223\n"
"v_cvt_pk_bf16_f32 v56, v56, v57\n"
"v_cvt_pk_bf16_f32 v57, v58, v59\n"
"v_cvt_pk_bf16_f32 v58, v60, v61\n"
"v_cvt_pk_bf16_f32 v59, v62, v63\n"
"v_mfma_f32_16x16x32_bf16 v[100:103], a[76:79], v[212:215], v[100:103]\n"
"ds_read_b64_tr_b16 a[68:69], v255 offset:0\n"
"ds_read_b64_tr_b16 a[70:71], v255 offset:4128\n"
"ds_write_b128 v216, v[56:59] offset:64\n"
"v_mfma_f32_16x16x32_bf16 v[104:107], a[84:87], v[212:215], v[104:107]\n"
"v_exp_f32 v225, v225\n"
"v_mfma_f32_16x16x32_bf16 v[108:111], a[92:95], v[212:215], v[108:111]\n"
"ds_read_b64_tr_b16 a[76:77], v255 offset:8\n"
"v_perm_b32 v204, v225, v224, s72\n"
"v_mfma_f32_16x16x32_bf16 v[232:235], a[148:151], a[224:227], 0\n"
"v_cvt_pk_bf16_f32 v160, v236, v237\n"
"v_cvt_pk_bf16_f32 v161, v238, v239\n"
"v_mfma_f32_16x16x32_bf16 v[236:239], a[148:151], a[228:231], 0\n"
"v_cvt_pk_bf16_f32 v162, v240, v241\n"
"v_cvt_pk_bf16_f32 v163, v242, v243\n"
"v_mfma_f32_16x16x32_bf16 v[112:115], a[100:103], v[212:215], v[112:115]\n"
"ds_read_b128 a[192:195], v217 offset:0\n"
"ds_read_b128 a[196:199], v217 offset:576\n"
"v_exp_f32 v226, v226\n"
"v_mfma_f32_16x16x32_bf16 v[116:119], a[108:111], v[212:215], v[116:119]\n"
"ds_read_b64_tr_b16 a[78:79], v255 offset:4136\n"
"ds_read_b64_tr_b16 a[80:81], v245 offset:256\n"
"v_mfma_f32_16x16x32_bf16 v[120:123], a[116:119], v[212:215], v[120:123]\n"
"v_exp_f32 v227, v227\n"
"v_mfma_f32_16x16x32_bf16 v[124:127], a[124:127], v[212:215], v[124:127]\n"
"ds_read_b64_tr_b16 a[82:83], v245 offset:4384\n"
"v_perm_b32 v205, v227, v226, s72\n"
"v_mfma_f32_16x16x32_bf16 v[192:195], a[0:3], v[144:147], -4.0\n"
"v_exp_f32 v228, v228\n"
"v_mfma_f32_16x16x32_bf16 v[192:195], a[4:7], v[148:151], v[192:195]\n"
"ds_read_b64_tr_b16 a[84:85], v255 offset:256\n"
"ds_read_b64_tr_b16 a[86:87], v255 offset:4384\n"
"v_mfma_f32_16x16x32_bf16 v[192:195], a[8:11], v[152:155], v[192:195]\n"
"v_exp_f32 v229, v229\n"
"v_mfma_f32_16x16x32_bf16 v[192:195], a[12:15], v[156:159], v[192:195]\n"
"ds_read_b64_tr_b16 a[92:93], v255 offset:264\n"
"v_perm_b32 v206, v229, v228, s72\n"
"v_mbcnt_lo_u32_b32 v254, -1, 0\n"
"v_mbcnt_hi_u32_b32 v254, -1, v254\n"
"v_lshl_add_u32 v253, v254, 4, s55\n"
"v_lshl_add_u32 v254, v254, 2, s55\n"
"s_cmp_eq_u32 s71, 0\n s_cbranch_scc1 Lswb%=\n v_swap_b32 v96, v112\n v_swap_b32 v97, v113\n v_swap_b32 v98, v114\n v_swap_b32 v99, v115\n v_swap_b32 v100, v116\n v_swap_b32 v101, v117\n v_swap_b32 v102, v118\n v_swap_b32 v103, v119\n v_swap_b32 v104, v120\n v_swap_b32 v105, v121\n v_swap_b32 v106, v122\n v_swap_b32 v107, v123\n v_swap_b32 v108, v124\n v_swap_b32 v109, v125\n v_swap_b32 v110, v126\n v_swap_b32 v111, v127\n Lswb%=:\n"
"ds_write_b128 v253, v[112:115] offset:0\n"
"ds_write_b128 v253, v[116:119] offset:1024\n"
"ds_write_b128 v253, v[120:123] offset:2048\n"
"ds_write_b128 v253, v[124:127] offset:3072\n"
"ds_write_b32 v254, a140 offset:4128\n"
"s_nop 0\n"
"v_add_u32 v126, s75, v254\n"
"v_add_u32 v127, s75, v253\n"
"v_mfma_f32_16x16x32_bf16 v[240:243], a[148:151], a[232:235], 0\n"
"v_cvt_pk_bf16_f32 v164, v232, v233\n"
"v_cvt_pk_bf16_f32 v165, v234, v235\n"
"v_accvgpr_read_b32 v254, a136\n"
"v_accvgpr_read_b32 v252, a137\n"
"v_add_f32 v254, v254, v252\n"
"v_rcp_f32 v252, v254\n"
"v_accvgpr_write_b32 a136, 0\n"
"v_accvgpr_write_b32 a137, 0\n"
"v_max_f32 v254, v254, v252\n"
"v_cmp_nlt_f32 s[62:63], v254, s58\n"
"v_mul_f32 v223, v218, v252\n"
"v_mul_f32 v254, 0x3b000000, v223\n"
"v_fma_f32 v64, v64, v252, -v223\n"
"v_fma_f32 v65, v65, v252, -v223\n"
"v_fma_f32 v66, v66, v252, -v223\n"
"v_fma_f32 v67, v67, v252, -v223\n"
"v_fma_f32 v68, v68, v252, -v223\n"
"v_fma_f32 v69, v69, v252, -v223\n"
"v_fma_f32 v70, v70, v252, -v223\n"
"v_fma_f32 v71, v71, v252, -v223\n"
"v_max3_f32 v253, abs(v64), abs(v65), abs(v66)\n"
"v_cmp_lt_f32 vcc, v253, v254\n"
"v_cvt_pk_bf16_f32 v64, v64, v65\n"
"v_cvt_pk_bf16_f32 v65, v66, v67\n"
"v_cvt_pk_bf16_f32 v66, v68, v69\n"
"v_cvt_pk_bf16_f32 v67, v70, v71\n"
"v_mfma_f32_16x16x32_bf16 v[232:235], a[148:151], a[236:239], 0\n"
"s_and_b32 s73, vcc_lo, vcc_hi\n"
"ds_write_b128 v216, v[64:67] offset:0\n"
"v_cvt_pk_bf16_f32 v166, v236, v237\n"
"v_cvt_pk_bf16_f32 v167, v238, v239\n"
"v_mfma_f32_16x16x32_bf16 v[196:199], a[28:31], v[156:159], -4.0\n"
"s_lshr_b32 s74, s73, 16\n"
"v_exp_f32 v230, v230\n"
"v_mfma_f32_16x16x32_bf16 v[196:199], a[24:27], v[152:155], v[196:199]\n"
"s_waitcnt lgkmcnt(1)\n"
"ds_read_b64_tr_b16 a[94:95], v255 offset:4392\n"
"ds_read_b64_tr_b16 a[88:89], v245 offset:264\n"
"v_mfma_f32_16x16x32_bf16 v[196:199], a[20:23], v[148:151], v[196:199]\n"
"s_and_b32 s73, s73, s74\n"
"v_exp_f32 v231, v231\n"
"v_mfma_f32_16x16x32_bf16 v[196:199], a[16:19], v[144:147], v[196:199]\n"
"s_barrier\n"
"ds_read_b64_tr_b16 a[90:91], v245 offset:4392\n"
"ds_read_b32 a141, v126 offset:4128\n"
"ds_read_b128 v[112:115], v127 offset:0\n"
"ds_read_b128 v[116:119], v127 offset:1024\n"
"ds_read_b128 v[120:123], v127 offset:2048\n"
"ds_read_b128 v[124:127], v127 offset:3072\n"
"v_perm_b32 v207, v231, v230, s72\n"
"v_mfma_f32_16x16x32_bf16 v[224:227], a[32:35], v[144:147], -4.0\n"
"s_or_b32 s73, s73, s62\n"
"v_exp_f32 v192, v192\n"
"v_mfma_f32_16x16x32_bf16 v[224:227], a[36:39], v[148:151], v[224:227]\n"
"s_or_b32 s73, s73, s63\n"
"ds_read_b64_tr_b16 a[96:97], v245 offset:2064\n"
"ds_read_b64_tr_b16 a[98:99], v245 offset:6192\n"
"v_mfma_f32_16x16x32_bf16 v[224:227], a[40:43], v[152:155], v[224:227]\n"
"s_addc_u32 s57, s57, s57\n"
"v_exp_f32 v193, v193\n"
"v_mfma_f32_16x16x32_bf16 v[224:227], a[44:47], v[156:159], v[224:227]\n"
"ds_read_b64_tr_b16 a[100:101], v255 offset:2064\n"
"v_perm_b32 v208, v193, v192, s72\n"
"v_mfma_f32_16x16x32_bf16 v[236:239], a[148:151], a[240:243], 0\n"
"v_cvt_pk_bf16_f32 v168, v240, v241\n"
"v_cvt_pk_bf16_f32 v169, v242, v243\n"
"v_mfma_f32_16x16x32_bf16 v[240:243], a[148:151], a[244:247], 0\n"
"v_cvt_pk_bf16_f32 v170, v232, v233\n"
"v_cvt_pk_bf16_f32 v171, v234, v235\n"
"v_mfma_f32_16x16x32_bf16 v[228:231], a[60:63], v[156:159], -4.0\n"
"v_exp_f32 v194, v194\n"
"v_mfma_f32_16x16x32_bf16 v[228:231], a[56:59], v[152:155], v[228:231]\n"
"ds_read_b64_tr_b16 a[102:103], v255 offset:6192\n"
"ds_read_b64_tr_b16 a[108:109], v255 offset:2072\n"
"v_mfma_f32_16x16x32_bf16 v[228:231], a[52:55], v[148:151], v[228:231]\n"
"v_exp_f32 v195, v195\n"
"v_mfma_f32_16x16x32_bf16 v[228:231], a[48:51], v[144:147], v[228:231]\n"
"ds_read_b64_tr_b16 a[110:111], v255 offset:6200\n"
"v_perm_b32 v209, v195, v194, s72\n"
"v_fma_f32 v72, v72, v252, -v223\n"
"v_fma_f32 v73, v73, v252, -v223\n"
"v_fma_f32 v74, v74, v252, -v223\n"
"v_fma_f32 v75, v75, v252, -v223\n"
"v_fma_f32 v76, v76, v252, -v223\n"
"v_fma_f32 v77, v77, v252, -v223\n"
"v_fma_f32 v78, v78, v252, -v223\n"
"v_fma_f32 v79, v79, v252, -v223\n"
"v_cvt_pk_bf16_f32 v72, v72, v73\n"
"v_cvt_pk_bf16_f32 v73, v74, v75\n"
"v_cvt_pk_bf16_f32 v74, v76, v77\n"
"v_cvt_pk_bf16_f32 v75, v78, v79\n"
"v_smfmac_f32_16x16x64_bf16 a[128:131], a[144:147], v[200:207], v251\n"
"ds_read_b64_tr_b16 a[104:105], v245 offset:2072\n"
"v_mfma_f32_16x16x32_bf16 v[0:3], a[64:67], v[200:203], v[218:221]\n"
"v_exp_f32 v196, v196\n"
"v_mfma_f32_16x16x32_bf16 v[0:3], a[68:71], v[204:207], v[0:3]\n"
"ds_read_b64_tr_b16 a[106:107], v245 offset:6200\n"
"ds_read_b64_tr_b16 a[112:113], v245 offset:2320\n"
"ds_write_b128 v216, v[72:75] offset:64\n"
"v_mfma_f32_16x16x32_bf16 v[4:7], a[76:79], v[204:207], v[218:221]\n"
"v_exp_f32 v197, v197\n"
"v_mfma_f32_16x16x32_bf16 v[4:7], a[72:75], v[200:203], v[4:7]\n"
"ds_read_b64_tr_b16 a[114:115], v245 offset:6448\n"
"ds_read_b128 a[216:219], v217 offset:0\n"
"ds_read_b128 a[220:223], v217 offset:576\n"
"v_perm_b32 v210, v197, v196, s72\n"
"v_mfma_f32_16x16x32_bf16 v[232:235], a[148:151], a[152:155], 0\n"
"v_cvt_pk_bf16_f32 v172, v236, v237\n"
"v_cvt_pk_bf16_f32 v173, v238, v239\n"
"v_mfma_f32_16x16x32_bf16 v[236:239], a[148:151], a[156:159], 0\n"
"v_cvt_pk_bf16_f32 v174, v240, v241\n"
"v_cvt_pk_bf16_f32 v175, v242, v243\n"
"v_mfma_f32_16x16x32_bf16 v[8:11], a[80:83], v[200:203], v[218:221]\n"
"v_exp_f32 v198, v198\n"
"v_mfma_f32_16x16x32_bf16 v[8:11], a[84:87], v[204:207], v[8:11]\n"
"s_waitcnt lgkmcnt(8)\n"
"ds_read_b64_tr_b16 a[116:117], v255 offset:2320\n"
"ds_read_b64_tr_b16 a[118:119], v255 offset:6448\n"
"v_mfma_f32_16x16x32_bf16 v[12:15], a[92:95], v[204:207], v[218:221]\n"
"v_exp_f32 v199, v199\n"
"v_mfma_f32_16x16x32_bf16 v[12:15], a[88:91], v[200:203], v[12:15]\n"
"ds_read_b64_tr_b16 a[124:125], v255 offset:2328\n"
"v_perm_b32 v211, v199, v198, s72\n"
"v_fma_f32 v80, v80, v252, -v223\n"
"v_fma_f32 v81, v81, v252, -v223\n"
"v_fma_f32 v82, v82, v252, -v223\n"
"v_fma_f32 v83, v83, v252, -v223\n"
"v_fma_f32 v84, v84, v252, -v223\n"
"v_fma_f32 v85, v85, v252, -v223\n"
"v_fma_f32 v86, v86, v252, -v223\n"
"v_fma_f32 v87, v87, v252, -v223\n"
"v_cvt_pk_bf16_f32 v80, v80, v81\n"
"v_cvt_pk_bf16_f32 v81, v82, v83\n"
"v_cvt_pk_bf16_f32 v82, v84, v85\n"
"v_cvt_pk_bf16_f32 v83, v86, v87\n"
"v_mfma_f32_16x16x32_bf16 v[16:19], a[96:99], v[200:203], v[218:221]\n"
"v_exp_f32 v224, v224\n"
"v_mfma_f32_16x16x32_bf16 v[16:19], a[100:103], v[204:207], v[16:19]\n"
"s_waitcnt lgkmcnt(9)\n"
"ds_read_b64_tr_b16 a[126:127], v255 offset:6456\n"
"ds_read_b64_tr_b16 a[120:121], v245 offset:2328\n"
"ds_write_b128 v216, v[80:83] offset:0\n"
"v_mfma_f32_16x16x32_bf16 v[20:23], a[108:111], v[204:207], v[218:221]\n"
"s_waitcnt lgkmcnt(9)\n"
"v_exp_f32 v225, v225\n"
"v_mfma_f32_16x16x32_bf16 v[20:23], a[104:107], v[200:203], v[20:23]\n"
"ds_read_b64_tr_b16 a[122:123], v245 offset:6456\n"
"v_perm_b32 v212, v225, v224, s72\n"
"v_mfma_f32_16x16x32_bf16 v[240:243], a[148:151], a[160:163], 0\n"
"v_cvt_pk_bf16_f32 v176, v232, v233\n"
"v_cvt_pk_bf16_f32 v177, v234, v235\n"
"v_mfma_f32_16x16x32_bf16 v[232:235], a[148:151], a[164:167], 0\n"
"s_waitcnt lgkmcnt(5)\n"
"v_cvt_pk_bf16_f32 v178, v236, v237\n"
"v_cvt_pk_bf16_f32 v179, v238, v239\n"
"v_mfma_f32_16x16x32_bf16 v[24:27], a[112:115], v[200:203], v[218:221]\n"
"s_add_u32 m0, s53, 52224\n"
"v_exp_f32 v226, v226\n"
"v_mfma_f32_16x16x32_bf16 v[24:27], a[116:119], v[204:207], v[24:27]\n"
"s_waitcnt lgkmcnt(3)\n"
"buffer_load_dwordx4 v247, s[36:39], s52 offen offset:0 lds\n"
"v_mfma_f32_16x16x32_bf16 v[28:31], a[124:127], v[204:207], v[218:221]\n"
"s_waitcnt lgkmcnt(0)\n"
"v_exp_f32 v227, v227\n"
"v_mfma_f32_16x16x32_bf16 v[28:31], a[120:123], v[200:203], v[28:31]\n"
"v_perm_b32 v213, v227, v226, s72\n"
"v_fma_f32 v88, v88, v252, -v223\n"
"v_mfma_f32_16x16x32_bf16 v[192:195], a[0:3], v[160:163], -4.0\n"
"v_exp_f32 v228, v228\n"
"v_mfma_f32_16x16x32_bf16 v[192:195], a[4:7], v[164:167], v[192:195]\n"
"v_fma_f32 v89, v89, v252, -v223\n"
"v_fma_f32 v90, v90, v252, -v223\n"
"v_mfma_f32_16x16x32_bf16 v[192:195], a[8:11], v[168:171], v[192:195]\n"
"v_exp_f32 v229, v229\n"
"v_mfma_f32_16x16x32_bf16 v[192:195], a[12:15], v[172:175], v[192:195]\n"
"v_perm_b32 v214, v229, v228, s72\n"
"v_fma_f32 v91, v91, v252, -v223\n"
"v_fma_f32 v92, v92, v252, -v223\n"
"v_fma_f32 v93, v93, v252, -v223\n"
"v_fma_f32 v94, v94, v252, -v223\n"
"v_fma_f32 v95, v95, v252, -v223\n"
"v_cvt_pk_bf16_f32 v88, v88, v89\n"
"v_cvt_pk_bf16_f32 v89, v90, v91\n"
"v_cvt_pk_bf16_f32 v90, v92, v93\n"
"v_cvt_pk_bf16_f32 v91, v94, v95\n"
"v_accvgpr_read_b32 v254, a140\n"
"v_accvgpr_read_b32 v252, a141\n"
"v_add_f32 v254, v254, v252\n"
"v_rcp_f32 v252, v254\n"
"v_accvgpr_write_b32 a140, 0\n"
"v_accvgpr_write_b32 a141, 0\n"
"v_max_f32 v254, v254, v252\n"
"v_cmp_nlt_f32 s[62:63], v254, s58\n"
"v_mul_f32 v223, v218, v252\n"
"v_add_f32 v223, v223, v223\n"
"v_mul_f32 v254, 0x3b000000, v223\n"
"v_add_f32 v96, v96, v112\n"
"v_add_f32 v97, v97, v113\n"
"v_add_f32 v98, v98, v114\n"
"v_add_f32 v99, v99, v115\n"
"v_add_f32 v100, v100, v116\n"
"v_add_f32 v101, v101, v117\n"
"v_add_f32 v102, v102, v118\n"
"v_add_f32 v103, v103, v119\n"
"v_fma_f32 v96, v96, v252, -v223\n"
"v_fma_f32 v97, v97, v252, -v223\n"
"v_fma_f32 v98, v98, v252, -v223\n"
"v_fma_f32 v99, v99, v252, -v223\n"
"v_fma_f32 v100, v100, v252, -v223\n"
"v_fma_f32 v101, v101, v252, -v223\n"
"v_fma_f32 v102, v102, v252, -v223\n"
"v_fma_f32 v103, v103, v252, -v223\n"
"v_max3_f32 v253, abs(v96), abs(v97), abs(v98)\n"
"v_cmp_lt_f32 vcc, v253, v254\n"
"v_cvt_pk_bf16_f32 v96, v96, v97\n"
"v_cvt_pk_bf16_f32 v97, v98, v99\n"
"v_cvt_pk_bf16_f32 v98, v100, v101\n"
"v_cvt_pk_bf16_f32 v99, v102, v103\n"
"v_mfma_f32_16x16x32_bf16 v[236:239], a[148:151], a[168:171], 0\n"
"s_and_b32 s73, vcc_lo, vcc_hi\n"
"ds_write_b128 v216, v[88:91] offset:64\n"
"v_cvt_pk_bf16_f32 v180, v240, v241\n"
"v_cvt_pk_bf16_f32 v181, v242, v243\n"
"v_mfma_f32_16x16x32_bf16 v[240:243], a[148:151], a[172:175], 0\n"
"s_lshr_b32 s74, s73, 16\n"
"v_cvt_pk_bf16_f32 v182, v232, v233\n"
"v_cvt_pk_bf16_f32 v183, v234, v235\n"
"v_mfma_f32_16x16x32_bf16 v[196:199], a[28:31], v[172:175], -4.0\n"
"s_add_u32 m0, s53, 52288\n"
"v_exp_f32 v230, v230\n"
"v_mfma_f32_16x16x32_bf16 v[196:199], a[24:27], v[168:171], v[196:199]\n"
"s_and_b32 s73, s73, s74\n"
"buffer_load_dwordx4 v247, s[36:39], s52 offen offset:1024 lds\n"
"v_mfma_f32_16x16x32_bf16 v[196:199], a[20:23], v[164:167], v[196:199]\n"
"s_or_b32 s73, s73, s62\n"
"v_exp_f32 v231, v231\n"
"v_mfma_f32_16x16x32_bf16 v[196:199], a[16:19], v[160:163], v[196:199]\n"
"s_or_b32 s73, s73, s63\n"
"ds_read_b128 a[224:227], v217 offset:0\n"
"ds_read_b128 a[228:231], v217 offset:576\n"
"ds_write_b128 v216, v[96:99] offset:0\n"
"v_perm_b32 v215, v231, v230, s72\n"
"v_add_f32 v104, v104, v120\n"
"v_add_f32 v105, v105, v121\n"
"v_add_f32 v106, v106, v122\n"
"v_add_f32 v107, v107, v123\n"
"v_add_f32 v108, v108, v124\n"
"v_add_f32 v109, v109, v125\n"
"v_add_f32 v110, v110, v126\n"
"v_add_f32 v111, v111, v127\n"
"v_fma_f32 v104, v104, v252, -v223\n"
"v_fma_f32 v105, v105, v252, -v223\n"
"v_fma_f32 v106, v106, v252, -v223\n"
"v_fma_f32 v107, v107, v252, -v223\n"
"v_fma_f32 v108, v108, v252, -v223\n"
"v_fma_f32 v109, v109, v252, -v223\n"
"v_fma_f32 v110, v110, v252, -v223\n"
"v_fma_f32 v111, v111, v252, -v223\n"
"v_cvt_pk_bf16_f32 v104, v104, v105\n"
"v_cvt_pk_bf16_f32 v105, v106, v107\n"
"v_cvt_pk_bf16_f32 v106, v108, v109\n"
"v_cvt_pk_bf16_f32 v107, v110, v111\n"
"v_mfma_f32_16x16x32_bf16 v[224:227], a[32:35], v[160:163], -4.0\n"
"Lhop1_5_%=:\n s_cbranch_execz Lback1_%=\n"
"s_addc_u32 s57, s57, s57\n"
"v_exp_f32 v192, v192\n"
"v_mfma_f32_16x16x32_bf16 v[224:227], a[36:39], v[164:167], v[224:227]\n"
"v_mfma_f32_16x16x32_bf16 v[224:227], a[40:43], v[168:171], v[224:227]\n"
"v_exp_f32 v193, v193\n"
"v_mfma_f32_16x16x32_bf16 v[224:227], a[44:47], v[172:175], v[224:227]\n"
"ds_write_b128 v216, v[104:107] offset:64\n"
"v_perm_b32 v200, v193, v192, s72\n"
"v_mfma_f32_16x16x32_bf16 v[232:235], a[148:151], a[176:179], 0\n"
"v_cvt_pk_bf16_f32 v184, v236, v237\n"
"v_cvt_pk_bf16_f32 v185, v238, v239\n"
"v_mfma_f32_16x16x32_bf16 v[236:239], a[148:151], a[180:183], 0\n"
"v_cvt_pk_bf16_f32 v186, v240, v241\n"
"v_cvt_pk_bf16_f32 v187, v242, v243\n"
"v_mfma_f32_16x16x32_bf16 v[228:231], a[60:63], v[172:175], -4.0\n"
"s_add_u32 m0, s53, 52352\n"
"v_exp_f32 v194, v194\n"
"v_mfma_f32_16x16x32_bf16 v[228:231], a[56:59], v[168:171], v[228:231]\n"
"buffer_load_dwordx4 v247, s[36:39], s52 offen offset:2048 lds\n"
"v_mfma_f32_16x16x32_bf16 v[228:231], a[52:55], v[164:167], v[228:231]\n"
"v_exp_f32 v195, v195\n"
"v_mfma_f32_16x16x32_bf16 v[228:231], a[48:51], v[160:163], v[228:231]\n"
"s_waitcnt vmcnt(11) lgkmcnt(5)\n"
"ds_read_b128 a[232:235], v217 offset:0\n"
"ds_read_b128 a[236:239], v217 offset:576\n"
"v_perm_b32 v201, v195, v194, s72\n"
"v_cvt_pk_bf16_f32 v188, v232, v233\n"
"v_cvt_pk_bf16_f32 v189, v234, v235\n"
"v_cvt_pk_bf16_f32 v190, v236, v237\n"
"v_cvt_pk_bf16_f32 v191, v238, v239\n"
"v_smfmac_f32_16x16x64_bf16 a[132:135], a[144:147], v[208:215], v251\n"
"v_mfma_f32_16x16x32_bf16 v[32:35], a[64:67], v[208:211], v[218:221]\n"
"v_exp_f32 v196, v196\n"
"v_mfma_f32_16x16x32_bf16 v[32:35], a[68:71], v[212:215], v[32:35]\n"
"s_barrier\n"
"v_mfma_f32_16x16x32_bf16 v[36:39], a[76:79], v[212:215], v[218:221]\n"
"v_exp_f32 v197, v197\n"
"v_mfma_f32_16x16x32_bf16 v[36:39], a[72:75], v[208:211], v[36:39]\n"
"v_perm_b32 v202, v197, v196, s72\n"
"v_mfma_f32_16x16x32_bf16 v[40:43], a[80:83], v[208:211], v[218:221]\n"
"s_add_u32 m0, s53, 52416\n"
"v_exp_f32 v198, v198\n"
"v_mfma_f32_16x16x32_bf16 v[40:43], a[84:87], v[212:215], v[40:43]\n"
"buffer_load_dwordx4 v247, s[36:39], s52 offen offset:3072 lds\n"
"v_mfma_f32_16x16x32_bf16 v[44:47], a[92:95], v[212:215], v[218:221]\n"
"v_exp_f32 v199, v199\n"
"v_mfma_f32_16x16x32_bf16 v[44:47], a[88:91], v[208:211], v[44:47]\n"
"v_perm_b32 v203, v199, v198, s72\n"
"v_mfma_f32_16x16x32_bf16 v[48:51], a[96:99], v[208:211], v[218:221]\n"
"v_exp_f32 v224, v224\n"
"v_mfma_f32_16x16x32_bf16 v[48:51], a[100:103], v[212:215], v[48:51]\n"
"v_mfma_f32_16x16x32_bf16 v[52:55], a[108:111], v[212:215], v[218:221]\n"
"v_exp_f32 v225, v225\n"
"v_mfma_f32_16x16x32_bf16 v[52:55], a[104:107], v[208:211], v[52:55]\n"
"v_perm_b32 v204, v225, v224, s72\n"
"v_mfma_f32_16x16x32_bf16 v[56:59], a[112:115], v[208:211], v[218:221]\n"
"s_add_u32 m0, s54, 49536\n"
"v_exp_f32 v226, v226\n"
"v_mfma_f32_16x16x32_bf16 v[56:59], a[116:119], v[212:215], v[56:59]\n"
"buffer_load_dwordx4 v248, s[40:43], s52 offen offset:0 lds\n"
"v_mfma_f32_16x16x32_bf16 v[60:63], a[124:127], v[212:215], v[218:221]\n"
"v_exp_f32 v227, v227\n"
"v_mfma_f32_16x16x32_bf16 v[60:63], a[120:123], v[208:211], v[60:63]\n"
"v_perm_b32 v205, v227, v226, s72\n"
"s_waitcnt lgkmcnt(0)\n"
"s_branch LAN%=\n"
"LTS%=:\n"
"v_mfma_f32_16x16x32_bf16 v[192:195], a[0:3], v[144:147], -4.0\n"
"Lhop2_4_%=:\n s_cbranch_execz Lback2_%=\n"
"v_exp_f32 v228, v228\n"
"v_mfma_f32_16x16x32_bf16 v[192:195], a[4:7], v[148:151], v[192:195]\n"
"ds_read_b64_tr_b16 a[92:93], v255 offset:49800\n"
"ds_read_b64_tr_b16 a[94:95], v255 offset:53928\n"
"v_mfma_f32_16x16x32_bf16 v[192:195], a[8:11], v[152:155], v[192:195]\n"
"v_exp_f32 v229, v229\n"
"v_mfma_f32_16x16x32_bf16 v[192:195], a[12:15], v[156:159], v[192:195]\n"
"s_waitcnt vmcnt(3)\n"
"ds_read_b64_tr_b16 a[100:101], v255 offset:51600\n"
"v_perm_b32 v206, v229, v228, s72\n"
"v_mfma_f32_16x16x32_bf16 v[196:199], a[28:31], v[156:159], -4.0\n"
"v_exp_f32 v230, v230\n"
"v_mfma_f32_16x16x32_bf16 v[196:199], a[24:27], v[152:155], v[196:199]\n"
"ds_read_b64_tr_b16 a[102:103], v255 offset:55728\n"
"ds_read_b64_tr_b16 a[108:109], v255 offset:51608\n"
"v_mfma_f32_16x16x32_bf16 v[196:199], a[20:23], v[148:151], v[196:199]\n"
"v_exp_f32 v231, v231\n"
"v_mfma_f32_16x16x32_bf16 v[196:199], a[16:19], v[144:147], v[196:199]\n"
"ds_read_b64_tr_b16 a[110:111], v255 offset:55736\n"
"v_perm_b32 v207, v231, v230, s72\n"
"v_mfma_f32_16x16x32_bf16 v[224:227], a[32:35], v[144:147], -4.0\n"
"v_exp_f32 v192, v192\n"
"v_mfma_f32_16x16x32_bf16 v[224:227], a[36:39], v[148:151], v[224:227]\n"
"ds_read_b64_tr_b16 a[116:117], v255 offset:51856\n"
"ds_read_b64_tr_b16 a[118:119], v255 offset:55984\n"
"v_mfma_f32_16x16x32_bf16 v[224:227], a[40:43], v[152:155], v[224:227]\n"
"v_exp_f32 v193, v193\n"
"v_mfma_f32_16x16x32_bf16 v[224:227], a[44:47], v[156:159], v[224:227]\n"
"ds_read_b64_tr_b16 a[124:125], v255 offset:51864\n"
"v_perm_b32 v208, v193, v192, s72\n"
"v_mfma_f32_16x16x32_bf16 v[228:231], a[60:63], v[156:159], -4.0\n"
"v_exp_f32 v194, v194\n"
"v_mfma_f32_16x16x32_bf16 v[228:231], a[56:59], v[152:155], v[228:231]\n"
"ds_read_b64_tr_b16 a[126:127], v255 offset:55992\n"
"ds_read_b64_tr_b16 a[122:123], v245 offset:55992\n"
"v_mfma_f32_16x16x32_bf16 v[228:231], a[52:55], v[148:151], v[228:231]\n"
"v_exp_f32 v195, v195\n"
"v_mfma_f32_16x16x32_bf16 v[228:231], a[48:51], v[144:147], v[228:231]\n"
"v_perm_b32 v209, v195, v194, s72\n"
"v_smfmac_f32_16x16x64_bf16 a[128:131], a[144:147], v[200:207], v251\n"
"v_mfma_f32_16x16x32_bf16 v[0:3], a[64:67], v[200:203], v[0:3]\n"
"s_waitcnt lgkmcnt(6)\n"
"v_exp_f32 v196, v196\n"
"v_mfma_f32_16x16x32_bf16 v[0:3], a[68:71], v[204:207], v[0:3]\n"
"v_mfma_f32_16x16x32_bf16 v[4:7], a[76:79], v[204:207], v[4:7]\n"
"v_exp_f32 v197, v197\n"
"v_mfma_f32_16x16x32_bf16 v[4:7], a[72:75], v[200:203], v[4:7]\n"
"v_perm_b32 v210, v197, v196, s72\n"
"v_mfma_f32_16x16x32_bf16 v[8:11], a[80:83], v[200:203], v[8:11]\n"
"v_exp_f32 v198, v198\n"
"v_mfma_f32_16x16x32_bf16 v[8:11], a[84:87], v[204:207], v[8:11]\n"
"s_nop 0\n"
"v_accvgpr_read_b32 v254, a128\n"
"v_accvgpr_read_b32 v252, a129\n"
"v_mfma_f32_16x16x32_bf16 v[12:15], a[92:95], v[204:207], v[12:15]\n"
"v_exp_f32 v199, v199\n"
"v_mfma_f32_16x16x32_bf16 v[12:15], a[88:91], v[200:203], v[12:15]\n"
"v_perm_b32 v211, v199, v198, s72\n"
"v_add_f32 v254, v254, v252\n"
"v_mfma_f32_16x16x32_bf16 v[16:19], a[96:99], v[200:203], v[16:19]\n"
"v_exp_f32 v224, v224\n"
"v_mfma_f32_16x16x32_bf16 v[16:19], a[100:103], v[204:207], v[16:19]\n"
"s_waitcnt lgkmcnt(0)\n"
"v_rcp_f32 v252, v254\n"
"s_nop 0\n"
"v_max_f32 v254, v254, v252\n"
"v_cmp_nlt_f32 s[62:63], v254, s58\n"
"v_mul_f32 v223, v218, v252\n"
"v_mul_f32 v254, 0x3b000000, v223\n"
"v_fma_f32 v0, v0, v252, -v223\n"
"v_fma_f32 v1, v1, v252, -v223\n"
"v_fma_f32 v2, v2, v252, -v223\n"
"v_fma_f32 v3, v3, v252, -v223\n"
"v_fma_f32 v4, v4, v252, -v223\n"
"v_fma_f32 v5, v5, v252, -v223\n"
"v_fma_f32 v6, v6, v252, -v223\n"
"v_fma_f32 v7, v7, v252, -v223\n"
"v_max3_f32 v253, abs(v0), abs(v1), abs(v2)\n"
"v_cmp_lt_f32 vcc, v253, v254\n"
"v_cvt_pk_bf16_f32 v0, v0, v1\n"
"v_cvt_pk_bf16_f32 v1, v2, v3\n"
"v_cvt_pk_bf16_f32 v2, v4, v5\n"
"v_cvt_pk_bf16_f32 v3, v6, v7\n"
"v_mfma_f32_16x16x32_bf16 v[20:23], a[108:111], v[204:207], v[20:23]\n"
"s_and_b32 s73, vcc_lo, vcc_hi\n"
"v_exp_f32 v225, v225\n"
"v_mfma_f32_16x16x32_bf16 v[20:23], a[104:107], v[200:203], v[20:23]\n"
"s_lshr_b32 s74, s73, 16\n"
"ds_write_b128 v216, v[0:3] offset:0\n"
"v_perm_b32 v212, v225, v224, s72\n"
"v_fma_f32 v8, v8, v252, -v223\n"
"v_mfma_f32_16x16x32_bf16 v[24:27], a[112:115], v[200:203], v[24:27]\n"
"s_and_b32 s73, s73, s74\n"
"v_exp_f32 v226, v226\n"
"v_mfma_f32_16x16x32_bf16 v[24:27], a[116:119], v[204:207], v[24:27]\n"
"s_or_b32 s73, s73, s62\n"
"v_fma_f32 v9, v9, v252, -v223\n"
"v_fma_f32 v10, v10, v252, -v223\n"
"v_mfma_f32_16x16x32_bf16 v[28:31], a[124:127], v[204:207], v[28:31]\n"
"s_or_b32 s73, s73, s63\n"
"v_exp_f32 v227, v227\n"
"v_mfma_f32_16x16x32_bf16 v[28:31], a[120:123], v[200:203], v[28:31]\n"
"s_addc_u32 s57, s57, s57\n"
"v_perm_b32 v213, v227, v226, s72\n"
"v_fma_f32 v11, v11, v252, -v223\n"
"v_mfma_f32_16x16x32_bf16 v[192:195], a[0:3], v[160:163], -4.0\n"
"v_exp_f32 v228, v228\n"
"v_mfma_f32_16x16x32_bf16 v[192:195], a[4:7], v[164:167], v[192:195]\n"
"v_fma_f32 v12, v12, v252, -v223\n"
"v_fma_f32 v13, v13, v252, -v223\n"
"v_mfma_f32_16x16x32_bf16 v[192:195], a[8:11], v[168:171], v[192:195]\n"
"v_exp_f32 v229, v229\n"
"v_mfma_f32_16x16x32_bf16 v[192:195], a[12:15], v[172:175], v[192:195]\n"
"v_perm_b32 v214, v229, v228, s72\n"
"v_fma_f32 v14, v14, v252, -v223\n"
"v_fma_f32 v15, v15, v252, -v223\n"
"v_cvt_pk_bf16_f32 v8, v8, v9\n"
"v_cvt_pk_bf16_f32 v9, v10, v11\n"
"v_cvt_pk_bf16_f32 v10, v12, v13\n"
"v_cvt_pk_bf16_f32 v11, v14, v15\n"
"v_mfma_f32_16x16x32_bf16 v[196:199], a[28:31], v[172:175], -4.0\n"
"v_exp_f32 v230, v230\n"
"v_mfma_f32_16x16x32_bf16 v[196:199], a[24:27], v[168:171], v[196:199]\n"
"v_fma_f32 v16, v16, v252, -v223\n"
"v_fma_f32 v17, v17, v252, -v223\n"
"v_mfma_f32_16x16x32_bf16 v[196:199], a[20:23], v[164:167], v[196:199]\n"
"v_exp_f32 v231, v231\n"
"v_mfma_f32_16x16x32_bf16 v[196:199], a[16:19], v[160:163], v[196:199]\n"
"ds_write_b128 v216, v[8:11] offset:64\n"
"v_perm_b32 v215, v231, v230, s72\n"
"v_fma_f32 v18, v18, v252, -v223\n"
"v_mfma_f32_16x16x32_bf16 v[224:227], a[32:35], v[160:163], -4.0\n"
"v_exp_f32 v192, v192\n"
"v_mfma_f32_16x16x32_bf16 v[224:227], a[36:39], v[164:167], v[224:227]\n"
"ds_read_b128 v[4:7], v217 offset:0\n"
"ds_read_b128 v[12:15], v217 offset:576\n"
"v_mfma_f32_16x16x32_bf16 v[224:227], a[40:43], v[168:171], v[224:227]\n"
"v_exp_f32 v193, v193\n"
"v_mfma_f32_16x16x32_bf16 v[224:227], a[44:47], v[172:175], v[224:227]\n"
"v_perm_b32 v200, v193, v192, s72\n"
"v_fma_f32 v19, v19, v252, -v223\n"
"v_fma_f32 v20, v20, v252, -v223\n"
"v_fma_f32 v21, v21, v252, -v223\n"
"v_fma_f32 v22, v22, v252, -v223\n"
"v_fma_f32 v23, v23, v252, -v223\n"
"v_cvt_pk_bf16_f32 v16, v16, v17\n"
"v_cvt_pk_bf16_f32 v17, v18, v19\n"
"v_cvt_pk_bf16_f32 v18, v20, v21\n"
"v_cvt_pk_bf16_f32 v19, v22, v23\n"
"v_mfma_f32_16x16x32_bf16 v[228:231], a[60:63], v[172:175], -4.0\n"
"v_exp_f32 v194, v194\n"
"v_mfma_f32_16x16x32_bf16 v[228:231], a[56:59], v[168:171], v[228:231]\n"
"v_fma_f32 v24, v24, v252, -v223\n"
"v_fma_f32 v25, v25, v252, -v223\n"
"v_mfma_f32_16x16x32_bf16 v[228:231], a[52:55], v[164:167], v[228:231]\n"
"v_exp_f32 v195, v195\n"
"v_mfma_f32_16x16x32_bf16 v[228:231], a[48:51], v[160:163], v[228:231]\n"
"s_waitcnt lgkmcnt(1)\n"
"buffer_store_dwordx4 v[4:7], v250, s[48:51], s84 offen offset:0 sc1\n"
"v_perm_b32 v201, v195, v194, s72\n"
"v_fma_f32 v26, v26, v252, -v223\n"
"v_fma_f32 v27, v27, v252, -v223\n"
"v_smfmac_f32_16x16x64_bf16 a[132:135], a[144:147], v[208:215], v251\n"
"s_waitcnt lgkmcnt(0)\n"
"buffer_store_dwordx4 v[12:15], v250, s[48:51], s84 offen offset:1024 sc1\n"
"v_fma_f32 v28, v28, v252, -v223\n"
"v_fma_f32 v29, v29, v252, -v223\n"
"v_mfma_f32_16x16x32_bf16 v[32:35], a[64:67], v[208:211], v[32:35]\n"
"v_exp_f32 v196, v196\n"
"v_mfma_f32_16x16x32_bf16 v[32:35], a[68:71], v[212:215], v[32:35]\n"
"ds_write_b128 v216, v[16:19] offset:0\n"
"v_fma_f32 v30, v30, v252, -v223\n"
"v_fma_f32 v31, v31, v252, -v223\n"
"v_mfma_f32_16x16x32_bf16 v[36:39], a[76:79], v[212:215], v[36:39]\n"
"v_exp_f32 v197, v197\n"
"v_mfma_f32_16x16x32_bf16 v[36:39], a[72:75], v[208:211], v[36:39]\n"
"v_perm_b32 v202, v197, v196, s72\n"
"v_cvt_pk_bf16_f32 v24, v24, v25\n"
"v_mfma_f32_16x16x32_bf16 v[40:43], a[80:83], v[208:211], v[40:43]\n"
"v_exp_f32 v198, v198\n"
"v_mfma_f32_16x16x32_bf16 v[40:43], a[84:87], v[212:215], v[40:43]\n"
"v_cvt_pk_bf16_f32 v25, v26, v27\n"
"v_cvt_pk_bf16_f32 v26, v28, v29\n"
"v_cvt_pk_bf16_f32 v27, v30, v31\n"
"v_mfma_f32_16x16x32_bf16 v[44:47], a[92:95], v[212:215], v[44:47]\n"
"v_exp_f32 v199, v199\n"
"v_mfma_f32_16x16x32_bf16 v[44:47], a[88:91], v[208:211], v[44:47]\n"
"ds_write_b128 v216, v[24:27] offset:64\n"
"v_perm_b32 v203, v199, v198, s72\n"
"v_accvgpr_read_b32 v254, a132\n"
"v_mfma_f32_16x16x32_bf16 v[48:51], a[96:99], v[208:211], v[48:51]\n"
"v_exp_f32 v224, v224\n"
"v_mfma_f32_16x16x32_bf16 v[48:51], a[100:103], v[212:215], v[48:51]\n"
"ds_read_b128 v[20:23], v217 offset:0\n"
"ds_read_b128 v[28:31], v217 offset:576\n"
"v_mfma_f32_16x16x32_bf16 v[52:55], a[108:111], v[212:215], v[52:55]\n"
"v_exp_f32 v225, v225\n"
"v_mfma_f32_16x16x32_bf16 v[52:55], a[104:107], v[208:211], v[52:55]\n"
"v_perm_b32 v204, v225, v224, s72\n"
"v_accvgpr_read_b32 v252, a133\n"
"v_mfma_f32_16x16x32_bf16 v[56:59], a[112:115], v[208:211], v[56:59]\n"
"v_exp_f32 v226, v226\n"
"v_mfma_f32_16x16x32_bf16 v[56:59], a[116:119], v[212:215], v[56:59]\n"
"s_waitcnt lgkmcnt(1)\n"
"v_add_f32 v254, v254, v252\n"
"v_rcp_f32 v252, v254\n"
"s_nop 0\n"
"v_max_f32 v254, v254, v252\n"
"v_cmp_nlt_f32 s[62:63], v254, s58\n"
"v_mul_f32 v223, v218, v252\n"
"v_mul_f32 v254, 0x3b000000, v223\n"
"v_fma_f32 v32, v32, v252, -v223\n"
"v_fma_f32 v33, v33, v252, -v223\n"
"v_fma_f32 v34, v34, v252, -v223\n"
"v_fma_f32 v35, v35, v252, -v223\n"
"v_fma_f32 v36, v36, v252, -v223\n"
"v_fma_f32 v37, v37, v252, -v223\n"
"v_fma_f32 v38, v38, v252, -v223\n"
"v_fma_f32 v39, v39, v252, -v223\n"
"v_max3_f32 v253, abs(v32), abs(v33), abs(v34)\n"
"v_cmp_lt_f32 vcc, v253, v254\n"
"v_cvt_pk_bf16_f32 v32, v32, v33\n"
"v_cvt_pk_bf16_f32 v33, v34, v35\n"
"v_cvt_pk_bf16_f32 v34, v36, v37\n"
"v_cvt_pk_bf16_f32 v35, v38, v39\n"
"v_mfma_f32_16x16x32_bf16 v[60:63], a[124:127], v[212:215], v[60:63]\n"
"s_and_b32 s73, vcc_lo, vcc_hi\n"
"v_exp_f32 v227, v227\n"
"v_mfma_f32_16x16x32_bf16 v[60:63], a[120:123], v[208:211], v[60:63]\n"
"s_lshr_b32 s74, s73, 16\n"
"buffer_store_dwordx4 v[20:23], v250, s[48:51], s84 offen offset:128 sc1\n"
"v_perm_b32 v205, v227, v226, s72\n"
"v_fma_f32 v40, v40, v252, -v223\n"
"v_fma_f32 v41, v41, v252, -v223\n"
"v_mfma_f32_16x16x32_bf16 v[192:195], a[32:35], v[176:179], -4.0\n"
"s_and_b32 s73, s73, s74\n"
"v_exp_f32 v228, v228\n"
"v_mfma_f32_16x16x32_bf16 v[192:195], a[36:39], v[180:183], v[192:195]\n"
"s_or_b32 s73, s73, s62\n"
"v_fma_f32 v42, v42, v252, -v223\n"
"v_fma_f32 v43, v43, v252, -v223\n"
"v_mfma_f32_16x16x32_bf16 v[192:195], a[40:43], v[184:187], v[192:195]\n"
"s_or_b32 s73, s73, s63\n"
"v_exp_f32 v229, v229\n"
"v_mfma_f32_16x16x32_bf16 v[192:195], a[44:47], v[188:191], v[192:195]\n"
"s_addc_u32 s57, s57, s57\n"
"s_waitcnt lgkmcnt(0)\n"
"buffer_store_dwordx4 v[28:31], v250, s[48:51], s84 offen offset:1152 sc1\n"
"v_perm_b32 v206, v229, v228, s72\n"
"v_fma_f32 v44, v44, v252, -v223\n"
"v_fma_f32 v45, v45, v252, -v223\n"
"v_mfma_f32_16x16x32_bf16 v[224:227], a[60:63], v[188:191], -4.0\n"
"s_add_u32 s84, s84, 4096\n"
"v_exp_f32 v230, v230\n"
"v_mfma_f32_16x16x32_bf16 v[224:227], a[56:59], v[184:187], v[224:227]\n"
"v_fma_f32 v46, v46, v252, -v223\n"
"v_fma_f32 v47, v47, v252, -v223\n"
"v_mfma_f32_16x16x32_bf16 v[224:227], a[52:55], v[180:183], v[224:227]\n"
"v_exp_f32 v231, v231\n"
"v_mfma_f32_16x16x32_bf16 v[224:227], a[48:51], v[176:179], v[224:227]\n"
"ds_write_b128 v216, v[32:35] offset:0\n"
"v_perm_b32 v207, v231, v230, s72\n"
"v_cvt_pk_bf16_f32 v40, v40, v41\n"
"v_cvt_pk_bf16_f32 v41, v42, v43\n"
"v_cvt_pk_bf16_f32 v42, v44, v45\n"
"v_cvt_pk_bf16_f32 v43, v46, v47\n"
"v_mfma_f32_16x16x32_bf16 v[64:67], a[64:67], v[200:203], v[64:67]\n"
"v_exp_f32 v192, v192\n"
"v_mfma_f32_16x16x32_bf16 v[64:67], a[68:71], v[204:207], v[64:67]\n"
"ds_write_b128 v216, v[40:43] offset:64\n"
"v_fma_f32 v48, v48, v252, -v223\n"
"v_fma_f32 v49, v49, v252, -v223\n"
"v_mfma_f32_16x16x32_bf16 v[68:71], a[76:79], v[204:207], v[68:71]\n"
"v_exp_f32 v193, v193\n"
"v_mfma_f32_16x16x32_bf16 v[68:71], a[72:75], v[200:203], v[68:71]\n"
"ds_read_b128 v[36:39], v217 offset:0\n"
"ds_read_b128 v[44:47], v217 offset:576\n"
"v_perm_b32 v212, v193, v192, s72\n"
"v_mfma_f32_16x16x32_bf16 v[72:75], a[80:83], v[200:203], v[72:75]\n"
"v_exp_f32 v194, v194\n"
"v_mfma_f32_16x16x32_bf16 v[72:75], a[84:87], v[204:207], v[72:75]\n"
"v_fma_f32 v50, v50, v252, -v223\n"
"v_fma_f32 v51, v51, v252, -v223\n"
"v_mfma_f32_16x16x32_bf16 v[76:79], a[92:95], v[204:207], v[76:79]\n"
"v_exp_f32 v195, v195\n"
"v_mfma_f32_16x16x32_bf16 v[76:79], a[88:91], v[200:203], v[76:79]\n"
"v_perm_b32 v213, v195, v194, s72\n"
"v_fma_f32 v52, v52, v252, -v223\n"
"v_fma_f32 v53, v53, v252, -v223\n"
"v_fma_f32 v54, v54, v252, -v223\n"
"v_fma_f32 v55, v55, v252, -v223\n"
"v_cvt_pk_bf16_f32 v48, v48, v49\n"
"v_cvt_pk_bf16_f32 v49, v50, v51\n"
"v_cvt_pk_bf16_f32 v50, v52, v53\n"
"v_cvt_pk_bf16_f32 v51, v54, v55\n"
"v_smfmac_f32_16x16x64_bf16 a[136:139], a[144:147], v[200:207], v251\n"
"ds_write_b128 v216, v[48:51] offset:0\n"
"v_fma_f32 v56, v56, v252, -v223\n"
"v_exp_f32 v224, v224\n"
"v_fma_f32 v57, v57, v252, -v223\n"
"v_fma_f32 v58, v58, v252, -v223\n"
"v_exp_f32 v225, v225\n"
"s_nop 0\n"
"v_perm_b32 v214, v225, v224, s72\n"
"v_fma_f32 v59, v59, v252, -v223\n"
"v_exp_f32 v226, v226\n"
"v_fma_f32 v60, v60, v252, -v223\n"
"v_fma_f32 v61, v61, v252, -v223\n"
"v_exp_f32 v227, v227\n"
"s_nop 0\n"
"v_perm_b32 v215, v227, v226, s72\n"
"v_fma_f32 v62, v62, v252, -v223\n"
"v_mfma_f32_16x16x32_bf16 v[80:83], a[96:99], v[200:203], v[80:83]\n"
"v_fma_f32 v63, v63, v252, -v223\n"
"v_cvt_pk_bf16_f32 v56, v56, v57\n"
"v_mfma_f32_16x16x32_bf16 v[80:83], a[100:103], v[204:207], v[80:83]\n"
"s_waitcnt lgkmcnt(1)\n"
"buffer_store_dwordx4 v[36:39], v250, s[48:51], s84 offen offset:0 sc1\n"
"v_cvt_pk_bf16_f32 v57, v58, v59\n"
"v_cvt_pk_bf16_f32 v58, v60, v61\n"
"v_cvt_pk_bf16_f32 v59, v62, v63\n"
"v_mfma_f32_16x16x32_bf16 v[84:87], a[108:111], v[204:207], v[84:87]\n"
"v_accvgpr_read_b32 v254, a136\n"
"v_accvgpr_read_b32 v252, a137\n"
"v_mfma_f32_16x16x32_bf16 v[84:87], a[104:107], v[200:203], v[84:87]\n"
"buffer_store_dwordx4 v[44:47], v250, s[48:51], s84 offen offset:1024 sc1\n"
"v_add_f32 v254, v254, v252\n"
"v_rcp_f32 v252, v254\n"
"v_mfma_f32_16x16x32_bf16 v[88:91], a[112:115], v[200:203], v[88:91]\n"
"v_max_f32 v254, v254, v252\n"
"v_cmp_nlt_f32 s[62:63], v254, s58\n"
"v_mfma_f32_16x16x32_bf16 v[88:91], a[116:119], v[204:207], v[88:91]\n"
"ds_write_b128 v216, v[56:59] offset:64\n"
"v_mul_f32 v223, v218, v252\n"
"v_mul_f32 v254, 0x3b000000, v223\n"
"v_mfma_f32_16x16x32_bf16 v[92:95], a[124:127], v[204:207], v[92:95]\n"
"ds_read_b128 v[52:55], v217 offset:0\n"
"ds_read_b128 v[60:63], v217 offset:576\n"
"v_mfma_f32_16x16x32_bf16 v[92:95], a[120:123], v[200:203], v[92:95]\n"
"v_fma_f32 v64, v64, v252, -v223\n"
"v_fma_f32 v65, v65, v252, -v223\n"
"v_fma_f32 v66, v66, v252, -v223\n"
"v_fma_f32 v67, v67, v252, -v223\n"
"v_fma_f32 v68, v68, v252, -v223\n"
"v_fma_f32 v69, v69, v252, -v223\n"
"v_fma_f32 v70, v70, v252, -v223\n"
"v_fma_f32 v71, v71, v252, -v223\n"
"v_max3_f32 v253, abs(v64), abs(v65), abs(v66)\n"
"v_cmp_lt_f32 vcc, v253, v254\n"
"v_cvt_pk_bf16_f32 v64, v64, v65\n"
"v_cvt_pk_bf16_f32 v65, v66, v67\n"
"v_cvt_pk_bf16_f32 v66, v68, v69\n"
"v_cvt_pk_bf16_f32 v67, v70, v71\n"
"s_and_b32 s73, vcc_lo, vcc_hi\n"
"ds_write_b128 v216, v[64:67] offset:0\n"
"v_fma_f32 v72, v72, v252, -v223\n"
"v_fma_f32 v73, v73, v252, -v223\n"
"s_lshr_b32 s74, s73, 16\n"
"v_fma_f32 v74, v74, v252, -v223\n"
"v_fma_f32 v75, v75, v252, -v223\n"
"s_and_b32 s73, s73, s74\n"
"s_waitcnt lgkmcnt(2)\n"
"buffer_store_dwordx4 v[52:55], v250, s[48:51], s84 offen offset:128 sc1\n"
"v_fma_f32 v76, v76, v252, -v223\n"
"v_fma_f32 v77, v77, v252, -v223\n"
"v_fma_f32 v78, v78, v252, -v223\n"
"s_or_b32 s73, s73, s62\n"
"s_waitcnt lgkmcnt(1)\n"
"buffer_store_dwordx4 v[60:63], v250, s[48:51], s84 offen offset:1152 sc1\n"
"v_fma_f32 v79, v79, v252, -v223\n"
"v_cvt_pk_bf16_f32 v72, v72, v73\n"
"v_cvt_pk_bf16_f32 v73, v74, v75\n"
"s_or_b32 s73, s73, s63\n"
"v_cvt_pk_bf16_f32 v74, v76, v77\n"
"v_cvt_pk_bf16_f32 v75, v78, v79\n"
"v_mfma_f32_16x16x32_bf16 a[140:143], a[144:147], v[212:215], a[140:143]\n"
"s_addc_u32 s57, s57, s57\n"
"ds_write_b128 v216, v[72:75] offset:64\n"
"v_fma_f32 v80, v80, v252, -v223\n"
"v_fma_f32 v81, v81, v252, -v223\n"
"v_mfma_f32_16x16x32_bf16 v[96:99], a[68:71], v[212:215], v[96:99]\n"
"s_add_u32 s84, s84, 4096\n"
"ds_read_b128 v[68:71], v217 offset:0\n"
"ds_read_b128 v[76:79], v217 offset:576\n"
"v_mfma_f32_16x16x32_bf16 v[100:103], a[76:79], v[212:215], v[100:103]\n"
"v_fma_f32 v82, v82, v252, -v223\n"
"v_fma_f32 v83, v83, v252, -v223\n"
"v_mfma_f32_16x16x32_bf16 v[104:107], a[84:87], v[212:215], v[104:107]\n"
"v_fma_f32 v84, v84, v252, -v223\n"
"v_fma_f32 v85, v85, v252, -v223\n"
"v_mfma_f32_16x16x32_bf16 v[108:111], a[92:95], v[212:215], v[108:111]\n"
"v_fma_f32 v86, v86, v252, -v223\n"
"v_fma_f32 v87, v87, v252, -v223\n"
"v_mfma_f32_16x16x32_bf16 v[112:115], a[100:103], v[212:215], v[112:115]\n"
"v_cvt_pk_bf16_f32 v80, v80, v81\n"
"v_cvt_pk_bf16_f32 v81, v82, v83\n"
"v_mfma_f32_16x16x32_bf16 v[116:119], a[108:111], v[212:215], v[116:119]\n"
"v_cvt_pk_bf16_f32 v82, v84, v85\n"
"v_cvt_pk_bf16_f32 v83, v86, v87\n"
"v_mfma_f32_16x16x32_bf16 v[120:123], a[116:119], v[212:215], v[120:123]\n"
"s_waitcnt lgkmcnt(1)\n"
"buffer_store_dwordx4 v[68:71], v250, s[48:51], s84 offen offset:0 sc1\n"
"v_fma_f32 v88, v88, v252, -v223\n"
"v_fma_f32 v89, v89, v252, -v223\n"
"v_fma_f32 v90, v90, v252, -v223\n"
"v_mfma_f32_16x16x32_bf16 v[124:127], a[124:127], v[212:215], v[124:127]\n"
"s_waitcnt lgkmcnt(0)\n"
"buffer_store_dwordx4 v[76:79], v250, s[48:51], s84 offen offset:1024 sc1\n"
"v_fma_f32 v91, v91, v252, -v223\n"
"v_fma_f32 v92, v92, v252, -v223\n"
"v_fma_f32 v93, v93, v252, -v223\n"
"ds_write_b128 v216, v[80:83] offset:0\n"
"v_fma_f32 v94, v94, v252, -v223\n"
"v_fma_f32 v95, v95, v252, -v223\n"
"v_cvt_pk_bf16_f32 v88, v88, v89\n"
"v_cvt_pk_bf16_f32 v89, v90, v91\n"
"v_cvt_pk_bf16_f32 v90, v92, v93\n"
"v_cvt_pk_bf16_f32 v91, v94, v95\n"
"ds_write_b128 v216, v[88:91] offset:64\n"
"ds_read_b128 v[84:87], v217 offset:0\n"
"ds_read_b128 v[92:95], v217 offset:576\n"
"v_mbcnt_lo_u32_b32 v254, -1, 0\n"
"v_mbcnt_hi_u32_b32 v254, -1, v254\n"
"v_lshl_add_u32 v253, v254, 4, s55\n"
"v_lshl_add_u32 v254, v254, 2, s55\n"
"s_cmp_eq_u32 s71, 0\n s_cbranch_scc1 Lswt%=\n v_swap_b32 v96, v112\n v_swap_b32 v97, v113\n v_swap_b32 v98, v114\n v_swap_b32 v99, v115\n v_swap_b32 v100, v116\n v_swap_b32 v101, v117\n v_swap_b32 v102, v118\n v_swap_b32 v103, v119\n v_swap_b32 v104, v120\n v_swap_b32 v105, v121\n v_swap_b32 v106, v122\n v_swap_b32 v107, v123\n v_swap_b32 v108, v124\n v_swap_b32 v109, v125\n v_swap_b32 v110, v126\n v_swap_b32 v111, v127\n Lswt%=:\n"
"ds_write_b128 v253, v[112:115] offset:0\n"
"ds_write_b128 v253, v[116:119] offset:1024\n"
"ds_write_b128 v253, v[120:123] offset:2048\n"
"ds_write_b128 v253, v[124:127] offset:3072\n"
"ds_write_b32 v254, a140 offset:4128\n"
"s_nop 0\n"
"v_add_u32 v126, s75, v254\n"
"v_add_u32 v127, s75, v253\n"
"s_waitcnt lgkmcnt(0)\n"
"s_barrier\n"
"ds_read_b32 a141, v126 offset:4128\n"
"ds_read_b128 v[112:115], v127 offset:0\n"
"ds_read_b128 v[116:119], v127 offset:1024\n"
"ds_read_b128 v[120:123], v127 offset:2048\n"
"ds_read_b128 v[124:127], v127 offset:3072\n"
"v_accvgpr_read_b32 v254, a140\n"
"s_waitcnt lgkmcnt(4)\n"
"v_accvgpr_read_b32 v252, a141\n"
"v_add_f32 v254, v254, v252\n"
"v_rcp_f32 v252, v254\n"
"s_nop 0\n"
"v_max_f32 v254, v254, v252\n"
"v_cmp_nlt_f32 s[62:63], v254, s58\n"
"v_mul_f32 v223, v218, v252\n"
"v_add_f32 v223, v223, v223\n"
"v_mul_f32 v254, 0x3b000000, v223\n"
"s_waitcnt lgkmcnt(3)\n"
"v_add_f32 v96, v96, v112\n"
"v_add_f32 v97, v97, v113\n"
"v_add_f32 v98, v98, v114\n"
"v_add_f32 v99, v99, v115\n"
"s_waitcnt lgkmcnt(2)\n"
"v_add_f32 v100, v100, v116\n"
"v_add_f32 v101, v101, v117\n"
"v_add_f32 v102, v102, v118\n"
"v_add_f32 v103, v103, v119\n"
"v_fma_f32 v96, v96, v252, -v223\n"
"v_fma_f32 v97, v97, v252, -v223\n"
"v_fma_f32 v98, v98, v252, -v223\n"
"v_fma_f32 v99, v99, v252, -v223\n"
"v_fma_f32 v100, v100, v252, -v223\n"
"v_fma_f32 v101, v101, v252, -v223\n"
"v_fma_f32 v102, v102, v252, -v223\n"
"v_fma_f32 v103, v103, v252, -v223\n"
"v_max3_f32 v253, abs(v96), abs(v97), abs(v98)\n"
"v_cmp_lt_f32 vcc, v253, v254\n"
"v_cvt_pk_bf16_f32 v96, v96, v97\n"
"v_cvt_pk_bf16_f32 v97, v98, v99\n"
"v_cvt_pk_bf16_f32 v98, v100, v101\n"
"v_cvt_pk_bf16_f32 v99, v102, v103\n"
"s_and_b32 s73, vcc_lo, vcc_hi\n"
"buffer_store_dwordx4 v[84:87], v250, s[48:51], s84 offen offset:128 sc1\n"
"s_waitcnt lgkmcnt(1)\n"
"v_add_f32 v104, v104, v120\n"
"v_add_f32 v105, v105, v121\n"
"v_add_f32 v106, v106, v122\n"
"v_add_f32 v107, v107, v123\n"
"s_waitcnt lgkmcnt(0)\n"
"v_add_f32 v108, v108, v124\n"
"v_add_f32 v109, v109, v125\n"
"v_add_f32 v110, v110, v126\n"
"v_add_f32 v111, v111, v127\n"
"v_fma_f32 v104, v104, v252, -v223\n"
"v_fma_f32 v105, v105, v252, -v223\n"
"v_fma_f32 v106, v106, v252, -v223\n"
"v_fma_f32 v107, v107, v252, -v223\n"
"v_fma_f32 v108, v108, v252, -v223\n"
"v_fma_f32 v109, v109, v252, -v223\n"
"v_fma_f32 v110, v110, v252, -v223\n"
"v_fma_f32 v111, v111, v252, -v223\n"
"v_cvt_pk_bf16_f32 v104, v104, v105\n"
"v_cvt_pk_bf16_f32 v105, v106, v107\n"
"v_cvt_pk_bf16_f32 v106, v108, v109\n"
"v_cvt_pk_bf16_f32 v107, v110, v111\n"
"s_lshr_b32 s74, s73, 16\n"
"buffer_store_dwordx4 v[92:95], v250, s[48:51], s84 offen offset:1152 sc1\n"
"s_and_b32 s73, s73, s74\n"
"ds_write_b128 v216, v[96:99] offset:0\n"
"s_or_b32 s73, s73, s62\n"
"ds_write_b128 v216, v[104:107] offset:64\n"
"s_or_b32 s73, s73, s63\n"
"ds_read_b128 v[100:103], v217 offset:0\n"
"ds_read_b128 v[108:111], v217 offset:576\n"
"s_addc_u32 s57, s57, s57\n"
"s_mov_b32 s84, s85\n"
"s_waitcnt lgkmcnt(1)\n"
"Lhop2_5_%=:\n s_cbranch_execz Lback2_%=\n"
"buffer_store_dwordx4 v[100:103], v250, s[48:51], s84 offen offset:0 sc1\n"
"s_waitcnt lgkmcnt(0)\n"
"buffer_store_dwordx4 v[108:111], v250, s[48:51], s84 offen offset:1024 sc1\n"
"s_cmp_eq_u32 s57, 0\n"
"s_cbranch_scc0 Lend%=\n"
"s_endpgm\n"
"Lend%=:\n"
"s_mov_b32 m0, s61\n"
"s_mov_b32 %0, s57\n"
      : "=s"(mask) : "s"(__builtin_amdgcn_kernarg_segment_ptr()), "s"(wave), "s"(wg), "s"(ldsb) : "v0","v1","v2","v3","v4","v5","v6","v7","v8","v9","v10","v11","v12","v13","v14","v15","v16","v17","v18","v19","v20","v21","v22","v23","v24","v25","v26","v27","v28","v29","v30","v31","v32","v33","v34","v35","v36","v37","v38","v39","v40","v41","v42","v43","v44","v45","v46","v47","v48","v49","v50","v51","v52","v53","v54","v55","v56","v57","v58","v59","v60","v61","v62","v63","v64","v65","v66","v67","v68","v69","v70","v71","v72","v73","v74","v75","v76","v77","v78","v79","v80","v81","v82","v83","v84","v85","v86","v87","v88","v89","v90","v91","v92","v93","v94","v95","v96","v97","v98","v99","v100","v101","v102","v103","v104","v105","v106","v107","v108","v109","v110","v111","v112","v113","v114","v115","v116","v117","v118","v119","v120","v121","v122","v123","v124","v125","v126","v127","v128","v129","v130","v131","v132","v133","v134","v135","v136","v137","v138","v139","v140","v141","v142","v143","v144","v145","v146","v147","v148","v149","v150","v151","v152","v153","v154","v155","v156","v157","v158","v159","v160","v161","v162","v163","v164","v165","v166","v167","v168","v169","v170","v171","v172","v173","v174","v175","v176","v177","v178","v179","v180","v181","v182","v183","v184","v185","v186","v187","v188","v189","v190","v191","v192","v193","v194","v195","v196","v197","v198","v199","v200","v201","v202","v203","v204","v205","v206","v207","v208","v209","v210","v211","v212","v213","v214","v215","v216","v217","v218","v219","v220","v221","v222","v223","v224","v225","v226","v227","v228","v229","v230","v231","v232","v233","v234","v235","v236","v237","v238","v239","v240","v241","v242","v243","v244","v245","v246","v247","v248","v249","v250","v251","v252","v253","v254","v255","a0","a1","a2","a3","a4","a5","a6","a7","a8","a9","a10","a11","a12","a13","a14","a15","a16","a17","a18","a19","a20","a21","a22","a23","a24","a25","a26","a27","a28","a29","a30","a31","a32","a33","a34","a35","a36","a37","a38","a39","a40","a41","a42","a43","a44","a45","a46","a47","a48","a49","a50","a51","a52","a53","a54","a55","a56","a57","a58","a59","a60","a61","a62","a63","a64","a65","a66","a67","a68","a69","a70","a71","a72","a73","a74","a75","a76","a77","a78","a79","a80","a81","a82","a83","a84","a85","a86","a87","a88","a89","a90","a91","a92","a93","a94","a95","a96","a97","a98","a99","a100","a101","a102","a103","a104","a105","a106","a107","a108","a109","a110","a111","a112","a113","a114","a115","a116","a117","a118","a119","a120","a121","a122","a123","a124","a125","a126","a127","a128","a129","a130","a131","a132","a133","a134","a135","a136","a137","a138","a139","a140","a141","a142","a143","a144","a145","a146","a147","a148","a149","a150","a151","a152","a153","a154","a155","a156","a157","a158","a159","a160","a161","a162","a163","a164","a165","a166","a167","a168","a169","a170","a171","a172","a173","a174","a175","a176","a177","a178","a179","a180","a181","a182","a183","a184","a185","a186","a187","a188","a189","a190","a191","a192","a193","a194","a195","a196","a197","a198","a199","a200","a201","a202","a203","a204","a205","a206","a207","a208","a209","a210","a211","a212","a213","a214","a215","a216","a217","a218","a219","a220","a221","a222","a223","a224","a225","a226","a227","a228","a229","a230","a231","a232","a233","a234","a235","a236","a237","a238","a239","a240","a241","a242","a243","a244","a245","a246","a247","a248","a249","a250","a251","a252","a253","a254","a255","s35","s36","s37","s38","s39","s40","s41","s42","s43","s44","s45","s46","s47","s48","s49","s50","s51","s52","s53","s54","s55","s56","s57","s58","s59","s60","s61","s62","s63","s64","s65","s66","s67","s68","s69","s70","s71","s72","s73","s74","s75","s76","s77","s78","s79","s80","s81","s82","s83","s84","s85","s86","s87","s88","s89","s90","s91","s92","s93","s94","s95","s96","s97","s98","s99","vcc","scc","memory");
  if (mask) {
    asm volatile("s_waitcnt vmcnt(0)" ::: "memory");
    const int lane = __builtin_amdgcn_mbcnt_hi(-1, __builtin_amdgcn_mbcnt_lo(-1, 0));
    for (int i = 0; i < 16; ++i) {
      if (!((mask >> (15 - i)) & 1)) continue;
      const int b = i >> 2, t = i & 3, g = (3 - b) * 64 + (wg >> 5) * 8 + (wg & 7), j = (wg >> 3) & 3;
      const int row = 256 * j - (j > b ? 32 : 0) + (j != b ? 64 * wave + 16 * t : t < 3 ? 48 * wave + 16 * t : 192 + 16 * (wave >> 1));
      const int n = (j == b && t == 3) ? 1 << (wave & 1) : 3;
      const size_t qo = ((size_t)g * 992 + row) * 128, ko = (size_t)g * 65536;
      exact_tile(Q + qo, K + ko, V + ko, O + qo, n, lane);
    }
  }
}

extern "C" void kernel_launch(void* const* d_in, const int* in_sizes, int n_in, void* d_out, int out_size, void* d_ws, size_t ws_size,
                              hipStream_t stream) {
  gqa_attn<<<256, 256, 0, stream>>>((const u16*)d_in[0], (const u16*)d_in[1], (const u16*)d_in[2], (__hip_bfloat16*)d_out);
}
```
